```python
import torch

def _softmax(x, axis=-1):
    return torch.softmax(x, dim=axis)

def _gelu(x):
    return torch.nn.functional.gelu(x, approximate='tanh')

def _silu(x):
    return torch.nn.functional.silu(x)

def _sigmoid(x):
    return torch.sigmoid(x)

def _layernorm(x, axis=-1, eps=1e-05):
    m = x.mean(dim=axis, keepdim=True)
    v = x.var(dim=axis, keepdim=True, unbiased=False)
    return (x - m) / torch.sqrt(v + eps)

def _scatter_add(tgt, idx, src, axis=0):
    return tgt.clone().index_add_(axis, idx, src)

def _segment_sum(src, seg, n):
    out = torch.zeros((n,) + src.shape[1:], dtype=src.dtype)
    return out.index_add_(0, seg, src)

def _pad(x, pw):
    flat = tuple((v for pair in reversed(pw) for v in pair))
    return torch.nn.functional.pad(x, flat)
kernel_name = 'adaLN_cond__ssm_conv_s4__glu_gated_glu__prompt'

def setup_inputs():

    _w = lambda a: (lambda t: t.to(torch.bfloat16) if t.is_floating_point() else t)(torch.as_tensor(a))

    def _A_setup():
        B, L, D, N = (1, 2048, 512, 128)
        A_re = -(lambda _x: torch.exp(torch.as_tensor(_x)))((lambda *s: torch.randn(*s, dtype=torch.bfloat16) * 3.2)(D, N))
        A_im = torch.pi * (lambda *s: torch.randn(*s, dtype=torch.bfloat16) * 0.1)(D, N)
        B_re = (lambda *s: torch.randn(*s, dtype=torch.bfloat16) * 0.2)(D, N)
        B_im = (lambda *s: torch.randn(*s, dtype=torch.bfloat16) * 0.2)(D, N)
        C_re = (lambda *s: torch.randn(*s, dtype=torch.bfloat16) * 0.2)(D, N)
        C_im = (lambda *s: torch.randn(*s, dtype=torch.bfloat16) * 0.2)(D, N)
        log_dt = (lambda *s: torch.randn(*s, dtype=torch.bfloat16) * 0.1)(D) * 0.5 - 4.0
        Dskip = (lambda *s: torch.randn(*s, dtype=torch.bfloat16) * 0.1)(D)
        return {'u': _w((lambda *s: torch.randn(*s, dtype=torch.bfloat16) * 0.05)(B, L, D)), 'A_re': _w(A_re), 'A_im': _w(A_im), 'B_re': _w(B_re), 'B_im': _w(B_im), 'C_re': _w(C_re), 'C_im': _w(C_im), 'log_dt': _w(log_dt), 'Dskip': _w(Dskip)}
    _A_in = _A_setup()

    def _B_setup():
        B, S, D, F = (1, 2048, 512, 1792)
        return {'x': _w((lambda *s: torch.randn(*s, dtype=torch.bfloat16) * 0.05)(B, S, D)), 'w_gate': _w((lambda *s: torch.randn(*s, dtype=torch.bfloat16) * 0.05)(D, F)), 'w_up': _w((lambda *s: torch.randn(*s, dtype=torch.bfloat16) * 0.05)(D, F)), 'w_down': _w((lambda *s: torch.randn(*s, dtype=torch.bfloat16) * 0.05)(F, D))}
    _B_in = _B_setup()
    return {'A_u': _A_in['u'], 'A_A_re': _A_in['A_re'], 'A_A_im': _A_in['A_im'], 'A_B_re': _A_in['B_re'], 'A_B_im': _A_in['B_im'], 'A_C_re': _A_in['C_re'], 'A_C_im': _A_in['C_im'], 'A_log_dt': _A_in['log_dt'], 'A_Dskip': _A_in['Dskip'], 'B_x': _B_in['x'], 'B_w_gate': _B_in['w_gate'], 'B_w_up': _B_in['w_up'], 'B_w_down': _B_in['w_down'], 'c': _w((lambda *s: torch.randn(*s, dtype=torch.bfloat16) * 0.1)(1, 512) * 8.0), 'w_mod': _w((lambda *s: torch.randn(*s, dtype=torch.bfloat16) * 0.1)(512, 6 * 512) * 512 ** (-0.5) * 8.0)}

def reference(A_u, A_A_re, A_A_im, A_B_re, A_B_im, A_C_re, A_C_im, A_log_dt, A_Dskip, B_x, B_w_gate, B_w_up, B_w_down, c, w_mod):

    def _A_ref(u, A_re, A_im, B_re, B_im, C_re, C_im, log_dt, Dskip):
        Bsz, L, D = u.shape
        dt = torch.exp(log_dt)[:, None]
        A = A_re + 1j * A_im
        Bc = B_re + 1j * B_im
        Cc = C_re + 1j * C_im
        logdA = dt * A
        dB = dt * Bc
        l = (lambda x, dt: x.to(dt))(torch.arange(L), A_re.dtype)
        P = torch.exp(logdA[:, :, None] * l[None, None, :])
        K = torch.einsum('dn,dnl,dn->dl', Cc, P, dB).real
        L2 = 2 * L
        Kp = _pad(K, ((0, 0), (0, L)))
        up = (lambda x, *d: x.permute(*d))(u, 0, 2, 1)
        up = _pad(up, ((0, 0), (0, 0), (0, L)))
        Kf = (lambda x, axes=(-2, -1): torch.fft.rfft2(x.float(), dim=axes))(Kp[:, None, :], axes=(-2, -1))
        Uf = (lambda x, axes=(-2, -1): torch.fft.rfft2(x.float(), dim=axes))(up[:, :, None, :], axes=(-2, -1))
        Yf = Uf * Kf[None, :, :, :]
        y = (lambda x, s, axes=(-2, -1): torch.fft.irfft2(x, s=s, dim=axes).to(torch.bfloat16))(Yf, (1, L2), axes=(-2, -1))[:, :, 0, :L]
        y = (lambda x, *d: x.permute(*d))(y, 0, 2, 1) + u * Dskip[None, None, :]
        return y

    def _B_ref(x, w_gate, w_up, w_down):
        g = torch.einsum('bsd,df->bsf', x, w_gate)
        u = torch.einsum('bsd,df->bsf', x, w_up)
        h = _sigmoid(g) * u
        return torch.einsum('bsf,fd->bsd', h, w_down)
    x = A_u
    mod = (_silu(c) @ w_mod).reshape(c.shape[0], 6, 1, -1)
    sa, ca, ga, sb, cb, gb = (mod[:, 0].clone(), mod[:, 1].clone(), mod[:, 2].clone(), mod[:, 3].clone(), mod[:, 4].clone(), mod[:, 5].clone())
    h = _layernorm(x, axis=-1) * (1 + ca) + sa
    x = x + ga * _A_ref(h, A_A_re, A_A_im, A_B_re, A_B_im, A_C_re, A_C_im, A_log_dt, A_Dskip)
    h = _layernorm(x, axis=-1) * (1 + cb) + sb
    x = x + gb * _B_ref(h, B_w_gate, B_w_up, B_w_down)
    return x
```

```cpp
#include <hip/hip_runtime.h>
#include <hip/hip_bf16.h>

typedef unsigned short u16;
typedef unsigned u32;
typedef u32 u32x2 __attribute__((ext_vector_type(2)));
typedef u32 u32x4 __attribute__((ext_vector_type(4)));
typedef float f32x2 __attribute__((ext_vector_type(2)));
typedef float f32x4 __attribute__((ext_vector_type(4)));
typedef float f32x16 __attribute__((ext_vector_type(16)));
typedef __bf16 bf16x2 __attribute__((ext_vector_type(2)));
typedef __bf16 bf16x8 __attribute__((ext_vector_type(8)));

#define DEVI __device__ __forceinline__

constexpr int SEQ = 2048, DM = 512, NS = 128, FF = 1792;
constexpr int KR_COPY = 2080;
constexpr int KR_PAIR_BYTES = 17408;

constexpr size_t WS_ZX  = 0;
constexpr size_t WS_KR  = WS_ZX + (size_t)256 * 16384;
constexpr size_t WS_MOD = WS_KR + (size_t)256 * KR_PAIR_BYTES;
constexpr size_t WS_X2  = WS_MOD + 4 * 3072 * 4;
constexpr size_t WS_WGU = WS_X2 + (size_t)256 * 2048 * 4;
constexpr size_t WS_UV  = WS_WGU + (size_t)16 * 7 * 32 * 1024;
constexpr size_t WS_WD  = WS_UV + 4 * 2 * 3584 * 4;
constexpr size_t WS_HH  = WS_WD + (size_t)16 * 112 * 1024;
constexpr size_t WS_END = WS_HH + (size_t)64 * 112 * 1024;

DEVI float bflo(u32 v) { return __builtin_bit_cast(float, v << 16); }
DEVI float bfhi(u32 v) { return __builtin_bit_cast(float, v & 0xffff0000u); }
DEVI float bf2f(u16 v) { return __builtin_bit_cast(float, (u32)v << 16); }
DEVI u32 pack2(float lo, float hi) {
    f32x2 f = {lo, hi};
    return __builtin_bit_cast(u32, __builtin_convertvector(f, bf16x2));
}
DEVI float rbf(float x) { return bflo(pack2(x, x)); }
DEVI bf16x8 asbf(u32x4 v) { return __builtin_bit_cast(bf16x8, v); }
DEVI float modsum(const float* mp, int col) {
    return rbf((mp[col] + mp[3072 + col]) + (mp[2 * 3072 + col] + mp[3 * 3072 + col]));
}

constexpr int TAB_STRIDE = 544;
constexpr int TAB_CH = 64 * TAB_STRIDE;
constexpr int KL_OFF = 2 * TAB_CH;
constexpr int K1_LDS = KL_OFF + 2 * 2064 * 2;

DEVI f32x2 cmul(f32x2 a, f32x2 b) {
    f32x2 r;
    r.x = a.x * b.x - a.y * b.y;
    r.y = a.x * b.y + a.y * b.x;
    return r;
}
DEVI f32x2 cexp_l(float lr, float li, float l) {
    float mag = __builtin_amdgcn_exp2f(lr * l * 1.44269504088896341f);
    float rev = __builtin_amdgcn_fractf(li * l * 0.15915494309189535f);
    f32x2 r = {mag * __builtin_amdgcn_cosf(rev), mag * __builtin_amdgcn_sinf(rev)};
    return r;
}

DEVI void k1_kgen(int pair, const u16* Are, const u16* Aim, const u16* Bre, const u16* Bim, const u16* Cre,
                  const u16* Cim, const u16* logdt, char* ws, char* lds) {
    const int tid = threadIdx.x, lane = tid & 63, wave = tid >> 6;
    const int c = tid >> 7, n = tid & 127, d = pair * 2 + c;
    const int idx = d * NS + n;
    const float are = bf2f(Are[idx]), aim = bf2f(Aim[idx]), bre = bf2f(Bre[idx]), bim = bf2f(Bim[idx]);
    const float cre = bf2f(Cre[idx]), cim = bf2f(Cim[idx]);
    const float dt = rbf(expf(bf2f(logdt[d])));
    const float lr = dt * are, li = dt * aim;
    const f32x2 W = cmul(f32x2{cre, cim}, f32x2{dt * bre, dt * bim});
    const f32x2 P1 = cexp_l(lr, li, 1.f), P8 = cexp_l(lr, li, 8.f), Q = cexp_l(lr, li, 128.f);
    const f32x2 P2 = cmul(P1, P1), P4 = cmul(P2, P2);
    char* tab = lds + c * TAB_CH + n * 4;
    auto putG = [&](int row, f32x2 v) { *(u32*)(tab + row * TAB_STRIDE) = pack2(v.x, v.y); };
    {
        f32x2 e = {1.f, 0.f};
        const f32x2 P8c = {P8.x, -P8.y};
#pragma unroll
        for (int b = 0; b < 16; ++b) { putG(48 + b, e); e = cmul(e, P8c); }
    }
    f32x2 g = W;
#pragma unroll
    for (int hi = 0; hi <= 16; ++hi) {
        if (hi < 2) {
            f32x2 t = g;
#pragma unroll
            for (int lo = 0; lo < 8; ++lo) { putG(hi * 8 + lo, t); if (lo < 7) t = cmul(t, P1); }
        } else if (hi < 4) {
            f32x2 t = g;
#pragma unroll
            for (int lo = 0; lo < 4; ++lo) { putG(16 + (hi - 2) * 4 + lo, t); if (lo < 3) t = cmul(t, P2); }
        } else if (hi < 8) {
            putG(24 + (hi - 4) * 2, g);
            putG(25 + (hi - 4) * 2, cmul(g, P4));
        } else {
            putG(32 + (hi - 8), g);
        }
        if (hi < 16) g = cmul(g, Q);
    }
    __syncthreads();
    {
        const int ch = wave >> 1, half = wave & 1;
        const char* tb = lds + ch * TAB_CH + (lane & 15) * TAB_STRIDE + (lane >> 4) * 16;
        u16* kl = (u16*)(lds + KL_OFF) + ch * 2064;
        const int b8 = (lane & 15) * 8, rg = lane >> 4;
        f32x4 acc0 = {0.f, 0.f, 0.f, 0.f}, acc1 = {0.f, 0.f, 0.f, 0.f};
        const int t0 = half, t1 = 2;
#pragma unroll
        for (int ks = 0; ks < 8; ++ks) {
            const u32x4 e = *(const u32x4*)(tb + 48 * TAB_STRIDE + ks * 64);
            const u32x4 a0 = *(const u32x4*)(tb + t0 * 16 * TAB_STRIDE + ks * 64);
            acc0 = __builtin_amdgcn_mfma_f32_16x16x32_bf16(asbf(a0), asbf(e), acc0, 0, 0, 0);
            if (half == 0) {
                const u32x4 a1 = *(const u32x4*)(tb + t1 * 16 * TAB_STRIDE + ks * 64);
                acc1 = __builtin_amdgcn_mfma_f32_16x16x32_bf16(asbf(a1), asbf(e), acc1, 0, 0, 0);
            }
        }
#pragma unroll
        for (int r = 0; r < 4; ++r) {
            const int row = rg * 4 + r;
            int base;
            if (half == 0) base = (row >> 3) * 128 + (row & 7);
            else base = row < 8 ? 256 + (row >> 2) * 128 + (row & 3) * 2 : 512 + ((row - 8) >> 1) * 128 + (row & 1) * 4;
            kl[base + b8] = (u16)pack2(acc0[r], acc0[r]);
            if (half == 0 && (row < 8 || (row == 8 && b8 == 0))) kl[1024 + row * 128 + b8] = (u16)pack2(acc1[r], acc1[r]);
        }
    }
    __syncthreads();
    char* kr = ws + WS_KR + (size_t)pair * KR_PAIR_BYTES;
    for (int task = tid; task < 520; task += 256) {
        const int ch = task >= 260, q = task - ch * 260;
        const u16* kl = (const u16*)(lds + KL_OFF) + ch * 2064;
        u32 v[9];
#pragma unroll
        for (int j = 0; j < 9; ++j) {
            const int l = 2047 - 8 * q - j;
            const int lr_ = (int)rbf((float)(l < 0 ? 0 : l));
            const u32 x = kl[lr_];
            v[j] = l < 0 ? 0u : x;
        }
        u32x4 c0, c1;
#pragma unroll
        for (int j = 0; j < 4; ++j) {
            c0[j] = v[2 * j] | (v[2 * j + 1] << 16);
            c1[j] = v[2 * j + 1] | (v[2 * j + 2] << 16);
        }
        *(u32x4*)(kr + ch * (2 * KR_COPY * 2) + q * 16) = c0;
        *(u32x4*)(kr + ch * (2 * KR_COPY * 2) + KR_COPY * 2 + q * 16) = c1;
    }
}

DEVI void k1_ln_mod(int blk, const u16* x, const u16* cvec, const u16* wmod, char* ws, char* lds) {
    const int tid = threadIdx.x, lane = tid & 63, wave = tid >> 6;
    const int tok = lane & 7, pg = lane >> 3;
    const u16* xr = x + (size_t)(blk * 8 + tok) * DM + wave * 128 + pg * 8;
    const u32x4 xa = *(const u32x4*)xr, xb = *(const u32x4*)(xr + 64);
    const bool do_mod = blk < 192;
    const int cg = blk % 48, rq = blk / 48;
    u32x4 wv[4];
    u16 cv[4];
    if (do_mod) {
#pragma unroll
        for (int it = 0; it < 4; ++it) {
            const int row = rq * 128 + it * 32 + (tid >> 3);
            wv[it] = *(const u32x4*)(wmod + (size_t)row * 3072 + cg * 64 + (tid & 7) * 8);
            cv[it] = cvec[row];
        }
    }
    float xf[16];
#pragma unroll
    for (int j = 0; j < 4; ++j) {
        xf[2 * j] = bflo(xa[j]); xf[2 * j + 1] = bfhi(xa[j]);
        xf[8 + 2 * j] = bflo(xb[j]); xf[8 + 2 * j + 1] = bfhi(xb[j]);
    }
    float* red = (float*)lds;
    float s = 0.f;
#pragma unroll
    for (int j = 0; j < 16; ++j) s += xf[j];
    s += __shfl_xor(s, 8); s += __shfl_xor(s, 16); s += __shfl_xor(s, 32);
    if (lane < 8) red[wave * 8 + tok] = s;
    __syncthreads();
    const float mean = ((red[tok] + red[8 + tok]) + (red[16 + tok] + red[24 + tok])) * (1.f / DM);
    float q = 0.f;
#pragma unroll
    for (int j = 0; j < 16; ++j) { const float t = xf[j] - mean; q += t * t; }
    q += __shfl_xor(q, 8); q += __shfl_xor(q, 16); q += __shfl_xor(q, 32);
    if (lane < 8) red[32 + wave * 8 + tok] = q;
    float macc[8];
    if (do_mod) {
#pragma unroll
        for (int j = 0; j < 8; ++j) macc[j] = 0.f;
#pragma unroll
        for (int it = 0; it < 4; ++it) {
            const float cf = bf2f(cv[it]);
            const float sl = rbf(cf / (1.f + __expf(-cf)));
#pragma unroll
            for (int j = 0; j < 4; ++j) { macc[2 * j] += sl * bflo(wv[it][j]); macc[2 * j + 1] += sl * bfhi(wv[it][j]); }
        }
#pragma unroll
        for (int j = 0; j < 8; ++j) {
            macc[j] += __shfl_xor(macc[j], 8); macc[j] += __shfl_xor(macc[j], 16); macc[j] += __shfl_xor(macc[j], 32);
        }
        if (lane < 8) {
#pragma unroll
            for (int j = 0; j < 8; ++j) red[64 + wave * 64 + lane * 8 + j] = macc[j];
        }
    }
    __syncthreads();
    const float var = ((red[32 + tok] + red[40 + tok]) + (red[48 + tok] + red[56 + tok])) * (1.f / DM);
    const float mb = rbf(mean);
    const float den = rbf(__builtin_sqrtf(rbf(rbf(var) + 1e-5f)));
    const float rinv = 1.f / den;
    char* zx = ws + WS_ZX + (size_t)blk * 16384 + tok * 8;
#pragma unroll
    for (int hlf = 0; hlf < 2; ++hlf) {
#pragma unroll
        for (int j = 0; j < 4; ++j) {
            const float x0 = xf[hlf * 8 + 2 * j], x1 = xf[hlf * 8 + 2 * j + 1];
            const u32 dd = pack2(x0 - mb, x1 - mb);
            const u32 zz = pack2(bflo(dd) * rinv, bfhi(dd) * rinv);
            const u32 xx = hlf ? xb[j] : xa[j];
            u32x2 o;
            o.x = (zz & 0xffffu) | (xx << 16);
            o.y = (zz >> 16) | (xx & 0xffff0000u);
            const int pr = wave * 64 + hlf * 32 + pg * 4 + j;
            *(u32x2*)(zx + pr * 64) = o;
        }
    }
    if (do_mod && tid < 64) {
        const float v = (red[64 + tid] + red[128 + tid]) + (red[192 + tid] + red[256 + tid]);
        ((float*)(ws + WS_MOD))[rq * 3072 + cg * 64 + tid] = v;
    }
}

__global__ __launch_bounds__(256) void k1_prep(const u16* x, const u16* Are, const u16* Aim, const u16* Bre,
                                               const u16* Bim, const u16* Cre, const u16* Cim, const u16* logdt,
                                               const u16* cvec, const u16* wmod, char* ws) {
    extern __shared__ __attribute__((aligned(16))) char lds[];
    const int bid = blockIdx.x;
    if (bid < 256) k1_kgen((bid & 7) * 32 + (bid >> 3), Are, Aim, Bre, Bim, Cre, Cim, logdt, ws, lds);
    else k1_ln_mod(bid - 256, x, cvec, wmod, ws, lds);
}

constexpr int C_KR = 0;
constexpr int C_H = KR_PAIR_BYTES;
constexpr int C_H_CH = (256 + 2048) * 2;
constexpr int C_P = C_H + 2 * C_H_CH;
constexpr int K2_LDS = C_P + 4 * 8192;

DEVI void glds16(const void* gsrc, u32 lds_dst) {
    u32 keep;
    asm volatile("s_mov_b32 %0, m0\n\ts_mov_b32 m0, %2\n\ts_nop 0\n\tglobal_load_lds_dwordx4 %1, off\n\ts_mov_b32 m0, %0"
                 : "=&s"(keep) : "v"(gsrc), "s"(lds_dst) : "memory");
}

DEVI void k2_conv(int pair, const u16* dskip, char* ws, char* lds) {
    const int tid = threadIdx.x, lane = tid & 63;
    const int wave = __builtin_amdgcn_readfirstlane(tid >> 6);
    const u32 lbase = (u32)(size_t)lds;
    u32x4 zx[4];
#pragma unroll
    for (int i = 0; i < 4; ++i) {
        const int tp = i * 256 + tid;
        zx[i] = *(const u32x4*)(ws + WS_ZX + (size_t)(tp >> 2) * 16384 + pair * 64 + (tp & 3) * 16);
    }
    const float* mp = (const float*)(ws + WS_MOD);
    const int d0 = pair * 2;
    const float sa0 = modsum(mp, d0), sa1 = modsum(mp, d0 + 1);
    const float ca0 = rbf(1.f + modsum(mp, 512 + d0)), ca1 = rbf(1.f + modsum(mp, 512 + d0 + 1));
    const float ga0 = modsum(mp, 1024 + d0), ga1 = modsum(mp, 1024 + d0 + 1);
    const float ds0 = bf2f(dskip[d0]), ds1 = bf2f(dskip[d0 + 1]);
    {
        const char* src = ws + WS_KR + (size_t)pair * KR_PAIR_BYTES + lane * 16;
        for (int p = wave; p < 17; p += 4) glds16(src + p * 1024, lbase + C_KR + p * 1024);
    }
    if (tid < 64) *(u32x4*)(lds + C_H + (tid >> 5) * C_H_CH + (tid & 31) * 16) = u32x4{0u, 0u, 0u, 0u};
    u32 h0[4], h1[4];
#pragma unroll
    for (int i = 0; i < 4; ++i) {
        h0[i] = pack2(bflo(zx[i].x) * ca0 + sa0, bflo(zx[i].z) * ca0 + sa0);
        h1[i] = pack2(bflo(zx[i].y) * ca1 + sa1, bflo(zx[i].w) * ca1 + sa1);
        const int tp = i * 256 + tid;
        *(u32*)(lds + C_H + 512 + tp * 4) = h0[i];
        *(u32*)(lds + C_H + C_H_CH + 512 + tp * 4) = h1[i];
    }
    asm volatile("s_waitcnt vmcnt(0)" ::: "memory");
    __syncthreads();

    const int ch = wave >> 1, hf = wave & 1;
    const int m = lane & 15, kq = lane >> 4;
    const char* ka = lds + C_KR + ch * (2 * KR_COPY * 2) + ((m & 1) ? 0 : KR_COPY * 2) + 4092 - 2 * (m & ~1) + 16 * kq - 32 * (2 * hf + 1);
    const char* hb = lds + C_H + ch * C_H_CH + 512 + 32 * m + 16 * kq - 32 * (2 * hf + 1);
    f32x4 acc[8];
#pragma unroll
    for (int c = 0; c < 8; ++c) acc[c] = f32x4{0.f, 0.f, 0.f, 0.f};
#pragma unroll
    for (int e = 0; e < 4; ++e) {
        u32x4 A[8], B[8];
#pragma unroll
        for (int i = 0; i < 8; ++i) {
            B[i] = *(const u32x4*)(hb + 512 * i - 128 * e);
            const u32* pa = (const u32*)(ka - 512 * i - 128 * e);
            A[i] = u32x4{pa[0], pa[1], pa[2], pa[3]};
        }
#pragma unroll
        for (int cp = 0; cp < 8; ++cp)
#pragma unroll
            for (int c = cp; c < 8; ++c)
                acc[c] = __builtin_amdgcn_mfma_f32_16x16x32_bf16(asbf(A[c - cp]), asbf(B[cp]), acc[c], 0, 0, 0);
    }
    {
        char* pb = lds + C_P + wave * 8192;
        const int s = 4 * m + kq;
        const int ph = s ^ ((s >> 3) & 3);
#pragma unroll
        for (int c = 0; c < 8; ++c) *(f32x4*)(pb + c * 1024 + ph * 16) = acc[c];
    }
    __syncthreads();
    u32* x2 = (u32*)(ws + WS_X2) + (size_t)pair * SEQ;
#pragma unroll
    for (int i = 0; i < 4; ++i) {
        const int tp = i * 256 + tid;
        const int s = tp >> 1;
        const char* pb = lds + C_P + (s ^ ((s >> 3) & 3)) * 16 + (tp & 1) * 8;
        const f32x2 y0 = *(const f32x2*)pb + *(const f32x2*)(pb + 8192);
        const f32x2 y1 = *(const f32x2*)(pb + 16384) + *(const f32x2*)(pb + 24576);
        u32x2 o;
        o.x = pack2(bfhi(zx[i].x) + ga0 * (y0.x + bflo(h0[i]) * ds0), bfhi(zx[i].y) + ga1 * (y1.x + bflo(h1[i]) * ds1));
        o.y = pack2(bfhi(zx[i].z) + ga0 * (y0.y + bfhi(h0[i]) * ds0), bfhi(zx[i].w) + ga1 * (y1.y + bfhi(h1[i]) * ds1));
        *(u32x2*)(x2 + tp * 2) = o;
    }
}

DEVI void k2_wprep(int b, const u16* wg, const u16* wu, const u16* wd, char* ws, char* lds) {
    const int tid = threadIdx.x;
    const int mt = b & 1, fgrp = (b >> 1) & 31, dq = b >> 6;
    const u16* w = mt ? wu : wg;
    const float* mp = (const float*)(ws + WS_MOD);
    float* sc = (float*)lds;
    f32x4* red = (f32x4*)(lds + 1024);
    u32 v[2][8], vd[8];
#pragma unroll
    for (int r = 0; r < 2; ++r) {
        const int task = tid + r * 256;
        if (task < 448) {
            const int fp = task % 28, kh = task / 28;
#pragma unroll
            for (int e = 0; e < 8; ++e)
                v[r][e] = *(const u32*)(w + (size_t)(dq * 128 + kh * 8 + e) * FF + fgrp * 56 + fp * 2);
        }
    }
    const int dt = b & 15, kg = b >> 4;
    const int dp = tid & 15, hh = (tid >> 4) & 1, ksd = kg * 7 + (tid >> 5);
    if (tid < 224) {
#pragma unroll
        for (int e = 0; e < 8; ++e)
            vd[e] = *(const u32*)(wd + (size_t)(ksd * 16 + (e >> 2) * 8 + hh * 4 + (e & 3)) * DM + dt * 32 + dp * 2);
    }
    if (tid < 128) sc[tid] = rbf(1.f + modsum(mp, 4 * 512 + dq * 128 + tid));
    else sc[tid] = modsum(mp, 3 * 512 + dq * 128 + tid - 128);
    __syncthreads();
#pragma unroll
    for (int r = 0; r < 2; ++r) {
        const int task = tid + r * 256;
        if (task < 448) {
            const int fp = task % 28, kh = task / 28;
            u32x4 o0, o1;
            f32x4 uv = {0.f, 0.f, 0.f, 0.f};
#pragma unroll
            for (int e = 0; e < 8; e += 2) {
                const float s0 = sc[kh * 8 + e], s1 = sc[kh * 8 + e + 1];
                const float b0 = sc[128 + kh * 8 + e], b1 = sc[128 + kh * 8 + e + 1];
                const float a0 = bflo(v[r][e]), a1 = bflo(v[r][e + 1]), c0 = bfhi(v[r][e]), c1 = bfhi(v[r][e + 1]);
                const u32 p0 = pack2(a0 * s0, a1 * s1), p1 = pack2(c0 * s0, c1 * s1);
                o0[e >> 1] = p0; o1[e >> 1] = p1;
                uv.x += bflo(p0) + bfhi(p0);
                uv.y += bflo(p1) + bfhi(p1);
                uv.z += a0 * b0 + a1 * b1;
                uv.w += c0 * b0 + c1 * b1;
            }
            red[kh * 28 + fp] = uv;
            const int f = fgrp * 56 + fp * 2;
            const int s = f / 112, j = (f % 112) >> 4, rr = (f & 15) + 16 * mt;
            const int ks = dq * 8 + (kh >> 1), h = kh & 1;
            char* dst = ws + WS_WGU + ((size_t)((s * 7 + j) * 32 + ks) * 64 + h * 32 + rr) * 16;
            *(u32x4*)dst = o0;
            *(u32x4*)(dst + 16) = o1;
        }
    }
    if (tid < 224) {
        u32x4 o0, o1;
#pragma unroll
        for (int e = 0; e < 8; e += 2) {
            o0[e >> 1] = (vd[e] & 0xffffu) | (vd[e + 1] << 16);
            o1[e >> 1] = (vd[e] >> 16) | (vd[e + 1] & 0xffff0000u);
        }
        char* dst = ws + WS_WD + ((size_t)(dt * 112 + ksd) * 64 + hh * 32 + dp * 2) * 16;
        *(u32x4*)dst = o0;
        *(u32x4*)(dst + 16) = o1;
    }
    __syncthreads();
    if (tid < 112) {
        const int fp = tid >> 2, comp = tid & 3;
        float sum = 0.f;
#pragma unroll
        for (int kh = 0; kh < 16; ++kh) sum += ((const float*)red)[(kh * 28 + fp) * 4 + comp];
        const int f = fgrp * 56 + fp * 2 + (comp & 1);
        const int col = (f / 112) * 224 + ((f % 112) >> 4) * 32 + (f & 15) + 16 * mt;
        ((float*)(ws + WS_UV))[(dq * 2 + (comp >> 1)) * 3584 + col] = sum;
    }
}

__global__ __launch_bounds__(256, 2) void k2_conv_prep(const u16* dskip, const u16* wg, const u16* wu, const u16* wd, char* ws) {
    extern __shared__ __attribute__((aligned(16))) char lds[];
    const int bid = blockIdx.x;
    if (bid < 256) k2_conv((bid & 7) * 32 + (bid >> 3), dskip, ws, lds);
    else k2_wprep(bid - 256, wg, wu, wd, ws, lds);
}

constexpr int G_UV = 4 * 32768;
constexpr int K3_LDS = G_UV + 2 * 224 * 4;

DEVI void glds_tile(const char* gbase, u32 voff, u32 lds_dst) {
    u32 keep;
    asm volatile("s_mov_b32 %0, m0\n\ts_mov_b32 m0, %3\n\ts_nop 0\n\t"
                 "global_load_lds_dwordx4 %1, %2 offset:-4096\n\t"
                 "global_load_lds_dwordx4 %1, %2 offset:-3072\n\t"
                 "global_load_lds_dwordx4 %1, %2 offset:-2048\n\t"
                 "global_load_lds_dwordx4 %1, %2 offset:-1024\n\t"
                 "global_load_lds_dwordx4 %1, %2\n\t"
                 "global_load_lds_dwordx4 %1, %2 offset:1024\n\t"
                 "global_load_lds_dwordx4 %1, %2 offset:2048\n\t"
                 "global_load_lds_dwordx4 %1, %2 offset:3072\n\t"
                 "s_mov_b32 m0, %0"
                 : "=&s"(keep) : "v"(voff), "s"(gbase), "s"(lds_dst) : "memory");
}

template <int N> DEVI void wait_vm() { asm volatile("s_waitcnt vmcnt(%0)" ::"i"(N) : "memory"); }
DEVI void raw_barrier() {
    asm volatile("s_waitcnt lgkmcnt(0)" ::: "memory");
    __builtin_amdgcn_s_barrier();
    asm volatile("" ::: "memory");
}

__global__ __launch_bounds__(256, 1) void k3_gemm1(char* ws) {
    extern __shared__ __attribute__((aligned(16))) char lds[];
    const int bid = blockIdx.x, xcd = bid & 7, rk = bid >> 3;
    const int mt = (xcd >> 2) * 8 + (rk >> 2), sl = (xcd & 3) * 4 + (rk & 3);
    const int tid = threadIdx.x, lane = tid & 63;
    const int wave = __builtin_amdgcn_readfirstlane(tid >> 6);
    const u32 lbase = (u32)(size_t)lds;
    const int tok = lane & 31, h = lane >> 5;

    float uvreg[2];
    {
        const float* uv = (const float*)(ws + WS_UV) + sl * 224;
#pragma unroll
        for (int r = 0; r < 2; ++r) {
            const int i = tid < 224 ? tid : 0;
            uvreg[r] = (uv[r * 3584 + i] + uv[(2 + r) * 3584 + i]) + (uv[(4 + r) * 3584 + i] + uv[(6 + r) * 3584 + i]);
        }
    }
    const char* wsrc = ws + WS_WGU + (size_t)sl * 7 * 32768 + wave * 8192 + 4096;
    const u32 wl = lbase + wave * 8192 + 4096;
    const u32 voff = lane * 16;
    if (tid < 224) {
        const float sc = (tid & 16) ? 1.f : -1.44269504088896341f;
        ((float*)(lds + G_UV))[tid] = uvreg[0] * sc;
        ((float*)(lds + G_UV))[224 + tid] = uvreg[1] * sc;
    }
    glds_tile(wsrc, voff, wl);

    u32x4 X[32];
    const char* xbase = ws + WS_X2 + (size_t)(mt * 128 + wave * 32) * 4;
    const u32 xo = tok * 4 + h * 4 * 8192;
    auto load_x = [&](int ks) {
        const char* p = xbase + (size_t)ks * 8 * 8192;
        const u32 o1 = xo + 8192, o2 = xo + 16384, o3 = xo + 24576;
        u32 a, b, c, d;
        asm volatile("global_load_dword %0, %4, %8\n\tglobal_load_dword %1, %5, %8\n\t"
                     "global_load_dword %2, %6, %8\n\tglobal_load_dword %3, %7, %8"
                     : "=&v"(a), "=&v"(b), "=&v"(c), "=&v"(d)
                     : "v"(xo), "v"(o1), "v"(o2), "v"(o3), "s"(p) : "memory");
        X[ks] = u32x4{a, b, c, d};
    };
#pragma unroll
    for (int ks = 0; ks < 12; ++ks) load_x(ks);

    const char* abase = lds + lane * 16;
    f32x16 acc[2];
    float sum = 0.f, ssq = 0.f;
    const u32 ones = 0x3f803f80u;
#pragma unroll
    for (int i = 0; i < 16; ++i) acc[0][i] = 0.f;
#pragma unroll
    for (int g = 0; g < 8; ++g) {
        if (g < 6) wait_vm<32>(); else if (g == 6) wait_vm<16 + 24>(); else wait_vm<24>();
#pragma unroll
        for (int k = 0; k < 4; ++k) asm volatile("" : "+v"(X[g * 4 + k]));
        if (g == 0) raw_barrier();
        if (g < 5) {
#pragma unroll
            for (int k = 0; k < 4; ++k) load_x(12 + g * 4 + k);
        }
        if (g == 5) {
            glds_tile(wsrc + 32768, voff, wl + 32768);
            glds_tile(wsrc + 2 * 32768, voff, wl + 2 * 32768);
            glds_tile(wsrc + 3 * 32768, voff, wl + 3 * 32768);
        }
#pragma unroll
        for (int k = 0; k < 4; ++k) {
            const int ks = g * 4 + k;
            const u32x4 a = *(const u32x4*)(abase + ks * 1024);
            acc[0] = __builtin_amdgcn_mfma_f32_32x32x16_bf16(asbf(a), asbf(X[ks]), acc[0], 0, 0, 0);
#pragma unroll
            for (int q = 0; q < 4; ++q) {
                const u32 xq = X[ks][q];
                const bf16x2 xv = __builtin_bit_cast(bf16x2, xq);
                sum = __builtin_amdgcn_fdot2_f32_bf16(xv, __builtin_bit_cast(bf16x2, ones), sum, false);
                ssq = __builtin_amdgcn_fdot2_f32_bf16(xv, xv, ssq, false);
            }
        }
    }
    sum += __shfl_xor(sum, 32);
    ssq += __shfl_xor(ssq, 32);
    const float mean = sum * (1.f / DM);
    const float var = fmaxf(ssq * (1.f / DM) - mean * mean, 0.f);
    const float rinv = 1.f / rbf(__builtin_sqrtf(rbf(rbf(var) + 1e-5f)));
    const float nmr = -rbf(mean) * rinv;

    char* hdst = ws + WS_HH + ((size_t)(mt * 4 + wave) * 112 + sl * 7) * 1024 + lane * 16;
    auto epilogue = [&](int j, const f32x16& a) {
        const float* up = (const float*)(lds + G_UV) + j * 32 + 4 * h;
        u32x4 o;
#pragma unroll
        for (int gq = 0; gq < 2; ++gq) {
            const f32x4 ug = *(const f32x4*)(up + gq * 8), vg = *(const f32x4*)(up + 224 + gq * 8);
            const f32x4 uu = *(const f32x4*)(up + 16 + gq * 8), vu = *(const f32x4*)(up + 224 + 16 + gq * 8);
            float r[4];
#pragma unroll
            for (int i = 0; i < 4; ++i) {
                const float gl = fmaf(a[gq * 4 + i], rinv * -1.44269504088896341f, fmaf(nmr, ug[i], vg[i]));
                const float ul = fmaf(a[8 + gq * 4 + i], rinv, fmaf(nmr, uu[i], vu[i]));
                r[i] = ul * __builtin_amdgcn_rcpf(1.f + __builtin_amdgcn_exp2f(gl));
            }
            o[gq * 2] = pack2(r[0], r[1]);
            o[gq * 2 + 1] = pack2(r[2], r[3]);
        }
        *(u32x4*)(hdst + j * 1024) = o;
    };
#pragma unroll
    for (int j = 1; j < 7; ++j) {
        if (j == 1) wait_vm<16>(); else if (j == 2) wait_vm<17>(); else if (j == 3) wait_vm<18>();
        else if (j == 4) wait_vm<19>(); else if (j == 5) wait_vm<11>(); else wait_vm<3>();
        raw_barrier();
        if (j <= 3) glds_tile(wsrc + (j + 3) * 32768, voff, wl + ((j + 3) & 3) * 32768);
        f32x16& cur = acc[j & 1];
        epilogue(j - 1, acc[(j - 1) & 1]);
#pragma unroll
        for (int i = 0; i < 16; ++i) cur[i] = 0.f;
#pragma unroll
        for (int ks = 0; ks < 32; ++ks) {
            const u32x4 a = *(const u32x4*)(abase + (j & 3) * 32768 + ks * 1024);
            cur = __builtin_amdgcn_mfma_f32_32x32x16_bf16(asbf(a), asbf(X[ks]), cur, 0, 0, 0);
        }
        asm volatile("" ::: "memory");
    }
    epilogue(6, acc[0]);
}

constexpr int K4_LDS = 4 * 3 * 4096;

__global__ __launch_bounds__(256, 1) void k4_gemm2(char* ws, u16* out) {
    extern __shared__ __attribute__((aligned(16))) char lds[];
    const int bid = blockIdx.x, xcd = bid & 7, rk = bid >> 3;
    const int t64 = xcd * 4 + (rk >> 3), c64 = rk & 7;
    const int tid = threadIdx.x, lane = tid & 63;
    const int wave = __builtin_amdgcn_readfirstlane(tid >> 6);
    const char* pa0 = ws + WS_WD + ((size_t)(c64 * 2) * 112 + wave * 28) * 1024 + lane * 16;
    const char* pa1 = pa0 + 112 * 1024;
    const char* pb0 = ws + WS_HH + ((size_t)(t64 * 2) * 112 + wave * 28) * 1024 + lane * 16;
    const char* pb1 = pb0 + 112 * 1024;
    constexpr int PD = 14;
    u32x4 A0[28], A1[28], B0[28], B1[28];
#pragma unroll
    for (int s = 0; s < PD; ++s) {
        A0[s] = *(const u32x4*)(pa0 + s * 1024); B0[s] = *(const u32x4*)(pb0 + s * 1024);
        A1[s] = *(const u32x4*)(pa1 + s * 1024); B1[s] = *(const u32x4*)(pb1 + s * 1024);
    }
    __builtin_amdgcn_sched_barrier(0);
    f32x16 acc[4];
#pragma unroll
    for (int t = 0; t < 4; ++t)
#pragma unroll
        for (int i = 0; i < 16; ++i) acc[t][i] = 0.f;
#pragma unroll
    for (int s = 0; s < 28; ++s) {
        acc[0] = __builtin_amdgcn_mfma_f32_32x32x16_bf16(asbf(A0[s]), asbf(B0[s]), acc[0], 0, 0, 0);
        acc[2] = __builtin_amdgcn_mfma_f32_32x32x16_bf16(asbf(A1[s]), asbf(B0[s]), acc[2], 0, 0, 0);
        acc[1] = __builtin_amdgcn_mfma_f32_32x32x16_bf16(asbf(A0[s]), asbf(B1[s]), acc[1], 0, 0, 0);
        acc[3] = __builtin_amdgcn_mfma_f32_32x32x16_bf16(asbf(A1[s]), asbf(B1[s]), acc[3], 0, 0, 0);
        if (s + PD < 28) {
            const int n = s + PD;
            A0[n] = *(const u32x4*)(pa0 + n * 1024); B0[n] = *(const u32x4*)(pb0 + n * 1024);
            A1[n] = *(const u32x4*)(pa1 + n * 1024); B1[n] = *(const u32x4*)(pb1 + n * 1024);
        }
        __builtin_amdgcn_sched_barrier(0);
    }
    const int dtl = wave >> 1, ttl = wave & 1;
    const int tok = t64 * 64 + ttl * 32 + (lane & 31), h = lane >> 5;
    const int dbase = c64 * 64 + dtl * 32 + 4 * h;
    const u32* x2 = (const u32*)(ws + WS_X2);
    const float* mp = (const float*)(ws + WS_MOD) + 5 * 512;
    u32 xr[8];
    f32x4 gb[4];
#pragma unroll
    for (int g = 0; g < 4; ++g) {
        xr[2 * g] = x2[(size_t)((dbase + 8 * g) >> 1) * SEQ + tok];
        xr[2 * g + 1] = x2[(size_t)(((dbase + 8 * g) >> 1) + 1) * SEQ + tok];
        const float* m4 = mp + dbase + 8 * g;
        gb[g] = (*(const f32x4*)m4 + *(const f32x4*)(m4 + 3072)) + (*(const f32x4*)(m4 + 2 * 3072) + *(const f32x4*)(m4 + 3 * 3072));
    }
#pragma unroll
    for (int t = 0; t < 4; ++t) {
        if (t != wave) {
            const int slot = t * 3 + (wave > t ? wave - 1 : wave);
#pragma unroll
            for (int g = 0; g < 4; ++g)
                *(f32x4*)(lds + slot * 4096 + g * 1024 + lane * 16) = f32x4{acc[t][4 * g], acc[t][4 * g + 1], acc[t][4 * g + 2], acc[t][4 * g + 3]};
        }
    }
    f32x16 mine = wave == 0 ? acc[0] : wave == 1 ? acc[1] : wave == 2 ? acc[2] : acc[3];
    __syncthreads();
    u16* orow = out + (size_t)tok * DM + dbase;
#pragma unroll
    for (int g = 0; g < 4; ++g) {
        const char* pp = lds + wave * 3 * 4096 + g * 1024 + lane * 16;
        const f32x4 s3 = *(const f32x4*)pp + *(const f32x4*)(pp + 4096) + *(const f32x4*)(pp + 8192);
        u32x2 o;
        o.x = pack2(bflo(xr[2 * g]) + rbf(gb[g].x) * (mine[4 * g] + s3.x), bfhi(xr[2 * g]) + rbf(gb[g].y) * (mine[4 * g + 1] + s3.y));
        o.y = pack2(bflo(xr[2 * g + 1]) + rbf(gb[g].z) * (mine[4 * g + 2] + s3.z), bfhi(xr[2 * g + 1]) + rbf(gb[g].w) * (mine[4 * g + 3] + s3.w));
        *(u32x2*)(orow + 8 * g) = o;
    }
}

extern "C" void kernel_launch(void* const* d_in, const int* in_sizes, int n_in, void* d_out, int out_size,
                              void* d_ws, size_t ws_size, hipStream_t stream) {
    const u16* x = (const u16*)d_in[0];
    char* ws = (char*)d_ws;
    (void)hipFuncSetAttribute((const void*)k1_prep, hipFuncAttributeMaxDynamicSharedMemorySize, K1_LDS);
    (void)hipFuncSetAttribute((const void*)k2_conv_prep, hipFuncAttributeMaxDynamicSharedMemorySize, K2_LDS);
    (void)hipFuncSetAttribute((const void*)k3_gemm1, hipFuncAttributeMaxDynamicSharedMemorySize, K3_LDS);
    (void)hipFuncSetAttribute((const void*)k4_gemm2, hipFuncAttributeMaxDynamicSharedMemorySize, K4_LDS);
    k1_prep<<<512, 256, K1_LDS, stream>>>(x, (const u16*)d_in[1], (const u16*)d_in[2], (const u16*)d_in[3],
                                          (const u16*)d_in[4], (const u16*)d_in[5], (const u16*)d_in[6],
                                          (const u16*)d_in[7], (const u16*)d_in[13], (const u16*)d_in[14], ws);
    k2_conv_prep<<<512, 256, K2_LDS, stream>>>((const u16*)d_in[8], (const u16*)d_in[10], (const u16*)d_in[11],
                                               (const u16*)d_in[12], ws);
    k3_gemm1<<<256, 256, K3_LDS, stream>>>(ws);
    k4_gemm2<<<256, 256, K4_LDS, stream>>>(ws, (u16*)d_out);
}
```

```cpp
#include <hip/hip_runtime.h>
#include <hip/hip_bf16.h>

typedef unsigned short u16;
typedef unsigned u32;
typedef u32 u32x2 __attribute__((ext_vector_type(2)));
typedef u32 u32x4 __attribute__((ext_vector_type(4)));
typedef float f32x2 __attribute__((ext_vector_type(2)));
typedef float f32x4 __attribute__((ext_vector_type(4)));
typedef float f32x16 __attribute__((ext_vector_type(16)));
typedef __bf16 bf16x2 __attribute__((ext_vector_type(2)));
typedef __bf16 bf16x8 __attribute__((ext_vector_type(8)));

#define DEVI __device__ __forceinline__
#pragma clang diagnostic ignored "-Wint-to-pointer-cast"

constexpr int SEQ = 2048, DM = 512, NS = 128, FF = 1792;
constexpr int KR_COPY = 2080;

constexpr size_t WS_ZX  = 0;
constexpr size_t WS_FL  = WS_ZX + (size_t)256 * 16384;
constexpr size_t WS_GB  = WS_FL + 1024 * 128;
constexpr size_t WS_MOD = WS_GB + 4096;
constexpr size_t WS_X2  = WS_MOD + 4 * 3072 * 4;
constexpr size_t WS_WGU = WS_X2 + (size_t)256 * 2048 * 4;
constexpr size_t WS_UV  = WS_WGU + (size_t)16 * 7 * 32 * 1024;
constexpr size_t WS_WD  = WS_UV + 4 * 2 * 3584 * 4;
constexpr size_t WS_HH  = WS_WD + (size_t)16 * 112 * 1024;
constexpr size_t WS_END = WS_HH + (size_t)64 * 112 * 1024;

#define STAMP(k, i)
#define STAMPC(k, i)
#define STAMPX(k, i)
#define STAMPFLUSH(k, a, b)
#define STAMPDECL

DEVI float bflo(u32 v) { return __builtin_bit_cast(float, v << 16); }
DEVI float bfhi(u32 v) { return __builtin_bit_cast(float, v & 0xffff0000u); }
DEVI float bf2f(u16 v) { return __builtin_bit_cast(float, (u32)v << 16); }
DEVI u32 pack2(float lo, float hi) {
    f32x2 f = {lo, hi};
    return __builtin_bit_cast(u32, __builtin_convertvector(f, bf16x2));
}
DEVI float rbf(float x) { return bflo(pack2(x, x)); }
DEVI bf16x8 asbf(u32x4 v) { return __builtin_bit_cast(bf16x8, v); }
DEVI float modsum(const float* mp, int col) {
    return rbf((mp[col] + mp[3072 + col]) + (mp[2 * 3072 + col] + mp[3 * 3072 + col]));
}

DEVI float dot2(u32 a, u32 b, float c) {
    return __builtin_amdgcn_fdot2_f32_bf16(__builtin_bit_cast(bf16x2, a), __builtin_bit_cast(bf16x2, b), c, false);
}
DEVI float swap32_add(float a, float b) {
    const auto r = __builtin_amdgcn_permlane32_swap(__builtin_bit_cast(u32, a), __builtin_bit_cast(u32, b), false, false);
    const u32 r0 = r[0], r1 = r[1];
    return __builtin_bit_cast(float, r0) + __builtin_bit_cast(float, r1);
}
DEVI float swap16_add(float a, float b) {
    const auto r = __builtin_amdgcn_permlane16_swap(__builtin_bit_cast(u32, a), __builtin_bit_cast(u32, b), false, false);
    const u32 r0 = r[0], r1 = r[1];
    return __builtin_bit_cast(float, r0) + __builtin_bit_cast(float, r1);
}
template <int CTRL> DEVI float dpp(float a) {
    return __builtin_bit_cast(float, __builtin_amdgcn_update_dpp(0, __builtin_bit_cast(int, a), CTRL, 0xf, 0xf, true));
}
DEVI u32 lo_pair(u32 a, u32 b) { return __builtin_amdgcn_perm(b, a, 0x05040100u); }
DEVI u32 hi_pair(u32 a, u32 b) { return __builtin_amdgcn_perm(b, a, 0x07060302u); }
DEVI __amdgpu_buffer_rsrc_t mkrsrc(const void* p, u32 bytes) {
    return __builtin_amdgcn_make_buffer_rsrc((void*)p, 0, bytes, 0x00020000);
}
constexpr int AUX_SC1 = 16;
constexpr u32 ONES = 0x3f803f80u;

__global__ __launch_bounds__(512) void k1_prep(const u16* __restrict__ x, const u16* __restrict__ Are,
                                               const u16* __restrict__ Aim, const u16* __restrict__ Bre,
                                               const u16* __restrict__ Bim, const u16* __restrict__ Cre,
                                               const u16* __restrict__ Cim, const u16* __restrict__ cvec,
                                               const u16* __restrict__ wmod, char* __restrict__ ws) {
    __shared__ __attribute__((aligned(16))) float red[64 + 256];
    const int blk = blockIdx.x, tid = threadIdx.x, lane = tid & 63;
    const int wave = __builtin_amdgcn_readfirstlane(tid >> 6);
    STAMP(0, 0);
    if (wave < 4) {
        const int t2 = lane & 3, pg = lane >> 2;
        const u16* xr = x + (size_t)(blk * 8 + t2 * 2) * DM + wave * 128 + pg * 8;
        const u32x4 xa = *(const u32x4*)xr, xb = *(const u32x4*)(xr + DM);
        float s0 = 0.f, q0 = 0.f, s1 = 0.f, q1 = 0.f;
#pragma unroll
        for (int j = 0; j < 4; ++j) {
            s0 = dot2(xa[j], ONES, s0); q0 = dot2(xa[j], xa[j], q0);
            s1 = dot2(xb[j], ONES, s1); q1 = dot2(xb[j], xb[j], q1);
        }
        float v = swap16_add(swap32_add(s0, s1), swap32_add(q0, q1));
        v += dpp<0x124>(v);
        v += dpp<0x128>(v);
        if ((lane & 12) == 0) red[t2 * 16 + (lane >> 4) * 4 + wave] = v;
        STAMP(0, 1);
        __syncthreads();
        float rinv[2], nb[2];
#pragma unroll
        for (int r = 0; r < 2; ++r) {
            const f32x4 sv = *(const f32x4*)(red + t2 * 16 + r * 8), qv = *(const f32x4*)(red + t2 * 16 + r * 8 + 4);
            const float mean = ((sv.x + sv.y) + (sv.z + sv.w)) * (1.f / DM);
            const float var = fmaxf(((qv.x + qv.y) + (qv.z + qv.w)) * (1.f / DM) - mean * mean, 0.f);
            rinv[r] = __builtin_amdgcn_rcpf(rbf(__builtin_amdgcn_sqrtf(rbf(rbf(var) + 1e-5f))));
            nb[r] = -rbf(mean) * rinv[r];
        }
        const auto rs = mkrsrc(ws + WS_ZX + (size_t)blk * 16384, 16384);
        const u32 vo = (wave * 64 + pg * 4) * 64 + t2 * 16;
#pragma unroll
        for (int j = 0; j < 4; ++j) {
            const u32 za = pack2(fmaf(bflo(xa[j]), rinv[0], nb[0]), fmaf(bfhi(xa[j]), rinv[0], nb[0]));
            const u32 zb = pack2(fmaf(bflo(xb[j]), rinv[1], nb[1]), fmaf(bfhi(xb[j]), rinv[1], nb[1]));
            const u32x4 o = {lo_pair(za, xa[j]), hi_pair(za, xa[j]), lo_pair(zb, xb[j]), hi_pair(zb, xb[j])};
            __builtin_amdgcn_raw_buffer_store_b128(o, rs, vo + j * 64, 0, AUX_SC1);
        }
        STAMP(0, 2);
    } else {
        const int t = tid - 256;
        const int cg = blk % 48, rq = blk / 48;
        u32x4 pf;
        {
            const int pair = (blk & 7) * 32 + (blk >> 3);
            const u16* pa = wave == 4 ? Are : wave == 5 ? Bre : Cre;
            const u16* pb = wave == 4 ? Aim : wave == 5 ? Bim : Cim;
            const u16* p = (lane < 32 ? pa : pb) + pair * 256 + (lane & 31) * 8;
            asm volatile("global_load_dwordx4 %0, %1, off" : "=v"(pf) : "v"(p) : "memory");
        }
        if (t < 4) *(u32*)(ws + WS_FL + (size_t)(blk * 4 + t) * 128) = 0u;
        float colsum = 0.f;
        if (blk < 192) {
            const int row = rq * 128 + (t >> 3) * 4;
            const u16* wp = wmod + (size_t)row * 3072 + cg * 64 + (t & 7) * 8;
            const u32x4 w0 = *(const u32x4*)wp, w1 = *(const u32x4*)(wp + 3072);
            const u32x4 w2 = *(const u32x4*)(wp + 2 * 3072), w3 = *(const u32x4*)(wp + 3 * 3072);
            const u32x2 cv = *(const u32x2*)(cvec + row);
            auto silu = [](float c) { return c * __builtin_amdgcn_rcpf(1.f + __builtin_amdgcn_exp2f(c * -1.44269504088896341f)); };
            const u32 s01 = pack2(silu(bflo(cv.x)), silu(bfhi(cv.x))), s23 = pack2(silu(bflo(cv.y)), silu(bfhi(cv.y)));
            float a[8];
#pragma unroll
            for (int j = 0; j < 4; ++j) {
                a[2 * j] = dot2(lo_pair(w2[j], w3[j]), s23, dot2(lo_pair(w0[j], w1[j]), s01, 0.f));
                a[2 * j + 1] = dot2(hi_pair(w2[j], w3[j]), s23, dot2(hi_pair(w0[j], w1[j]), s01, 0.f));
            }
            const float b0 = swap16_add(swap32_add(a[0], a[4]), swap32_add(a[2], a[6]));
            const float b1 = swap16_add(swap32_add(a[1], a[5]), swap32_add(a[3], a[7]));
            const bool odd = lane & 8;
            colsum = (odd ? b1 : b0) + dpp<0x128>(odd ? b0 : b1);
            red[64 + (wave - 4) * 64 + (lane & 7) * 8 + (lane >> 5) * 4 + ((lane >> 4) & 1) * 2 + ((lane >> 3) & 1)] = colsum;
        }
        __syncthreads();
        if (blk < 192 && t < 64) {
            const float v = (red[64 + t] + red[128 + t]) + (red[192 + t] + red[256 + t]);
            ((float*)(ws + WS_MOD))[rq * 3072 + cg * 64 + t] = v;
        }
        asm volatile("s_waitcnt vmcnt(0)" ::"v"(pf) : "memory");
    }
}

constexpr int TAB_STRIDE = 544;
constexpr int TAB_CH = 57 * TAB_STRIDE;
constexpr int C_KR = 0;
constexpr int C_P = 4 * KR_COPY * 2;
constexpr int C_KL = 2 * TAB_CH;
constexpr int C_H = C_KL + 2 * 2080 * 2;
constexpr int C_H_CH = (256 + 2048) * 2;
constexpr int K2_LDS = C_H + 2 * C_H_CH;
static_assert(C_P + 4 * 8192 <= C_KL && K2_LDS <= 81920, "LDS map");

#define LDSP(T, off) ((__attribute__((address_space(3))) T*)(off))
#define SB() __builtin_amdgcn_sched_barrier(0)

struct cmulk { f32x2 b, bs; };
DEVI cmulk mkmul(f32x2 b) { return cmulk{b, f32x2{-b.y, b.x}}; }
DEVI f32x2 cmul(f32x2 a, const cmulk& k) { return __builtin_elementwise_fma(a.yy, k.bs, a.xx * k.b); }
DEVI f32x2 cexp_l(float lr2, float lrev, float l) {
    const float mag = __builtin_amdgcn_exp2f(lr2 * l);
    const float rev = __builtin_amdgcn_fractf(lrev * l);
    return f32x2{mag * __builtin_amdgcn_cosf(rev), mag * __builtin_amdgcn_sinf(rev)};
}

DEVI void k2_conv(int pair, const u16* __restrict__ Are, const u16* __restrict__ Aim, const u16* __restrict__ Bre,
                  const u16* __restrict__ Bim, const u16* __restrict__ Cre, const u16* __restrict__ Cim,
                  const u16* __restrict__ logdt, const u16* __restrict__ dskip, char* __restrict__ ws, u32 lb) {
    const int tid = threadIdx.x, lane = tid & 63;
    const int wave = __builtin_amdgcn_readfirstlane(tid >> 6);
    const int ch = wave >> 1, hf = wave & 1;
    __builtin_amdgcn_s_setprio(2);
    STAMP(1, 0);
    STAMPC(1, 14);
    const int idx = pair * 256 + tid;
    const u16 rare = Are[idx], raim = Aim[idx], rbre = Bre[idx], rbim = Bim[idx], rcre = Cre[idx], rcim = Cim[idx];
    const u16 rldt = logdt[pair * 2 + ch];
    SB();
    u32x4 zx[4];
#pragma unroll
    for (int i = 0; i < 2; ++i) {
        const int tq = i * 256 + tid;
        const char* p = ws + WS_ZX + (size_t)(tq >> 1) * 16384 + pair * 64 + (tq & 1) * 32;
        zx[2 * i] = *(const u32x4*)p;
        zx[2 * i + 1] = *(const u32x4*)(p + 16);
    }
    const float* mp = (const float*)(ws + WS_MOD);
    const int d0 = pair * 2;
    const float sa0 = modsum(mp, d0), sa1 = modsum(mp, d0 + 1);
    const float ca0 = rbf(1.f + modsum(mp, 512 + d0)), ca1 = rbf(1.f + modsum(mp, 512 + d0 + 1));
    const float ga0 = modsum(mp, 1024 + d0), ga1 = modsum(mp, 1024 + d0 + 1);
    const float gd0 = ga0 * bf2f(dskip[d0]), gd1 = ga1 * bf2f(dskip[d0 + 1]);
    if (tid < 64) *LDSP(u32x4, lb + C_H + (tid >> 5) * C_H_CH + (tid & 31) * 16) = u32x4{0u, 0u, 0u, 0u};
    if (tid < 2) *LDSP(u32x4, lb + C_KL + tid * 4160) = u32x4{0u, 0u, 0u, 0u};
    {
        const float dt = rbf(expf(bf2f(rldt)));
        const float lr2 = (dt * bf2f(rare)) * 1.44269504088896341f, lrev = (dt * bf2f(raim)) * 0.15915494309189535f;
        const f32x2 W = cmul(f32x2{bf2f(rcre), bf2f(rcim)}, mkmul(f32x2{dt * bf2f(rbre), dt * bf2f(rbim)}));
        const f32x2 p1 = cexp_l(lr2, lrev, 1.f), p8 = cexp_l(lr2, lrev, 8.f);
        const cmulk P1 = mkmul(p1), P8c = mkmul(f32x2{p8.x, -p8.y}), Q = mkmul(cexp_l(lr2, lrev, 128.f));
        const f32x2 p2 = cmul(p1, P1);
        const cmulk P2 = mkmul(p2), P4 = mkmul(cmul(p2, P2));
        STAMP(1, 1);
        const u32 tab = lb + ch * TAB_CH + (tid & 127) * 4;
        auto put = [&](int row, f32x2 v) { *LDSP(u32, tab + row * TAB_STRIDE) = pack2(v.x, v.y); };
        f32x2 e = P8c.b, g = W;
        put(0, f32x2{1.f, 0.f});
#pragma unroll
        for (int hi = 0; hi <= 16; ++hi) {
            if (hi < 15) { put(1 + hi, e); if (hi < 14) e = cmul(e, P8c); }
            if (hi < 2) {
                f32x2 t = g;
#pragma unroll
                for (int lo = 0; lo < 8; ++lo) { put(16 + hi * 8 + lo, t); if (lo < 7) t = cmul(t, P1); }
            } else if (hi < 4) {
                f32x2 t = g;
#pragma unroll
                for (int lo = 0; lo < 4; ++lo) { put(32 + (hi - 2) * 4 + lo, t); if (lo < 3) t = cmul(t, P2); }
            } else if (hi < 8) {
                put(40 + (hi - 4) * 2, g);
                put(41 + (hi - 4) * 2, cmul(g, P4));
            } else {
                put(48 + (hi - 8), g);
            }
            if (hi < 16) g = cmul(g, Q);
        }
    }
    STAMP(1, 2);
    __syncthreads();
    const int m = lane & 15, kq = lane >> 4;
    {
        const u32 tb = lb + ch * TAB_CH + m * TAB_STRIDE + kq * 16;
        const u32 kl = lb + C_KL + ch * 4160 + 16 + m * 16;
        f32x4 acc0 = {0.f, 0.f, 0.f, 0.f}, acc1 = {0.f, 0.f, 0.f, 0.f};
        u32x4 e[8], a[8];
#pragma unroll
        for (int ks = 0; ks < 8; ++ks) {
            e[ks] = *LDSP(u32x4, tb + ks * 64);
            a[ks] = *LDSP(u32x4, tb + (16 + hf * 16) * TAB_STRIDE + ks * 64);
        }
        if (hf == 0) {
            u32x4 a2[8];
#pragma unroll
            for (int ks = 0; ks < 8; ++ks) a2[ks] = *LDSP(u32x4, tb + 48 * TAB_STRIDE + ks * 64);
            SB();
#pragma unroll
            for (int ks = 0; ks < 8; ++ks) acc0 = __builtin_amdgcn_mfma_f32_16x16x32_bf16(asbf(a[ks]), asbf(e[ks]), acc0, 0, 0, 0);
#pragma unroll
            for (int ks = 0; ks < 8; ++ks) acc1 = __builtin_amdgcn_mfma_f32_16x16x32_bf16(asbf(a2[ks]), asbf(e[ks]), acc1, 0, 0, 0);
            *LDSP(u32x2, kl + (kq >> 1) * 256 + (kq & 1) * 8) = u32x2{pack2(acc0[0], acc0[1]), pack2(acc0[2], acc0[3])};
            const u32 p0 = pack2(acc1[0], acc1[1]), p1 = pack2(acc1[2], acc1[3]);
            if (kq < 2) {
                const u32 o = kl + 2048 + kq * 1024;
                *LDSP(u16, o) = (u16)p0; *LDSP(u16, o + 256) = (u16)(p0 >> 16);
                *LDSP(u16, o + 512) = (u16)p1; *LDSP(u16, o + 768) = (u16)(p1 >> 16);
            } else if (lane == 32) {
                *LDSP(u16, kl + 4096) = (u16)p0;
            }
        } else {
            SB();
#pragma unroll
            for (int ks = 0; ks < 8; ++ks) acc0 = __builtin_amdgcn_mfma_f32_16x16x32_bf16(asbf(a[ks]), asbf(e[ks]), acc0, 0, 0, 0);
            const u32 p0 = pack2(acc0[0], acc0[1]), p1 = pack2(acc0[2], acc0[3]);
            const bool s1 = kq < 2;
            const u32 o = kl + (s1 ? 512 + kq * 256 : kq * 512);
            *LDSP(u16, o) = (u16)p0;
            *LDSP(u16, o + (s1 ? 4 : 8)) = (u16)(p0 >> 16);
            *LDSP(u16, o + (s1 ? 8 : 256)) = (u16)p1;
            *LDSP(u16, o + (s1 ? 12 : 264)) = (u16)(p1 >> 16);
        }
    }
    STAMP(1, 3);
    f32x2 base0[4], base1[4];
#pragma unroll
    for (int i = 0; i < 2; ++i) {
        u32x2 h0, h1;
#pragma unroll
        for (int k = 0; k < 2; ++k) {
            const u32x4 v = zx[2 * i + k];
            const u32 a = pack2(fmaf(bflo(v.x), ca0, sa0), fmaf(bflo(v.z), ca0, sa0));
            const u32 b = pack2(fmaf(bflo(v.y), ca1, sa1), fmaf(bflo(v.w), ca1, sa1));
            h0[k] = a; h1[k] = b;
            base0[2 * i + k] = f32x2{fmaf(bflo(a), gd0, bfhi(v.x)), fmaf(bfhi(a), gd0, bfhi(v.z))};
            base1[2 * i + k] = f32x2{fmaf(bflo(b), gd1, bfhi(v.y)), fmaf(bfhi(b), gd1, bfhi(v.w))};
        }
        const u32 o = lb + C_H + 512 + (i * 256 + tid) * 8;
        *LDSP(u32x2, o) = h0;
        *LDSP(u32x2, o + C_H_CH) = h1;
    }
    STAMP(1, 4);
    __syncthreads();
    {
        const int w = 255 - tid;
        const bool s0 = w < 32, s1 = w < 64, s3 = w >= 128;
        const u32 sel0 = s0 ? 0x01000302u : s1 ? 0x01000504u : 0x05040504u;
        const u32 sel1 = s0 ? 0x01000302u : !s3 ? 0x01000100u : (w & 1) ? 0x05040504u : 0x01000504u;
        const u32 sel2 = s0 ? 0x01000302u : !s3 ? 0x01000504u : 0x01000100u;
        const u32 sel3 = s0 ? 0x01000302u : 0x01000100u;
#pragma unroll
        for (int c = 0; c < 2; ++c) {
            const u32 src = lb + C_KL + c * 4160 + 16 + w * 16;
            const u32x4 r = *LDSP(u32x4, src);
            const u32 e = *LDSP(u16, src + 16), f = *LDSP(u16, src - 2);
            u32x4 c0, c1;
            c0.x = __builtin_amdgcn_perm(e, r.w, sel0);
            c0.y = __builtin_amdgcn_perm(e, s3 ? r.x : r.z, sel1);
            c0.z = __builtin_amdgcn_perm(r.z, s1 ? r.y : r.x, sel2);
            c0.w = __builtin_amdgcn_perm(r.x, r.x, sel3);
            c1.x = __builtin_amdgcn_alignbit(c0.y, c0.x, 16);
            c1.y = __builtin_amdgcn_alignbit(c0.z, c0.y, 16);
            c1.z = __builtin_amdgcn_alignbit(c0.w, c0.z, 16);
            c1.w = __builtin_amdgcn_alignbit(w <= 32 ? f : r.x, c0.w, 16);
            const u32 dst = lb + C_KR + c * (2 * KR_COPY * 2) + tid * 16;
            *LDSP(u32x4, dst) = c0;
            *LDSP(u32x4, dst + KR_COPY * 2) = c1;
        }
        if (tid < 16) *LDSP(u32x4, lb + C_KR + (tid >> 2) * (KR_COPY * 2) + 4096 + (tid & 3) * 16) = u32x4{0u, 0u, 0u, 0u};
    }
    __syncthreads();
    STAMP(1, 5);

    u32 kab[4];
#pragma unroll
    for (int p = 0; p < 4; ++p) {
        kab[p] = lb + C_KR + ch * (2 * KR_COPY * 2) + ((m & 1) ? 0 : KR_COPY * 2) + 4092 - 2 * (m & ~1) + 16 * kq - 32 * (2 * hf + 1) - 1024 * p - 896;
        asm volatile("" : "+v"(kab[p]));
    }
    u32 hb = lb + C_H + ch * C_H_CH + 512 + 32 * m + 16 * kq - 32 * (2 * hf + 1) - 384;
    asm volatile("" : "+v"(hb));
    u32x4 A[2][8], B[2][8];
    auto ld = [&](int e, int n) {
        if (n == 0 || n > 8) {
            const int i = n == 0 ? 0 : n - 8;
            B[e & 1][i] = *LDSP(u32x4, hb + 512 * i + 384 - 128 * e);
        } else {
            const int i = n - 1;
            const u32 o = kab[i >> 1] + ((i & 1) ? 384 : 896) - 128 * e;
            A[e & 1][i] = u32x4{*LDSP(u32, o), *LDSP(u32, o + 4), *LDSP(u32, o + 8), *LDSP(u32, o + 12)};
        }
    };
    f32x4 acc[8];
#pragma unroll
    for (int c = 0; c < 8; ++c) acc[c] = f32x4{0.f, 0.f, 0.f, 0.f};
#pragma unroll
    for (int n = 0; n < 16; ++n) ld(0, n);
    SB();
#pragma unroll
    for (int e = 0; e < 4; ++e) {
        int cnt = 0;
#pragma unroll
        for (int cp = 0; cp < 8; ++cp)
#pragma unroll
            for (int c = cp; c < 8; ++c) {
                acc[c] = __builtin_amdgcn_mfma_f32_16x16x32_bf16(asbf(A[e & 1][c - cp]), asbf(B[e & 1][cp]), acc[c], 0, 0, 0);
                if (e < 3 && (cnt & 1) == 0 && (cnt >> 1) < 16) ld(e + 1, cnt >> 1);
                ++cnt;
                SB();
            }
    }
    STAMP(1, 6);
    {
        const int s = 4 * m + kq;
        const u32 pb = lb + C_P + wave * 8192 + (s ^ ((s >> 3) & 3)) * 16;
#pragma unroll
        for (int c = 0; c < 8; ++c) *LDSP(f32x4, pb + c * 1024) = acc[c];
    }
    __syncthreads();
    STAMP(1, 7);
    const auto rs = mkrsrc(ws + WS_X2, 2u << 20);
#pragma unroll
    for (int i = 0; i < 2; ++i) {
        const int tq = i * 256 + tid;
        const u32 pb = lb + C_P + (tq ^ ((tq >> 3) & 3)) * 16;
        const f32x4 y0 = *LDSP(f32x4, pb) + *LDSP(f32x4, pb + 8192);
        const f32x4 y1 = *LDSP(f32x4, pb + 16384) + *LDSP(f32x4, pb + 24576);
        u32x4 o;
        o.x = pack2(fmaf(ga0, y0.x, base0[2 * i].x), fmaf(ga1, y1.x, base1[2 * i].x));
        o.y = pack2(fmaf(ga0, y0.y, base0[2 * i].y), fmaf(ga1, y1.y, base1[2 * i].y));
        o.z = pack2(fmaf(ga0, y0.z, base0[2 * i + 1].x), fmaf(ga1, y1.z, base1[2 * i + 1].x));
        o.w = pack2(fmaf(ga0, y0.w, base0[2 * i + 1].y), fmaf(ga1, y1.w, base1[2 * i + 1].y));
        __builtin_amdgcn_raw_buffer_store_b128(o, rs, (tq >> 3) * 32768 + pair * 128 + (tq & 7) * 16, 0, AUX_SC1);
    }
    STAMP(1, 8);
    STAMPC(1, 15);
}

DEVI void k2_wprep(int b, const u16* __restrict__ wg, const u16* __restrict__ wu, const u16* __restrict__ wd,
                   char* __restrict__ ws, u32 lb) {
    const int tid = threadIdx.x;
    const int mt = b & 1, fgrp = (b >> 1) & 31, dq = b >> 6;
    STAMP(1, 0);
    const u16* w = mt ? wu : wg;
    const float* mp = (const float*)(ws + WS_MOD);
    const float mv = modsum(mp, (tid < 128 ? 4 * 512 : 3 * 512 - 128) + dq * 128 + tid);
    float gbv = 0.f;
    if (b < 2) gbv = modsum(mp, 5 * 512 + b * 256 + tid);
    const int fp0 = tid % 28, kh0 = tid / 28, fp1 = (tid + 256) % 28, kh1 = tid < 192 ? (tid + 256) / 28 : 0;
    u32 v[2][8], vd[8];
#pragma unroll
    for (int e = 0; e < 8; ++e) {
        v[0][e] = *(const u32*)(w + (size_t)(dq * 128 + kh0 * 8 + e) * FF + fgrp * 56 + fp0 * 2);
        v[1][e] = *(const u32*)(w + (size_t)(dq * 128 + kh1 * 8 + e) * FF + fgrp * 56 + fp1 * 2);
    }
    const int dt = b & 15, kg = b >> 4;
    const int dp = tid & 15, hh = (tid >> 4) & 1, ksd = kg * 7 + (tid < 224 ? tid >> 5 : 0);
#pragma unroll
    for (int e = 0; e < 8; ++e)
        vd[e] = *(const u32*)(wd + (size_t)(ksd * 16 + (e >> 2) * 8 + hh * 4 + (e & 3)) * DM + dt * 32 + dp * 2);
    *LDSP(float, lb + tid * 4) = tid < 128 ? rbf(1.f + mv) : mv;
    if (b < 2) ((float*)(ws + WS_GB))[b * 256 + tid] = gbv;
    __syncthreads();
    STAMP(1, 1);
    const auto rsw = mkrsrc(ws + WS_WGU, 16 * 7 * 32 * 1024);
#pragma unroll
    for (int r = 0; r < 2; ++r) {
        if (r == 0 || tid < 192) {
            const int fp = r ? fp1 : fp0, kh = r ? kh1 : kh0;
            const f32x4 sA = *LDSP(f32x4, lb + kh * 32), sB = *LDSP(f32x4, lb + kh * 32 + 16);
            const f32x4 bA = *LDSP(f32x4, lb + 512 + kh * 32), bB = *LDSP(f32x4, lb + 512 + kh * 32 + 16);
            u32x4 o0, o1;
            float u0 = 0.f, u1 = 0.f;
            f32x2 vv = {0.f, 0.f};
#pragma unroll
            for (int e = 0; e < 8; e += 2) {
                const float s0 = e < 4 ? sA[e] : sB[e - 4], s1 = e < 4 ? sA[e + 1] : sB[e - 3];
                const float b0 = e < 4 ? bA[e] : bB[e - 4], b1 = e < 4 ? bA[e + 1] : bB[e - 3];
                const f32x2 r0 = {bflo(v[r][e]), bfhi(v[r][e])}, r1 = {bflo(v[r][e + 1]), bfhi(v[r][e + 1])};
                const f32x2 m0 = r0 * s0, m1 = r1 * s1;
                const u32 p0 = pack2(m0.x, m1.x), p1 = pack2(m0.y, m1.y);
                o0[e >> 1] = p0; o1[e >> 1] = p1;
                u0 = dot2(p0, ONES, u0); u1 = dot2(p1, ONES, u1);
                vv = __builtin_elementwise_fma(r1, f32x2{b1, b1}, __builtin_elementwise_fma(r0, f32x2{b0, b0}, vv));
            }
            *LDSP(f32x4, lb + 1024 + (kh * 28 + fp) * 16) = f32x4{u0, u1, vv.x, vv.y};
            const int fl = (fgrp & 1) * 56 + fp * 2;
            const u32 o = ((((fgrp >> 1) * 7 + (fl >> 4)) * 32 + dq * 8 + (kh >> 1)) * 64 + (kh & 1) * 32 + (fl & 15) + 16 * mt) * 16;
            __builtin_amdgcn_raw_buffer_store_b128(o0, rsw, o, 0, AUX_SC1);
            __builtin_amdgcn_raw_buffer_store_b128(o1, rsw, o + 16, 0, AUX_SC1);
        }
    }
    if (tid < 224) {
        u32x4 o0, o1;
#pragma unroll
        for (int e = 0; e < 8; e += 2) { o0[e >> 1] = lo_pair(vd[e], vd[e + 1]); o1[e >> 1] = hi_pair(vd[e], vd[e + 1]); }
        const auto rsd = mkrsrc(ws + WS_WD, 16 * 112 * 1024);
        const u32 o = ((dt * 112 + ksd) * 64 + hh * 32 + dp * 2) * 16;
        __builtin_amdgcn_raw_buffer_store_b128(o0, rsd, o, 0, AUX_SC1);
        __builtin_amdgcn_raw_buffer_store_b128(o1, rsd, o + 16, 0, AUX_SC1);
    }
    STAMP(1, 2);
    __syncthreads();
    if (tid < 112) {
        const int fp = tid >> 2, comp = tid & 3;
        float sum = 0.f;
#pragma unroll
        for (int kh = 0; kh < 16; ++kh) sum += *LDSP(float, lb + 1024 + (kh * 28) * 16 + tid * 4);
        const int fl = (fgrp & 1) * 56 + fp * 2 + (comp & 1);
        const int col = (fgrp >> 1) * 224 + (fl >> 4) * 32 + (fl & 15) + 16 * mt;
        ((float*)(ws + WS_UV))[(dq * 2 + (comp >> 1)) * 3584 + col] = sum;
    }
    STAMP(1, 8);
}

__global__ __launch_bounds__(256, 2) void k2_conv_prep(const u16* __restrict__ Are, const u16* __restrict__ Aim,
                                                       const u16* __restrict__ Bre, const u16* __restrict__ Bim,
                                                       const u16* __restrict__ Cre, const u16* __restrict__ Cim,
                                                       const u16* __restrict__ logdt, const u16* __restrict__ dskip,
                                                       const u16* __restrict__ wg, const u16* __restrict__ wu,
                                                       const u16* __restrict__ wd, char* __restrict__ ws) {
    extern __shared__ __attribute__((aligned(16))) char lds[];
    const u32 lb = (u32)(size_t)lds;
    const int bid = blockIdx.x;
    if (bid < 256) k2_conv((bid & 7) * 32 + (bid >> 3), Are, Aim, Bre, Bim, Cre, Cim, logdt, dskip, ws, lb);
    else k2_wprep(bid - 256, wg, wu, wd, ws, lb);
}

constexpr int G_UV = 4 * 32768;
constexpr int K3_LDS = G_UV + 2 * 224 * 4;

DEVI void glds_tile(const char* gbase, u32 voff, u32 lds_dst) {
    u32 keep;
    asm volatile("s_mov_b32 %0, m0\n\ts_mov_b32 m0, %3\n\ts_nop 0\n\t"
                 "global_load_lds_dwordx4 %1, %2 offset:-4096\n\t"
                 "global_load_lds_dwordx4 %1, %2 offset:-3072\n\t"
                 "global_load_lds_dwordx4 %1, %2 offset:-2048\n\t"
                 "global_load_lds_dwordx4 %1, %2 offset:-1024\n\t"
                 "global_load_lds_dwordx4 %1, %2\n\t"
                 "global_load_lds_dwordx4 %1, %2 offset:1024\n\t"
                 "global_load_lds_dwordx4 %1, %2 offset:2048\n\t"
                 "global_load_lds_dwordx4 %1, %2 offset:3072\n\t"
                 "s_mov_b32 m0, %0"
                 : "=&s"(keep) : "v"(voff), "s"(gbase), "s"(lds_dst) : "memory");
}

template <int N> DEVI void wait_vm() { asm volatile("s_waitcnt vmcnt(%0)" ::"i"(N) : "memory"); }
DEVI void raw_barrier() {
    asm volatile("s_waitcnt lgkmcnt(0)" ::: "memory");
    __builtin_amdgcn_s_barrier();
    asm volatile("" ::: "memory");
}

__global__ __launch_bounds__(256, 1) void k3_gemm1(char* ws) {
    extern __shared__ __attribute__((aligned(16))) char lds[];
    const int bid = blockIdx.x, xcd = bid & 7, rk = bid >> 3;
    const int mt = (xcd >> 2) * 8 + (rk >> 2), sl = (xcd & 3) * 4 + (rk & 3);
    const int tid = threadIdx.x, lane = tid & 63;
    const int wave = __builtin_amdgcn_readfirstlane(tid >> 6);
    const u32 lbase = (u32)(size_t)lds;
    const int tok = lane & 31, h = lane >> 5;
    STAMPDECL;

    STAMP(2, 0);
    float uvreg[2];
    {
        const float* uv = (const float*)(ws + WS_UV) + sl * 224;
#pragma unroll
        for (int r = 0; r < 2; ++r) {
            const int i = tid < 224 ? tid : 0;
            uvreg[r] = (uv[r * 3584 + i] + uv[(2 + r) * 3584 + i]) + (uv[(4 + r) * 3584 + i] + uv[(6 + r) * 3584 + i]);
        }
    }
    const char* wsrc = ws + WS_WGU + (size_t)sl * 7 * 32768 + wave * 8192 + 4096;
    const u32 wl = lbase + wave * 8192 + 4096;
    const u32 voff = lane * 16;
    if (tid < 224) {
        const float sc = (tid & 16) ? 1.f : -1.44269504088896341f;
        ((float*)(lds + G_UV))[tid] = uvreg[0] * sc;
        ((float*)(lds + G_UV))[224 + tid] = uvreg[1] * sc;
    }
    STAMP(2, 1);
    glds_tile(wsrc, voff, wl);

    u32x4 X[32];
    const char* xbase = ws + WS_X2 + (size_t)(mt * 4 + wave) * 32768;
    const u32 xo = tok * 4 + h * 512;
    auto load_x = [&](int ks) {
        const char* p = xbase + (size_t)ks * 1024;
        const u32 o1 = xo + 128, o2 = xo + 256, o3 = xo + 384;
        u32 a, b, c, d;
        asm volatile("global_load_dword %0, %4, %8\n\tglobal_load_dword %1, %5, %8\n\t"
                     "global_load_dword %2, %6, %8\n\tglobal_load_dword %3, %7, %8"
                     : "=&v"(a), "=&v"(b), "=&v"(c), "=&v"(d)
                     : "v"(xo), "v"(o1), "v"(o2), "v"(o3), "s"(p) : "memory");
        X[ks] = u32x4{a, b, c, d};
    };
#pragma unroll
    for (int ks = 0; ks < 12; ++ks) load_x(ks);

    const char* abase = lds + lane * 16;
    f32x16 acc[2];
    float sum = 0.f, ssq = 0.f;
    const u32 ones = 0x3f803f80u;
#pragma unroll
    for (int i = 0; i < 16; ++i) acc[0][i] = 0.f;
#pragma unroll
    for (int g = 0; g < 8; ++g) {
        if (g < 6) wait_vm<32>(); else if (g == 6) wait_vm<16 + 24>(); else wait_vm<24>();
#pragma unroll
        for (int k = 0; k < 4; ++k) asm volatile("" : "+v"(X[g * 4 + k]));
        if (g == 0) { raw_barrier(); STAMPX(2, 2); }
        if (g < 5) {
#pragma unroll
            for (int k = 0; k < 4; ++k) load_x(12 + g * 4 + k);
        }
        if (g == 5) {
            glds_tile(wsrc + 32768, voff, wl + 32768);
            glds_tile(wsrc + 2 * 32768, voff, wl + 2 * 32768);
            glds_tile(wsrc + 3 * 32768, voff, wl + 3 * 32768);
        }
#pragma unroll
        for (int k = 0; k < 4; ++k) {
            const int ks = g * 4 + k;
            const u32x4 a = *(const u32x4*)(abase + ks * 1024);
            acc[0] = __builtin_amdgcn_mfma_f32_32x32x16_bf16(asbf(a), asbf(X[ks]), acc[0], 0, 0, 0);
#pragma unroll
            for (int q = 0; q < 4; ++q) {
                const u32 xq = X[ks][q];
                const bf16x2 xv = __builtin_bit_cast(bf16x2, xq);
                sum = __builtin_amdgcn_fdot2_f32_bf16(xv, __builtin_bit_cast(bf16x2, ones), sum, false);
                ssq = __builtin_amdgcn_fdot2_f32_bf16(xv, xv, ssq, false);
            }
        }
    }
    STAMPX(2, 3);
    sum += __shfl_xor(sum, 32);
    ssq += __shfl_xor(ssq, 32);
    const float mean = sum * (1.f / DM);
    const float var = fmaxf(ssq * (1.f / DM) - mean * mean, 0.f);
    const float rinv = 1.f / rbf(__builtin_sqrtf(rbf(rbf(var) + 1e-5f)));
    const float nmr = -rbf(mean) * rinv;

    char* hdst = ws + WS_HH + ((size_t)(mt * 4 + wave) * 112 + sl * 7) * 1024 + lane * 16;
    auto epilogue = [&](int j, const f32x16& a) {
        const float* up = (const float*)(lds + G_UV) + j * 32 + 4 * h;
        u32x4 o;
#pragma unroll
        for (int gq = 0; gq < 2; ++gq) {
            const f32x4 ug = *(const f32x4*)(up + gq * 8), vg = *(const f32x4*)(up + 224 + gq * 8);
            const f32x4 uu = *(const f32x4*)(up + 16 + gq * 8), vu = *(const f32x4*)(up + 224 + 16 + gq * 8);
            float r[4];
#pragma unroll
            for (int i = 0; i < 4; ++i) {
                const float gl = fmaf(a[gq * 4 + i], rinv * -1.44269504088896341f, fmaf(nmr, ug[i], vg[i]));
                const float ul = fmaf(a[8 + gq * 4 + i], rinv, fmaf(nmr, uu[i], vu[i]));
                r[i] = ul * __builtin_amdgcn_rcpf(1.f + __builtin_amdgcn_exp2f(gl));
            }
            o[gq * 2] = pack2(r[0], r[1]);
            o[gq * 2 + 1] = pack2(r[2], r[3]);
        }
        *(u32x4*)(hdst + j * 1024) = o;
    };
#pragma unroll
    for (int j = 1; j < 7; ++j) {
        if (j == 1) wait_vm<16>(); else if (j == 2) wait_vm<17>(); else if (j == 3) wait_vm<18>();
        else if (j == 4) wait_vm<19>(); else if (j == 5) wait_vm<11>(); else wait_vm<3>();
        raw_barrier();
        if (j <= 3) glds_tile(wsrc + (j + 3) * 32768, voff, wl + ((j + 3) & 3) * 32768);
        f32x16& cur = acc[j & 1];
        epilogue(j - 1, acc[(j - 1) & 1]);
#pragma unroll
        for (int i = 0; i < 16; ++i) cur[i] = 0.f;
#pragma unroll
        for (int ks = 0; ks < 32; ++ks) {
            const u32x4 a = *(const u32x4*)(abase + (j & 3) * 32768 + ks * 1024);
            cur = __builtin_amdgcn_mfma_f32_32x32x16_bf16(asbf(a), asbf(X[ks]), cur, 0, 0, 0);
        }
        asm volatile("" ::: "memory");
        STAMPX(2, 3 + j);
    }
    epilogue(6, acc[0]);
    STAMPX(2, 10);
    STAMPFLUSH(2, 2, 10);
}

constexpr int K4_LDS = 4 * 3 * 4096;

__global__ __launch_bounds__(256, 1) void k4_gemm2(char* ws, u16* out) {
    extern __shared__ __attribute__((aligned(16))) char lds[];
    const int bid = blockIdx.x, xcd = bid & 7, rk = bid >> 3;
    const int t64 = xcd * 4 + (rk >> 3), c64 = rk & 7;
    const int tid = threadIdx.x, lane = tid & 63;
    const int wave = __builtin_amdgcn_readfirstlane(tid >> 6);
    const char* pa0 = ws + WS_WD + ((size_t)(c64 * 2) * 112 + wave * 28) * 1024 + lane * 16;
    const char* pa1 = pa0 + 112 * 1024;
    const char* pb0 = ws + WS_HH + ((size_t)(t64 * 2) * 112 + wave * 28) * 1024 + lane * 16;
    const char* pb1 = pb0 + 112 * 1024;
    STAMP(3, 0);
    constexpr int PD = 14;
    u32x4 A0[28], A1[28], B0[28], B1[28];
#pragma unroll
    for (int s = 0; s < PD; ++s) {
        A0[s] = *(const u32x4*)(pa0 + s * 1024); B0[s] = *(const u32x4*)(pb0 + s * 1024);
        A1[s] = *(const u32x4*)(pa1 + s * 1024); B1[s] = *(const u32x4*)(pb1 + s * 1024);
    }
    __builtin_amdgcn_sched_barrier(0);
    f32x16 acc[4];
#pragma unroll
    for (int t = 0; t < 4; ++t)
#pragma unroll
        for (int i = 0; i < 16; ++i) acc[t][i] = 0.f;
#pragma unroll
    for (int s = 0; s < 28; ++s) {
        acc[0] = __builtin_amdgcn_mfma_f32_32x32x16_bf16(asbf(A0[s]), asbf(B0[s]), acc[0], 0, 0, 0);
        acc[2] = __builtin_amdgcn_mfma_f32_32x32x16_bf16(asbf(A1[s]), asbf(B0[s]), acc[2], 0, 0, 0);
        acc[1] = __builtin_amdgcn_mfma_f32_32x32x16_bf16(asbf(A0[s]), asbf(B1[s]), acc[1], 0, 0, 0);
        acc[3] = __builtin_amdgcn_mfma_f32_32x32x16_bf16(asbf(A1[s]), asbf(B1[s]), acc[3], 0, 0, 0);
        if (s + PD < 28) {
            const int n = s + PD;
            A0[n] = *(const u32x4*)(pa0 + n * 1024); B0[n] = *(const u32x4*)(pb0 + n * 1024);
            A1[n] = *(const u32x4*)(pa1 + n * 1024); B1[n] = *(const u32x4*)(pb1 + n * 1024);
        }
        __builtin_amdgcn_sched_barrier(0);
    }
    STAMP(3, 1);
    const int dtl = wave >> 1, ttl = wave & 1;
    const int tok = t64 * 64 + ttl * 32 + (lane & 31), h = lane >> 5;
    const int dbase = c64 * 64 + dtl * 32 + 4 * h;
    const u32* x2 = (const u32*)(ws + WS_X2);
    const float* mp = (const float*)(ws + WS_MOD) + 5 * 512;
    u32 xr[8];
    f32x4 gb[4];
#pragma unroll
    for (int g = 0; g < 4; ++g) {
        xr[2 * g] = x2[(size_t)(tok >> 5) * 8192 + ((dbase + 8 * g) >> 1) * 32 + (tok & 31)];
        xr[2 * g + 1] = x2[(size_t)(tok >> 5) * 8192 + (((dbase + 8 * g) >> 1) + 1) * 32 + (tok & 31)];
        const float* m4 = mp + dbase + 8 * g;
        gb[g] = (*(const f32x4*)m4 + *(const f32x4*)(m4 + 3072)) + (*(const f32x4*)(m4 + 2 * 3072) + *(const f32x4*)(m4 + 3 * 3072));
    }
#pragma unroll
    for (int t = 0; t < 4; ++t) {
        if (t != wave) {
            const int slot = t * 3 + (wave > t ? wave - 1 : wave);
#pragma unroll
            for (int g = 0; g < 4; ++g)
                *(f32x4*)(lds + slot * 4096 + g * 1024 + lane * 16) = f32x4{acc[t][4 * g], acc[t][4 * g + 1], acc[t][4 * g + 2], acc[t][4 * g + 3]};
        }
    }
    f32x16 mine = wave == 0 ? acc[0] : wave == 1 ? acc[1] : wave == 2 ? acc[2] : acc[3];
    __syncthreads();
    STAMP(3, 2);
    u16* orow = out + (size_t)tok * DM + dbase;
#pragma unroll
    for (int g = 0; g < 4; ++g) {
        const char* pp = lds + wave * 3 * 4096 + g * 1024 + lane * 16;
        const f32x4 s3 = *(const f32x4*)pp + *(const f32x4*)(pp + 4096) + *(const f32x4*)(pp + 8192);
        u32x2 o;
        o.x = pack2(bflo(xr[2 * g]) + rbf(gb[g].x) * (mine[4 * g] + s3.x), bfhi(xr[2 * g]) + rbf(gb[g].y) * (mine[4 * g + 1] + s3.y));
        o.y = pack2(bflo(xr[2 * g + 1]) + rbf(gb[g].z) * (mine[4 * g + 2] + s3.z), bfhi(xr[2 * g + 1]) + rbf(gb[g].w) * (mine[4 * g + 3] + s3.w));
        *(u32x2*)(orow + 8 * g) = o;
    }
    STAMP(3, 3);
}

extern "C" void kernel_launch(void* const* d_in, const int* in_sizes, int n_in, void* d_out, int out_size,
                              void* d_ws, size_t ws_size, hipStream_t stream) {
    const u16* x = (const u16*)d_in[0];
    char* ws = (char*)d_ws;
    (void)hipFuncSetAttribute((const void*)k2_conv_prep, hipFuncAttributeMaxDynamicSharedMemorySize, K2_LDS);
    (void)hipFuncSetAttribute((const void*)k3_gemm1, hipFuncAttributeMaxDynamicSharedMemorySize, K3_LDS);
    (void)hipFuncSetAttribute((const void*)k4_gemm2, hipFuncAttributeMaxDynamicSharedMemorySize, K4_LDS);
    const u16 *Are = (const u16*)d_in[1], *Aim = (const u16*)d_in[2], *Bre = (const u16*)d_in[3];
    const u16 *Bim = (const u16*)d_in[4], *Cre = (const u16*)d_in[5], *Cim = (const u16*)d_in[6];
    k1_prep<<<256, 512, 0, stream>>>(x, Are, Aim, Bre, Bim, Cre, Cim, (const u16*)d_in[13], (const u16*)d_in[14], ws);
    k2_conv_prep<<<512, 256, K2_LDS, stream>>>(Are, Aim, Bre, Bim, Cre, Cim, (const u16*)d_in[7], (const u16*)d_in[8],
                                               (const u16*)d_in[10], (const u16*)d_in[11], (const u16*)d_in[12], ws);
    k3_gemm1<<<256, 256, K3_LDS, stream>>>(ws);
    k4_gemm2<<<256, 256, K4_LDS, stream>>>(ws, (u16*)d_out);
}
```

```cpp
#include <hip/hip_runtime.h>
#include <hip/hip_bf16.h>

typedef unsigned short u16;
typedef unsigned u32;
typedef u32 u32x2 __attribute__((ext_vector_type(2)));
typedef u32 u32x4 __attribute__((ext_vector_type(4)));
typedef float f32x2 __attribute__((ext_vector_type(2)));
typedef float f32x4 __attribute__((ext_vector_type(4)));
typedef float f32x16 __attribute__((ext_vector_type(16)));
typedef __bf16 bf16x2 __attribute__((ext_vector_type(2)));
typedef __bf16 bf16x8 __attribute__((ext_vector_type(8)));

#define DEVI __device__ __forceinline__
#define SB() __builtin_amdgcn_sched_barrier(0)
#pragma clang diagnostic ignored "-Wint-to-pointer-cast"

constexpr int SEQ = 2048, DM = 512, NS = 128, FF = 1792;
constexpr int KR_COPY = 2080;

constexpr size_t WS_ZX  = 0;
constexpr size_t WS_FL  = WS_ZX + (size_t)512 * 4096;
constexpr size_t WS_GB  = WS_FL + 1024 * 128;
constexpr size_t WS_ST_ = WS_GB + 4096;
constexpr size_t WS_MOD = WS_ST_ + 2048 * 8;
constexpr size_t WS_X2  = WS_MOD + 4 * 3072 * 8;
constexpr size_t WS_WGU = WS_X2 + (size_t)256 * 2048 * 4;
constexpr size_t WS_UV  = WS_WGU + (size_t)16 * 7 * 32 * 1024;
constexpr size_t WS_WD  = WS_UV + 4 * 2 * 3584 * 4;
constexpr size_t WS_HH  = WS_WD + (size_t)16 * 112 * 1024;
constexpr size_t WS_FW  = WS_HH + (size_t)64 * 112 * 1024;
constexpr size_t WS_EP  = WS_FW + 4096;
constexpr size_t WS_END = WS_EP + 128;

#define STAMP(k, i)
#define STAMPC(k, i)
#define STAMPX(k, i)
#define STAMPFLUSH(k, a, b)
#define STAMPDECL

DEVI float bflo(u32 v) { return __builtin_bit_cast(float, v << 16); }
DEVI float bfhi(u32 v) { return __builtin_bit_cast(float, v & 0xffff0000u); }
DEVI float bf2f(u16 v) { return __builtin_bit_cast(float, (u32)v << 16); }
DEVI u32 pack2(float lo, float hi) {
    f32x2 f = {lo, hi};
    return __builtin_bit_cast(u32, __builtin_convertvector(f, bf16x2));
}
DEVI float rbf(float x) { return bflo(pack2(x, x)); }
DEVI bf16x8 asbf(u32x4 v) { return __builtin_bit_cast(bf16x8, v); }

constexpr int K2_LDS = 157696;

#define K2_CLOBBERS \
    "v1","v2","v3","v4","v5","v6","v7","v8","v9","v10","v11","v12","v13","v14","v15","v16","v17","v18","v19","v20","v21","v22","v23","v24","v25","v26", \
    "v27","v28","v29","v30","v31","v32","v33","v34","v35","v36","v37","v38","v39","v40","v41","v42","v43","v44","v45","v46","v47","v48","v49","v50","v51", \
    "v52","v53","v54","v55","v56","v57","v58","v59","v60","v61","v62","v63","v64","v65","v66","v67","v68","v69","v70","v71","v72","v73","v74","v75","v76", \
    "v77","v78","v79","v80","v81","v82","v83","v84","v85","v86","v87","v88","v89","v90","v91","v92","v93","v94","v95","v96","v97","v98","v99","v100", \
    "v101","v102","v103","v104","v105","v106","v107","v108","v109","v110","v111","v112","v113","v114","v115","v116","v117","v118","v119","v120","v121", \
    "v122","v123","v124","v125","v126","v127","v128","v129","v130","v131","v132","v133","v134","v135","v136","v137","v138","v139","v140","v141","v142", \
    "v143","v144","v145","v146","v147","v148","v149","v150","v151","v152","v153","v154","v155","v156","v157","v158","v159","v160","v161","v162","v163", \
    "v164","v165","v166","v167","v168","v169","v170","v171","v172","v173","v174","v175","v176","v177","v178","v179","v180","v181","v182","v183","v184", \
    "v185","v186","v187","v188","v189","v190","v191","v192","v193","v194","v195","v196","v197","v198","v199","v200","v201","v202","v203","v204","v205", \
    "v206","v207","v208","v209","v210","v211","v212","v213","v214","v215","v216","v217","v218","v219","v220","v221","v222","v223","v224","v225","v226", \
    "v227","v228","v229","v230","v231","v232","v233","v234","v235","v236","v237","v238","v239","v240","v241","v242","v243","v244","v245","v246","v247", \
    "v248","v249","v250","v251","v252","v253","v254","v255","a0","a1","a2","a3","a4","a5","a6","a7","a8","a9","a10","a11","a12","a13","a14","a15","a16", \
    "a17","a18","a19","a20","a21","a22","a23","a24","a25","a26","a27","a28","a29","a30","a31","a32","a33","a34","a35","a36","a37","a38","a39","a40","a41", \
    "a42","a43","a44","a45","a46","a47","a48","a49","a50","a51","a52","a53","a54","a55","a56","a57","a58","a59","a60","a61","a62","a63","a64","a65","a66", \
    "a67","a68","a69","a70","a71","a72","a73","a74","a75","a76","a77","a78","a79","a80","a81","a82","a83","a84","a85","a86","a87","a88","a89","a90","a91", \
    "a92","a93","a94","a95","a96","a97","a98","a99","a100","a101","a102","a103","a104","a105","a106","a107","a108","a109","a110","a111","a112","a113", \
    "a114","a115","a116","a117","a118","a119","a120","a121","a122","a123","a124","a125","a126","a127","a128","a129","a130","a131","a132","a133","a134", \
    "a135","a136","a137","a138","a139","a140","a141","a142","a143","a144","a145","a146","a147","a148","a149","a150","a151","a152","a153","a154","a155", \
    "a156","a157","a158","a159","a160","a161","a162","a163","a164","a165","a166","a167","a168","a169","a170","a171","a172","a173","a174","a175","a176", \
    "a177","a178","a179","a180","a181","a182","a183","a184","a185","a186","a187","a188","a189","a190","a191","a192","a193","a194","a195","a196","a197", \
    "a198","a199","a200","a201","a202","a203","a204","a205","a206","a207","a208","a209","a210","a211","a212","a213","a214","a215","a216","a217","a218", \
    "a219","a220","a221","a222","a223","a224","a225","a226","a227","a228","a229","a230","a231","a232","a233","a234","a235","a236","a237","a238","a239", \
    "a240","a241","a242","a243","a244","a245","a246","a247","a248","a249","a250","a251","a252","a253","a254","a255","s3","s4","s5","s6","s7","s8","s9", \
    "s10","s11","s12","s13","s14","s15","s16","s17","s18","s19","s20","s21","s22","s23","s24","s25","s26","s27","s28","s29","s30","s31","s32","s33","s34", \
    "s35","s36","s37","s38","s39","s40","s41","s42","s43","s44","s45","s46","s47","s48","s49","s50","s51","s52","s53","s54","s55","s56","s57","s58","s59", \
    "s60","s61","s62","s63","s64","s65","s66","s67","s68","s69","s70","s71","s72","s73","s74","s75","s76","s77","s78","s79","s80","s81","s82","s83","s84", \
    "s85","s86","s87","s88","s89","s90","s91","s92","s93","s94","s95","s96","s97","s98","s99"

extern "C" __global__ __launch_bounds__(256, 1) void k_fused(const u16* x, const u16* c, const u16* wmod, const u16* wd,
                                                             const u16* wg, const u16* wu, char* ws, u16* out, const u16* Are,
                                                             const u16* Aim, const u16* Bre, const u16* Bim, const u16* Cre,
                                                             const u16* Cim, const u16* logdt, const u16* dskip) {
    u32 tid = threadIdx.x;
    asm volatile(
    "s_load_dwordx16 s[8:23], %[karg], 0\n"
    "s_load_dwordx16 s[76:91], %[karg], 64\n"
    "v_readfirstlane_b32 s48, %[tid]\n"
    "v_and_b32 v65, 63, %[tid]\n"
    "s_getpc_b64 s[36:37]\n"
    "s_add_u32 s36, s36, k_fused@rel32@lo+4\n"
    "s_addc_u32 s37, s37, k_fused@rel32@hi+12\n"
    "s_and_b32 s37, s37, 0xffff\n"
    "s_mov_b32 s38, k3_code_end-k_fused\n"
    "s_mov_b32 s39, 0x20000\n"
    "s_lshr_b32 s48, s48, 6\n"
    "s_bfe_u32 s54, %[bid], 0x10003\n"
    "s_lshl2_add_u32 s54, s54, s48\n"
    "s_lshl_b32 s54, s54, 13\n"
    "v_lshlrev_b32 v66, 7, v65\n"
    "buffer_load_dword v217, v66, s[36:39], s54 offen\n"
    "v_and_b32 v66, 3, v65\n"
    "v_lshlrev_b32 v66, 11, v66\n"
    "v_and_b32 v67, 60, v65\n"
    "v_lshl_add_u32 v66, v67, 2, v66\n"
    "s_lshl_b32 s54, %[bid], 13\n"
    "s_lshl_b32 s55, s48, 8\n"
    "s_add_u32 s54, s54, s55\n"
    "v_add_u32 v66, s54, v66\n"
    "s_mul_i32 s32, %[bid], 1366\n"
    "s_lshr_b32 s32, s32, 16\n"
    "s_mul_i32 s54, s32, 48\n"
    "s_sub_u32 s33, %[bid], s54\n"
    "v_lshrrev_b32 v132, 3, %[tid]\n"
    "v_and_b32 v133, 7, %[tid]\n"
    "s_waitcnt lgkmcnt(0)\n"
    "global_load_dwordx4 v[96:99], v66, s[8:9] nt\n"
    "global_load_dwordx4 v[100:103], v66, s[8:9] offset:1024 nt\n"
    "v_mul_u32_u24 v67, 24576, v132\n"
    "v_lshl_add_u32 v67, v133, 4, v67\n"
    "v_lshlrev_b32 v132, 3, v132\n"
    "s_mul_i32 s54, s32, 786432\n"
    "s_lshl_b32 s55, s33, 7\n"
    "s_add_u32 s54, s54, s55\n"
    "s_lshl_b32 s55, s32, 8\n"
    "s_mov_b32 s36, s10\n"
    "s_and_b32 s37, s11, 0xffff\n"
    "s_mov_b32 s38, 1024\n"
    "buffer_load_dwordx2 v[120:121], v132, s[36:39], s55 offen\n"
    "s_mov_b32 s36, s12\n"
    "s_and_b32 s37, s13, 0xffff\n"
    "s_mov_b32 s38, 3145728\n"
    "buffer_load_dwordx4 v[104:107], v67, s[36:39], s54 offen nt\n"
    "s_add_u32 s54, s54, 6144\n"
    "buffer_load_dwordx4 v[108:111], v67, s[36:39], s54 offen nt\n"
    "s_add_u32 s54, s54, 6144\n"
    "buffer_load_dwordx4 v[112:115], v67, s[36:39], s54 offen nt\n"
    "s_add_u32 s54, s54, 6144\n"
    "buffer_load_dwordx4 v[116:119], v67, s[36:39], s54 offen nt\n"
    "s_and_b32 s52, %[bid], 7\n"
    "s_lshr_b32 s53, %[bid], 3\n"
    "s_lshl_b32 s54, s52, 5\n"
    "s_add_u32 s51, s54, s53\n"
    "s_lshr_b32 s49, s48, 1\n"
    "s_and_b32 s50, s48, 1\n"
    "s_lshl_b32 s56, s51, 9\n"
    "v_lshl_add_u32 v133, %[tid], 1, s56\n"
    "v_mov_b32 v1, 0\n"
    "v_mov_b32 v2, 0\n"
    "v_mov_b32 v4, 0\n"
    "v_mov_b32 v5, 0\n"
    "v_mov_b32 v6, 0\n"
    "v_mov_b32 v7, 0\n"
    "global_load_short_d16_hi v1, v133, s[76:77]\n"
    "global_load_short_d16_hi v2, v133, s[78:79]\n"
    "global_load_short_d16_hi v4, v133, s[80:81]\n"
    "global_load_short_d16_hi v5, v133, s[82:83]\n"
    "global_load_short_d16_hi v6, v133, s[84:85]\n"
    "global_load_short_d16_hi v7, v133, s[86:87]\n"
    "s_mov_b32 s42, %[cEND]\n"
    "s_mov_b32 s43, 0x20000\n"
    "s_lshl_b32 s56, s51, 2\n"
    "s_mov_b32 s55, %[cEP]\n"
    "s_load_dwordx2 s[30:31], s[20:21], s55\n"
    "s_load_dword s94, s[90:91], s56\n"
    "s_load_dword s95, s[88:89], s56\n"
    "s_mov_b32 s40, s20\n"
    "s_and_b32 s41, s21, 0xffff\n"
    "v_min_u32 v184, 0xdf, %[tid]\n"
    "v_lshrrev_b32 v66, 5, v184\n"
    "v_bfe_u32 v67, v184, 4, 1\n"
    "v_lshlrev_b32 v66, 14, v66\n"
    "v_lshl_add_u32 v66, v67, 12, v66\n"
    "v_and_b32 v67, 15, v184\n"
    "v_lshl_add_u32 v66, v67, 2, v66\n"
    "s_lshr_b32 s54, %[bid], 7\n"
    "s_lshl1_add_u32 s54, s52, s54\n"
    "s_mul_i32 s54, s54, 7\n"
    "s_and_b32 s55, s53, 15\n"
    "s_mul_i32 s34, s55, 112\n"
    "s_add_u32 s34, s34, s54\n"
    "s_lshl_b32 s34, s34, 10\n"
    "s_add_u32 s34, s34, %[cWD]\n"
    "s_lshl_b32 s54, s54, 14\n"
    "s_lshl_b32 s55, s55, 6\n"
    "s_add_u32 s35, s54, s55\n"
    "v_mov_b32 v85, v66\n"
    "s_waitcnt lgkmcnt(0)\n"
    "s_add_u32 s30, s30, 1\n"
    "s_add_u32 s31, s31, 0x9e3779b1\n"
    "s_and_b32 s89, %[bid], 1\n"
    "s_bfe_u32 s90, %[bid], 0x20001\n"
    "s_lshr_b32 s91, %[bid], 3\n"
    "v_mul_u32_u24 v185, 0x124a, v184\n"
    "v_lshrrev_b32 v185, 16, v185\n"
    "v_mul_u32_u24 v186, 14, v185\n"
    "v_sub_u32 v186, v184, v186\n"
    "v_mul_u32_u24 v66, 28672, v185\n"
    "v_lshl_add_u32 v66, v186, 3, v66\n"
    "s_cmp_eq_u32 s89, 0\n"
    "s_cselect_b32 s16, s16, s18\n"
    "s_cselect_b32 s17, s17, s19\n"
    "s_mul_i32 s54, s90, 458752\n"
    "s_mul_i32 s55, s91, 112\n"
    "s_add_u32 s29, s54, s55\n"
    "v_mov_b32 v86, v66\n"
    "v_cmp_gt_u32 vcc, 0x80, %[tid]\n"
    "v_mov_b32 v67, 1408\n"
    "v_mov_b32 v81, 2048\n"
    "v_lshlrev_b32 v80, 12, %[tid]\n"
    "v_cndmask_b32 v67, v67, v81, vcc\n"
    "v_and_b32 v81, 0xfe, %[tid]\n"
    "v_add_lshl_u32 v81, v81, v67, 3\n"
    "v_lshlrev_b32 v84, 4, %[tid]\n"
    "s_lshl4_add_u32 s60, s51, %[cZX]\n"
    "s_add_u32 s61, s60, 1048576\n"
    "v_and_b32 v24, 3, v65\n"
    "v_mul_u32_u24 v24, 24576, v24\n"
    "v_bfe_u32 v25, v65, 2, 3\n"
    "v_min_u32 v25, 5, v25\n"
    "v_lshl_add_u32 v24, v25, 12, v24\n"
    "v_lshrrev_b32 v25, 3, v65\n"
    "v_and_b32 v66, 1, v65\n"
    "v_cmp_eq_u32 vcc, 0, v66\n"
    "v_mov_b32 v218, s31\n"
    "v_mov_b32 v67, s30\n"
    "v_cndmask_b32 v218, v218, v67, vcc\n"
    "v_and_b32 v67, 7, v65\n"
    "v_lshlrev_b32 v213, 5, v67\n"
    "v_add_u32 v213, 134144, v213\n"
    "v_and_b32 v66, 3, v65\n"
    "v_lshlrev_b32 v212, 6, v66\n"
    "v_add_u32 v212, 134144, v212\n"
    "v_and_b32 v67, 48, v65\n"
    "v_add_u32 v212, v212, v67\n"
    "s_lshl_b32 s54, s48, 2\n"
    "v_add_u32 v212, s54, v212\n"
    "v_and_b32 v66, 7, v65\n"
    "v_lshl_add_u32 v66, v66, 3, v25\n"
    "s_lshl_b32 s54, s48, 8\n"
    "s_add_u32 s54, s54, 134400\n"
    "v_lshl_add_u32 v214, v66, 2, s54\n"
    "v_lshlrev_b32 v215, 2, v65\n"
    "v_add_u32 v215, 134400, v215\n"
    "v_lshlrev_b32 v82, 5, v184\n"
    "v_add_u32 v82, 110592, v82\n"
    "v_cmp_gt_u32 vcc, 0xe0, %[tid]\n"
    "v_mov_b32 v66, 0x7ff00000\n"
    "v_lshlrev_b32 v83, 4, %[tid]\n"
    "v_cndmask_b32 v83, v66, v83, vcc\n"
    "s_and_b32 s54, s52, 3\n"
    "s_and_b32 s55, s53, 3\n"
    "s_lshl_b32 s54, s54, 2\n"
    "s_add_u32 s76, s54, s55\n"
    "s_lshr_b32 s54, s52, 2\n"
    "s_lshr_b32 s55, s53, 2\n"
    "s_lshl_b32 s54, s54, 3\n"
    "s_add_u32 s54, s54, s55\n"
    "s_lshl2_add_u32 s77, s54, s48\n"
    "s_lshl4_add_u32 s84, s77, s76\n"
    "s_lshl_b32 s84, s84, 7\n"
    "s_add_u32 s84, s84, %[cFL]\n"
    "v_lshlrev_b32 v68, 4, v65\n"
    "v_lshlrev_b32 v72, 2, %[tid]\n"
    "v_add_u32 v72, 134144, v72\n"
    "v_lshlrev_b32 v73, 5, v185\n"
    "v_add_u32 v73, 134144, v73\n"
    "v_lshlrev_b32 v87, 4, v184\n"
    "v_add_u32 v87, 110592, v87\n"
    "v_and_b32 v176, 15, v65\n"
    "v_lshrrev_b32 v177, 4, v65\n"
    "s_mul_i32 s54, s49, 31008\n"
    "v_mul_u32_u24 v39, 544, v176\n"
    "v_lshl_add_u32 v39, v177, 4, v39\n"
    "v_add_u32 v39, s54, v39\n"
    "s_mul_i32 s55, s49, 4160\n"
    "s_add_u32 s55, s55, 62032\n"
    "v_lshl_add_u32 v41, v176, 4, s55\n"
    "v_mov_b32 v180, 0\n"
    "v_mov_b32 v181, 0\n"
    "v_mov_b32 v182, 0\n"
    "v_mov_b32 v183, 0\n"
    "v_lshrrev_b32 v66, 5, v65\n"
    "v_mul_u32_u24 v66, 4608, v66\n"
    "v_and_b32 v67, 31, v65\n"
    "v_lshl_add_u32 v66, v67, 4, v66\n"
    "v_add_u32 v66, 70336, v66\n"
    "v_mul_u32_u24 v67, 4160, v65\n"
    "ds_write_b128 v66, v[180:183]\n"
    "s_mov_b64 exec, 3\n"
    "ds_write_b128 v67, v[180:183] offset:62016\n"
    "s_mov_b64 exec, -1\n"
    "v_lshrrev_b32 v66, 1, v177\n"
    "v_and_b32 v67, 1, v177\n"
    "v_lshlrev_b32 v67, 3, v67\n"
    "v_lshl_add_u32 v67, v66, 8, v67\n"
    "v_lshl_add_u32 v38, v177, 10, v41\n"
    "v_lshlrev_b32 v43, 8, v177\n"
    "v_add_u32 v43, 512, v43\n"
    "v_lshlrev_b32 v44, 9, v177\n"
    "v_cmp_gt_u32 vcc, 2, v177\n"
    "s_cmp_eq_u32 s50, 0\n"
    "s_cselect_b64 s[74:75], -1, 0\n"
    "v_cndmask_b32 v43, v44, v43, vcc\n"
    "v_cndmask_b32 v43, v43, v67, s[74:75]\n"
    "v_add_u32 v41, v41, v43\n"
    "v_cndmask_b32 v66, 8, 4, vcc\n"
    "v_cndmask_b32 v66, v66, 2, s[74:75]\n"
    "v_add_u32 v42, v41, v66\n"
    "v_mov_b32 v67, 0x100\n"
    "v_cndmask_b32 v67, v67, 8, vcc\n"
    "v_cndmask_b32 v67, v67, 4, s[74:75]\n"
    "v_add_u32 v43, v41, v67\n"
    "v_add_u32 v44, v43, v66\n"
    "s_mul_i32 s54, s50, 8704\n"
    "v_add_u32 v40, s54, v39\n"
    "v_mov_b32 v224, -1\n"
    "v_mov_b32 v225, -1\n"
    "v_mov_b32 v226, -1\n"
    "v_mov_b32 v227, -1\n"
    "v_mov_b32 v228, 0\n"
    "s_lshl_b32 s6, %[bid], 13\n"
    "s_lshl_b32 s7, s48, 11\n"
    "s_add_u32 s6, s6, s7\n"
    "s_add_u32 s6, s6, %[cX2]\n"
    "s_lshl2_add_u32 s7, %[bid], s48\n"
    "s_lshl2_add_u32 s7, s7, %[cFW]\n"
    "buffer_store_dwordx4 v[224:227], v68, s[40:43], s6 offen offset:0 sc1\n"
    "buffer_store_dwordx4 v[224:227], v68, s[40:43], s6 offen offset:1024 sc1\n"
    "s_mov_b64 exec, 1\n"
    "buffer_store_dword v228, off, s[40:43], s7 sc1\n"
    "buffer_store_dword v228, off, s[40:43], s84 sc1\n"
    "s_mov_b64 exec, -1\n"
    "s_mov_b32 s56, 0x3f803f80\n"
    "v_mov_b32 v132, 0\n"
    "v_mov_b32 v133, 0\n"
    "v_mov_b32 v134, 0\n"
    "v_mov_b32 v135, 0\n"
    "v_mov_b32 v156, 0\n"
    "v_mov_b32 v157, 0\n"
    "v_mov_b32 v158, 0\n"
    "v_mov_b32 v159, 0\n"
    "v_mov_b32 v160, 0\n"
    "v_mov_b32 v161, 0\n"
    "v_mov_b32 v162, 0\n"
    "v_mov_b32 v163, 0\n"
    "s_waitcnt vmcnt(15)\n"
    "s_mov_b32 vcc_lo, 0xaaaaaaaa\n"
    "s_mov_b32 vcc_hi, 0xaaaaaaaa\n"
    "v_cndmask_b32_dpp v145, v96, v97, vcc quad_perm:[1,0,3,2] row_mask:0xf bank_mask:0xf\n"
    "v_cndmask_b32_dpp v147, v98, v99, vcc quad_perm:[1,0,3,2] row_mask:0xf bank_mask:0xf\n"
    "v_cndmask_b32_dpp v149, v100, v101, vcc quad_perm:[1,0,3,2] row_mask:0xf bank_mask:0xf\n"
    "v_cndmask_b32_dpp v151, v102, v103, vcc quad_perm:[1,0,3,2] row_mask:0xf bank_mask:0xf\n"
    "s_mov_b32 vcc_lo, 0x55555555\n"
    "s_mov_b32 vcc_hi, 0x55555555\n"
    "v_cndmask_b32_dpp v144, v97, v96, vcc quad_perm:[1,0,3,2] row_mask:0xf bank_mask:0xf\n"
    "v_cndmask_b32_dpp v146, v99, v98, vcc quad_perm:[1,0,3,2] row_mask:0xf bank_mask:0xf\n"
    "v_cndmask_b32_dpp v148, v101, v100, vcc quad_perm:[1,0,3,2] row_mask:0xf bank_mask:0xf\n"
    "v_cndmask_b32_dpp v150, v103, v102, vcc quad_perm:[1,0,3,2] row_mask:0xf bank_mask:0xf\n"
    "s_mov_b32 vcc_lo, 0xcccccccc\n"
    "s_mov_b32 vcc_hi, 0xcccccccc\n"
    "v_cndmask_b32_dpp v168, v144, v146, vcc quad_perm:[2,3,0,1] row_mask:0xf bank_mask:0xf\n"
    "v_cndmask_b32_dpp v170, v145, v147, vcc quad_perm:[2,3,0,1] row_mask:0xf bank_mask:0xf\n"
    "v_cndmask_b32_dpp v169, v148, v150, vcc quad_perm:[2,3,0,1] row_mask:0xf bank_mask:0xf\n"
    "v_cndmask_b32_dpp v171, v149, v151, vcc quad_perm:[2,3,0,1] row_mask:0xf bank_mask:0xf\n"
    "s_mov_b32 vcc_lo, 0x33333333\n"
    "s_mov_b32 vcc_hi, 0x33333333\n"
    "v_cndmask_b32_dpp v164, v146, v144, vcc quad_perm:[2,3,0,1] row_mask:0xf bank_mask:0xf\n"
    "v_cndmask_b32_dpp v166, v147, v145, vcc quad_perm:[2,3,0,1] row_mask:0xf bank_mask:0xf\n"
    "v_cndmask_b32_dpp v165, v150, v148, vcc quad_perm:[2,3,0,1] row_mask:0xf bank_mask:0xf\n"
    "v_cndmask_b32_dpp v167, v151, v149, vcc quad_perm:[2,3,0,1] row_mask:0xf bank_mask:0xf\n"
    "s_lshl_b32 s6, %[bid], 13\n"
    "s_lshl_b32 s7, s48, 10\n"
    "s_add_u32 s6, s6, s7\n"
    "s_add_u32 s6, s6, %[cZX]\n"
    "s_add_u32 s7, s6, 4096\n"
    "buffer_store_dwordx4 v[164:167], v68, s[40:43], s6 offen sc1\n"
    "buffer_store_dwordx4 v[168:171], v68, s[40:43], s7 offen sc1\n"
    "v_dot2c_f32_bf16 v132, s56, v96\n"
    "v_dot2c_f32_bf16 v133, v96, v96\n"
    "v_dot2c_f32_bf16 v134, s56, v100\n"
    "v_dot2c_f32_bf16 v135, v100, v100\n"
    "v_dot2c_f32_bf16 v132, s56, v97\n"
    "v_dot2c_f32_bf16 v133, v97, v97\n"
    "v_dot2c_f32_bf16 v134, s56, v101\n"
    "v_dot2c_f32_bf16 v135, v101, v101\n"
    "v_dot2c_f32_bf16 v132, s56, v98\n"
    "v_dot2c_f32_bf16 v133, v98, v98\n"
    "v_dot2c_f32_bf16 v134, s56, v102\n"
    "v_dot2c_f32_bf16 v135, v102, v102\n"
    "v_dot2c_f32_bf16 v132, s56, v99\n"
    "v_dot2c_f32_bf16 v133, v99, v99\n"
    "v_dot2c_f32_bf16 v134, s56, v103\n"
    "v_dot2c_f32_bf16 v135, v103, v103\n"
    "s_waitcnt vmcnt(16)\n"
    "v_lshlrev_b32 v136, 16, v120\n"
    "v_and_b32 v137, 0xffff0000, v120\n"
    "v_lshlrev_b32 v138, 16, v121\n"
    "v_permlane32_swap_b32 v132, v134\n"
    "v_permlane32_swap_b32 v133, v135\n"
    "v_and_b32 v139, 0xffff0000, v121\n"
    "v_add_f32 v132, v132, v134\n"
    "v_add_f32 v133, v133, v135\n"
    "v_mul_f32 v140, 0xbfb8aa3b, v136\n"
    "v_mul_f32 v141, 0xbfb8aa3b, v137\n"
    "v_permlane16_swap_b32 v132, v133\n"
    "v_mul_f32 v142, 0xbfb8aa3b, v138\n"
    "v_add_f32 v132, v132, v133\n"
    "v_mul_f32 v143, 0xbfb8aa3b, v139\n"
    "v_exp_f32 v140, v140\n"
    "v_add_f32_dpp v132, v132, v132 row_ror:4 row_mask:0xf bank_mask:0xf\n"
    "v_exp_f32 v141, v141\n"
    "v_exp_f32 v142, v142\n"
    "v_add_f32_dpp v132, v132, v132 row_ror:8 row_mask:0xf bank_mask:0xf\n"
    "v_exp_f32 v143, v143\n"
    "s_mov_b32 exec_lo, 0x000f000f\n"
    "s_mov_b32 exec_hi, 0x000f000f\n"
    "ds_write_b32 v212, v132\n"
    "s_mov_b64 exec, -1\n"
    "v_add_f32 v140, 1.0, v140\n"
    "v_add_f32 v141, 1.0, v141\n"
    "v_add_f32 v142, 1.0, v142\n"
    "v_add_f32 v143, 1.0, v143\n"
    "v_rcp_f32 v140, v140\n"
    "v_rcp_f32 v141, v141\n"
    "v_rcp_f32 v142, v142\n"
    "v_rcp_f32 v143, v143\n"
    "s_mov_b32 s54, 0x05040100\n"
    "s_mov_b32 s55, 0x07060302\n"
    "v_mul_f32 v136, v136, v140\n"
    "v_mul_f32 v137, v137, v141\n"
    "v_mul_f32 v138, v138, v142\n"
    "v_mul_f32 v139, v139, v143\n"
    "v_cvt_pk_bf16_f32 v120, v136, v137\n"
    "v_cvt_pk_bf16_f32 v121, v138, v139\n"
    "s_waitcnt vmcnt(14)\n"
    "v_perm_b32 v144, v108, v104, s54\n"
    "v_perm_b32 v145, v108, v104, s55\n"
    "v_perm_b32 v146, v109, v105, s54\n"
    "v_perm_b32 v147, v109, v105, s55\n"
    "v_perm_b32 v148, v110, v106, s54\n"
    "v_perm_b32 v149, v110, v106, s55\n"
    "v_perm_b32 v150, v111, v107, s54\n"
    "v_perm_b32 v151, v111, v107, s55\n"
    "v_dot2c_f32_bf16 v156, v144, v120\n"
    "v_dot2c_f32_bf16 v157, v145, v120\n"
    "v_dot2c_f32_bf16 v158, v146, v120\n"
    "v_dot2c_f32_bf16 v159, v147, v120\n"
    "v_dot2c_f32_bf16 v160, v148, v120\n"
    "v_dot2c_f32_bf16 v161, v149, v120\n"
    "v_dot2c_f32_bf16 v162, v150, v120\n"
    "v_dot2c_f32_bf16 v163, v151, v120\n"
    "s_waitcnt vmcnt(12)\n"
    "v_perm_b32 v144, v116, v112, s54\n"
    "v_perm_b32 v145, v116, v112, s55\n"
    "v_perm_b32 v146, v117, v113, s54\n"
    "v_perm_b32 v147, v117, v113, s55\n"
    "v_perm_b32 v148, v118, v114, s54\n"
    "v_perm_b32 v149, v118, v114, s55\n"
    "v_perm_b32 v150, v119, v115, s54\n"
    "v_perm_b32 v151, v119, v115, s55\n"
    "v_dot2c_f32_bf16 v156, v144, v121\n"
    "v_dot2c_f32_bf16 v157, v145, v121\n"
    "v_dot2c_f32_bf16 v158, v146, v121\n"
    "v_dot2c_f32_bf16 v159, v147, v121\n"
    "v_dot2c_f32_bf16 v160, v148, v121\n"
    "v_dot2c_f32_bf16 v161, v149, v121\n"
    "v_dot2c_f32_bf16 v162, v150, v121\n"
    "v_dot2c_f32_bf16 v163, v151, v121\n"
    "s_nop 2\n"
    "v_permlane32_swap_b32 v156, v160\n"
    "v_permlane32_swap_b32 v158, v162\n"
    "v_permlane32_swap_b32 v157, v161\n"
    "v_permlane32_swap_b32 v159, v163\n"
    "v_add_f32 v156, v156, v160\n"
    "v_add_f32 v158, v158, v162\n"
    "v_add_f32 v157, v157, v161\n"
    "v_add_f32 v159, v159, v163\n"
    "s_nop 1\n"
    "v_permlane16_swap_b32 v156, v158\n"
    "v_permlane16_swap_b32 v157, v159\n"
    "v_add_f32 v156, v156, v158\n"
    "v_add_f32 v157, v157, v159\n"
    "s_mov_b32 vcc_lo, 0xff00ff00\n"
    "s_mov_b32 vcc_hi, 0xff00ff00\n"
    "v_cndmask_b32 v158, v157, v156, vcc\n"
    "v_cndmask_b32 v159, v156, v157, vcc\n"
    "s_nop 1\n"
    "v_add_f32_dpp v159, v158, v159 row_ror:8 row_mask:0xf bank_mask:0xf\n"
    "ds_write_b32 v214, v159\n"
    "s_lshl_b32 s54, s95, 16\n"
    "s_and_b32 s55, s95, 0xffff0000\n"
    "s_cmp_eq_u32 s49, 0\n"
    "s_cselect_b32 s54, s54, s55\n"
    "s_mov_b32 s55, 0x3fb8aa3b\n"
    "v_mov_b32 v168, s54\n"
    "v_mul_f32 v169, s55, v168\n"
    "v_rndne_f32 v170, v169\n"
    "v_fma_f32 v171, v168, s55, -v169\n"
    "v_sub_f32 v169, v169, v170\n"
    "v_fmamk_f32 v171, v168, 0x32a5705f, v171\n"
    "v_cvt_i32_f32 v170, v170\n"
    "v_add_f32 v169, v169, v171\n"
    "v_exp_f32 v169, v169\n"
    "v_cmp_ngt_f32 vcc, 0xc2ce8ed0, v168\n"
    "v_ldexp_f32 v169, v169, v170\n"
    "v_mov_b32 v171, 0x7f800000\n"
    "v_cndmask_b32 v169, 0, v169, vcc\n"
    "v_cmp_nlt_f32 vcc, 0x42b17218, v168\n"
    "s_nop 1\n"
    "v_cndmask_b32 v169, v171, v169, vcc\n"
    "v_cvt_pk_bf16_f32 v169, v169, v169\n"
    "v_lshlrev_b32 v216, 16, v169\n"
    "s_waitcnt vmcnt(6)\n"
    "v_mul_f32 v36, v216, v1\n"
    "v_mul_f32 v37, v216, v2\n"
    "v_mul_f32 v36, 0x3fb8aa3b, v36\n"
    "v_mul_f32 v37, 0.15915494, v37\n"
    "v_mul_f32 v97, 0x41000000, v36\n"
    "v_mul_f32 v101, 0x41000000, v37\n"
    "v_mul_f32 v98, 0x43000000, v36\n"
    "v_mul_f32 v102, 0x43000000, v37\n"
    "v_fract_f32 v101, v101\n"
    "v_fract_f32 v102, v102\n"
    "v_fract_f32 v100, v37\n"
    "v_exp_f32 v104, v97\n"
    "v_cos_f32 v28, v101\n"
    "v_sin_f32 v29, v101\n"
    "v_exp_f32 v106, v98\n"
    "v_cos_f32 v30, v102\n"
    "v_sin_f32 v31, v102\n"
    "v_exp_f32 v108, v36\n"
    "v_cos_f32 v26, v100\n"
    "v_sin_f32 v27, v100\n"
    "v_pk_mul_f32 v[28:29], v[104:105], v[28:29] op_sel_hi:[0,1]\n"
    "v_pk_mul_f32 v[30:31], v[106:107], v[30:31] op_sel_hi:[0,1]\n"
    "v_pk_mul_f32 v[26:27], v[108:109], v[26:27] op_sel_hi:[0,1]\n"
    "v_mul_f32 v4, v216, v4\n"
    "v_mul_f32 v5, v216, v5\n"
    "s_waitcnt vmcnt(0)\n"
    "s_waitcnt lgkmcnt(0)\n"
    "s_barrier\n"
    "ds_read_b128 v[132:135], v213 offset:0\n"
    "ds_read_b128 v[136:139], v213 offset:16\n"
    "ds_read2st64_b32 v[140:141], v215 offset0:0 offset1:1\n"
    "ds_read2st64_b32 v[142:143], v215 offset0:2 offset1:3\n"
    "s_lshl_b32 s54, %[bid], 9\n"
    "s_add_u32 s54, s54, %[cMOD]\n"
    "s_cmp_lt_u32 %[bid], 192\n"
    "s_cselect_b32 s54, s54, 0x7ff00000\n"
    "s_cmp_eq_u32 s48, 1\n"
    "s_cselect_b32 s54, s54, 0x7ff00000\n"
    "s_lshl_b32 s55, %[bid], 5\n"
    "s_add_u32 s55, s55, %[cST]\n"
    "s_cmp_eq_u32 s48, 0\n"
    "s_cselect_b32 s54, s55, s54\n"
    "s_cselect_b64 s[74:75], -1, 0\n"
    "v_lshrrev_b32 v66, 1, v68\n"
    "v_bfe_u32 v67, v65, 1, 1\n"
    "v_lshlrev_b32 v67, 13, v67\n"
    "v_and_b32 v144, 4, v65\n"
    "v_lshl_add_u32 v67, v144, 2, v67\n"
    "v_and_b32 v144, 1, v65\n"
    "v_lshl_add_u32 v67, v144, 3, v67\n"
    "v_cndmask_b32 v66, v66, v67, s[74:75]\n"
    "s_waitcnt lgkmcnt(0)\n"
    "v_add_f32 v132, v132, v133\n"
    "v_add_f32 v140, v140, v141\n"
    "v_add_f32 v134, v134, v135\n"
    "v_add_f32 v142, v142, v143\n"
    "v_add_f32 v136, v136, v137\n"
    "v_add_f32 v140, v140, v142\n"
    "v_add_f32 v138, v138, v139\n"
    "v_mov_b32 v141, v218\n"
    "v_add_f32 v132, v132, v134\n"
    "v_add_f32 v136, v136, v138\n"
    "v_mul_f32 v132, 0x3b000000, v132\n"
    "v_mul_f32 v136, 0x3b000000, v136\n"
    "s_nop 0\n"
    "v_fma_f32 v136, -v132, v132, v136\n"
    "s_nop 0\n"
    "v_max_f32 v136, 0, v136\n"
    "s_nop 0\n"
    "v_cvt_pk_bf16_f32 v136, v136, v136\n"
    "s_nop 0\n"
    "v_lshlrev_b32 v136, 16, v136\n"
    "s_nop 0\n"
    "v_add_f32 v136, 0x3727c5ac, v136\n"
    "s_nop 0\n"
    "v_cvt_pk_bf16_f32 v136, v136, v136\n"
    "s_nop 0\n"
    "v_lshlrev_b32 v136, 16, v136\n"
    "s_nop 0\n"
    "v_sqrt_f32 v136, v136\n"
    "s_nop 0\n"
    "v_cvt_pk_bf16_f32 v132, v132, v136\n"
    "v_cndmask_b32 v140, v140, v132, s[74:75]\n"
    "s_cmp_eq_u32 s48, 0\n"
    "s_cselect_b64 exec, 0xff, -1\n"
    "buffer_store_dwordx2 v[140:141], v66, s[40:43], s54 offen sc1\n"
    "s_mov_b64 exec, -1\n"
    "s_mul_i32 s54, s49, 31008\n"
    "s_lshl_b32 s55, s50, 8\n"
    "s_add_u32 m0, s54, s55\n"
    "v_mov_b32 v164, 0x3f80\n"
    "v_pk_mul_f32 v[154:155], v[6:7], v[4:5] op_sel_hi:[0,1]\n"
    "v_pk_mul_f32 v[158:159], v[26:27], v[26:27] op_sel_hi:[0,1]\n"
    "ds_write_addtid_b32 v164\n"
    "v_pk_fma_f32 v[116:117], v[6:7], v[4:5], v[154:155] op_sel:[1,1,0] op_sel_hi:[1,0,1] neg_lo:[0,1,0]\n"
    "v_pk_fma_f32 v[32:33], v[26:27], v[26:27], v[158:159] op_sel:[1,1,0] op_sel_hi:[1,0,1] neg_lo:[0,1,0]\n"
    "v_mov_b32 v112, v28\n"
    "v_pk_mul_f32 v[154:155], v[116:117], v[30:31] op_sel_hi:[0,1]\n"
    "s_mov_b32 s36, s14\n"
    "s_and_b32 s37, s15, 0xffff\n"
    "s_mov_b32 s38, 1835008\n"
    "s_add_u32 s28, s35, 8192\n"
    "buffer_load_dword v240, v85, s[36:39], s35 offen offset:0\n"
    "v_pk_mul_f32 v[156:157], v[116:117], v[26:27] op_sel_hi:[0,1]\n"
    "v_pk_mul_f32 v[158:159], v[32:33], v[32:33] op_sel_hi:[0,1]\n"
    "v_xor_b32 v113, 0x80000000, v29\n"
    "v_pk_fma_f32 v[118:119], v[116:117], v[30:31], v[154:155] op_sel:[1,1,0] op_sel_hi:[1,0,1] neg_lo:[0,1,0]\n"
    "v_pk_fma_f32 v[160:161], v[116:117], v[26:27], v[156:157] op_sel:[1,1,0] op_sel_hi:[1,0,1] neg_lo:[0,1,0]\n"
    "v_pk_fma_f32 v[34:35], v[32:33], v[32:33], v[158:159] op_sel:[1,1,0] op_sel_hi:[1,0,1] neg_lo:[0,1,0]\n"
    "v_cvt_pk_bf16_f32 v164, v112, v113\n"
    "v_cvt_pk_bf16_f32 v165, v116, v117\n"
    "buffer_load_dword v241, v85, s[36:39], s35 offen offset:1024\n"
    "v_cvt_pk_bf16_f32 v166, v160, v161\n"
    "v_pk_mul_f32 v[158:159], v[118:119], v[26:27] op_sel_hi:[0,1]\n"
    "ds_write_addtid_b32 v164 offset:544\n"
    "ds_write_addtid_b32 v165 offset:8704\n"
    "ds_write_addtid_b32 v166 offset:9248\n"
    "v_pk_fma_f32 v[162:163], v[118:119], v[26:27], v[158:159] op_sel:[1,1,0] op_sel_hi:[1,0,1] neg_lo:[0,1,0]\n"
    "v_pk_mul_f32 v[152:153], v[112:113], v[28:29] op_sel_hi:[0,1] neg_hi:[0,1]\n"
    "v_pk_mul_f32 v[154:155], v[118:119], v[30:31] op_sel_hi:[0,1]\n"
    "buffer_load_dword v242, v85, s[36:39], s35 offen offset:2048\n"
    "v_pk_mul_f32 v[156:157], v[160:161], v[26:27] op_sel_hi:[0,1]\n"
    "v_cvt_pk_bf16_f32 v167, v162, v163\n"
    "v_pk_fma_f32 v[112:113], v[112:113], v[28:29], v[152:153] op_sel:[1,1,0] op_sel_hi:[1,0,1]\n"
    "v_pk_fma_f32 v[120:121], v[118:119], v[30:31], v[154:155] op_sel:[1,1,0] op_sel_hi:[1,0,1] neg_lo:[0,1,0]\n"
    "v_pk_fma_f32 v[160:161], v[160:161], v[26:27], v[156:157] op_sel:[1,1,0] op_sel_hi:[1,0,1] neg_lo:[0,1,0]\n"
    "ds_write_addtid_b32 v167 offset:13600\n"
    "v_cvt_pk_bf16_f32 v164, v112, v113\n"
    "v_cvt_pk_bf16_f32 v165, v118, v119\n"
    "buffer_load_dword v243, v85, s[36:39], s35 offen offset:3072\n"
    "v_cvt_pk_bf16_f32 v166, v160, v161\n"
    "v_pk_mul_f32 v[158:159], v[162:163], v[26:27] op_sel_hi:[0,1]\n"
    "ds_write_addtid_b32 v164 offset:1088\n"
    "ds_write_addtid_b32 v165 offset:13056\n"
    "ds_write_addtid_b32 v166 offset:9792\n"
    "v_pk_fma_f32 v[162:163], v[162:163], v[26:27], v[158:159] op_sel:[1,1,0] op_sel_hi:[1,0,1] neg_lo:[0,1,0]\n"
    "v_pk_mul_f32 v[152:153], v[112:113], v[28:29] op_sel_hi:[0,1] neg_hi:[0,1]\n"
    "v_pk_mul_f32 v[154:155], v[120:121], v[30:31] op_sel_hi:[0,1]\n"
    "buffer_load_dword v244, v85, s[36:39], s28 offen offset:0\n"
    "v_pk_mul_f32 v[156:157], v[160:161], v[26:27] op_sel_hi:[0,1]\n"
    "v_cvt_pk_bf16_f32 v167, v162, v163\n"
    "v_pk_fma_f32 v[112:113], v[112:113], v[28:29], v[152:153] op_sel:[1,1,0] op_sel_hi:[1,0,1]\n"
    "v_pk_fma_f32 v[122:123], v[120:121], v[30:31], v[154:155] op_sel:[1,1,0] op_sel_hi:[1,0,1] neg_lo:[0,1,0]\n"
    "v_pk_fma_f32 v[160:161], v[160:161], v[26:27], v[156:157] op_sel:[1,1,0] op_sel_hi:[1,0,1] neg_lo:[0,1,0]\n"
    "ds_write_addtid_b32 v167 offset:14144\n"
    "v_cvt_pk_bf16_f32 v164, v112, v113\n"
    "v_cvt_pk_bf16_f32 v165, v120, v121\n"
    "buffer_load_dword v245, v85, s[36:39], s28 offen offset:1024\n"
    "v_cvt_pk_bf16_f32 v166, v160, v161\n"
    "v_pk_mul_f32 v[158:159], v[162:163], v[26:27] op_sel_hi:[0,1]\n"
    "ds_write_addtid_b32 v164 offset:1632\n"
    "ds_write_addtid_b32 v165 offset:17408\n"
    "ds_write_addtid_b32 v166 offset:10336\n"
    "v_pk_fma_f32 v[162:163], v[162:163], v[26:27], v[158:159] op_sel:[1,1,0] op_sel_hi:[1,0,1] neg_lo:[0,1,0]\n"
    "v_pk_mul_f32 v[152:153], v[112:113], v[28:29] op_sel_hi:[0,1] neg_hi:[0,1]\n"
    "v_pk_mul_f32 v[154:155], v[122:123], v[30:31] op_sel_hi:[0,1]\n"
    "buffer_load_dword v246, v85, s[36:39], s28 offen offset:2048\n"
    "v_pk_mul_f32 v[156:157], v[160:161], v[26:27] op_sel_hi:[0,1]\n"
    "v_cvt_pk_bf16_f32 v167, v162, v163\n"
    "v_pk_fma_f32 v[112:113], v[112:113], v[28:29], v[152:153] op_sel:[1,1,0] op_sel_hi:[1,0,1]\n"
    "v_pk_fma_f32 v[124:125], v[122:123], v[30:31], v[154:155] op_sel:[1,1,0] op_sel_hi:[1,0,1] neg_lo:[0,1,0]\n"
    "v_pk_fma_f32 v[160:161], v[160:161], v[26:27], v[156:157] op_sel:[1,1,0] op_sel_hi:[1,0,1] neg_lo:[0,1,0]\n"
    "ds_write_addtid_b32 v167 offset:14688\n"
    "v_cvt_pk_bf16_f32 v164, v112, v113\n"
    "v_cvt_pk_bf16_f32 v165, v122, v123\n"
    "buffer_load_dword v247, v85, s[36:39], s28 offen offset:3072\n"
    "v_cvt_pk_bf16_f32 v166, v160, v161\n"
    "v_pk_mul_f32 v[158:159], v[162:163], v[26:27] op_sel_hi:[0,1]\n"
    "ds_write_addtid_b32 v164 offset:2176\n"
    "ds_write_addtid_b32 v165 offset:19584\n"
    "ds_write_addtid_b32 v166 offset:10880\n"
    "v_pk_fma_f32 v[162:163], v[162:163], v[26:27], v[158:159] op_sel:[1,1,0] op_sel_hi:[1,0,1] neg_lo:[0,1,0]\n"
    "v_pk_mul_f32 v[152:153], v[112:113], v[28:29] op_sel_hi:[0,1] neg_hi:[0,1]\n"
    "v_pk_mul_f32 v[154:155], v[124:125], v[30:31] op_sel_hi:[0,1]\n"
    "s_mov_b32 s36, s16\n"
    "s_and_b32 s37, s17, 0xffff\n"
    "s_mov_b32 s38, 1835008\n"
    "buffer_load_dwordx2 v[192:193], v86, s[36:39], s29 offen\n"
    "v_pk_mul_f32 v[156:157], v[160:161], v[26:27] op_sel_hi:[0,1]\n"
    "v_cvt_pk_bf16_f32 v167, v162, v163\n"
    "v_pk_fma_f32 v[112:113], v[112:113], v[28:29], v[152:153] op_sel:[1,1,0] op_sel_hi:[1,0,1]\n"
    "v_pk_fma_f32 v[126:127], v[124:125], v[30:31], v[154:155] op_sel:[1,1,0] op_sel_hi:[1,0,1] neg_lo:[0,1,0]\n"
    "v_pk_fma_f32 v[160:161], v[160:161], v[26:27], v[156:157] op_sel:[1,1,0] op_sel_hi:[1,0,1] neg_lo:[0,1,0]\n"
    "ds_write_addtid_b32 v167 offset:15232\n"
    "v_cvt_pk_bf16_f32 v164, v112, v113\n"
    "v_cvt_pk_bf16_f32 v165, v124, v125\n"
    "s_add_u32 s29, s29, 3584\n"
    "buffer_load_dwordx2 v[194:195], v86, s[36:39], s29 offen\n"
    "v_cvt_pk_bf16_f32 v166, v160, v161\n"
    "v_pk_mul_f32 v[158:159], v[162:163], v[26:27] op_sel_hi:[0,1]\n"
    "ds_write_addtid_b32 v164 offset:2720\n"
    "ds_write_addtid_b32 v165 offset:21760\n"
    "ds_write_addtid_b32 v166 offset:11424\n"
    "v_pk_fma_f32 v[162:163], v[162:163], v[26:27], v[158:159] op_sel:[1,1,0] op_sel_hi:[1,0,1] neg_lo:[0,1,0]\n"
    "v_pk_mul_f32 v[152:153], v[112:113], v[28:29] op_sel_hi:[0,1] neg_hi:[0,1]\n"
    "v_pk_mul_f32 v[154:155], v[126:127], v[30:31] op_sel_hi:[0,1]\n"
    "s_add_u32 s29, s29, 3584\n"
    "buffer_load_dwordx2 v[196:197], v86, s[36:39], s29 offen\n"
    "v_pk_mul_f32 v[156:157], v[160:161], v[26:27] op_sel_hi:[0,1]\n"
    "v_cvt_pk_bf16_f32 v167, v162, v163\n"
    "v_pk_fma_f32 v[112:113], v[112:113], v[28:29], v[152:153] op_sel:[1,1,0] op_sel_hi:[1,0,1]\n"
    "v_pk_fma_f32 v[128:129], v[126:127], v[30:31], v[154:155] op_sel:[1,1,0] op_sel_hi:[1,0,1] neg_lo:[0,1,0]\n"
    "v_pk_fma_f32 v[160:161], v[160:161], v[26:27], v[156:157] op_sel:[1,1,0] op_sel_hi:[1,0,1] neg_lo:[0,1,0]\n"
    "ds_write_addtid_b32 v167 offset:15776\n"
    "v_cvt_pk_bf16_f32 v164, v112, v113\n"
    "v_cvt_pk_bf16_f32 v165, v126, v127\n"
    "s_add_u32 s29, s29, 3584\n"
    "buffer_load_dwordx2 v[198:199], v86, s[36:39], s29 offen\n"
    "v_cvt_pk_bf16_f32 v166, v160, v161\n"
    "v_pk_mul_f32 v[158:159], v[162:163], v[26:27] op_sel_hi:[0,1]\n"
    "ds_write_addtid_b32 v164 offset:3264\n"
    "ds_write_addtid_b32 v165 offset:22848\n"
    "ds_write_addtid_b32 v166 offset:11968\n"
    "v_pk_fma_f32 v[162:163], v[162:163], v[26:27], v[158:159] op_sel:[1,1,0] op_sel_hi:[1,0,1] neg_lo:[0,1,0]\n"
    "v_pk_mul_f32 v[152:153], v[112:113], v[28:29] op_sel_hi:[0,1] neg_hi:[0,1]\n"
    "v_pk_mul_f32 v[154:155], v[128:129], v[30:31] op_sel_hi:[0,1]\n"
    "s_add_u32 s29, s29, 3584\n"
    "buffer_load_dwordx2 v[200:201], v86, s[36:39], s29 offen\n"
    "v_pk_mul_f32 v[156:157], v[160:161], v[26:27] op_sel_hi:[0,1]\n"
    "v_cvt_pk_bf16_f32 v167, v162, v163\n"
    "v_pk_fma_f32 v[112:113], v[112:113], v[28:29], v[152:153] op_sel:[1,1,0] op_sel_hi:[1,0,1]\n"
    "v_pk_fma_f32 v[130:131], v[128:129], v[30:31], v[154:155] op_sel:[1,1,0] op_sel_hi:[1,0,1] neg_lo:[0,1,0]\n"
    "v_pk_fma_f32 v[160:161], v[160:161], v[26:27], v[156:157] op_sel:[1,1,0] op_sel_hi:[1,0,1] neg_lo:[0,1,0]\n"
    "ds_write_addtid_b32 v167 offset:16320\n"
    "v_cvt_pk_bf16_f32 v164, v112, v113\n"
    "v_cvt_pk_bf16_f32 v165, v128, v129\n"
    "s_add_u32 s29, s29, 3584\n"
    "buffer_load_dwordx2 v[202:203], v86, s[36:39], s29 offen\n"
    "v_cvt_pk_bf16_f32 v166, v160, v161\n"
    "v_pk_mul_f32 v[158:159], v[162:163], v[26:27] op_sel_hi:[0,1]\n"
    "ds_write_addtid_b32 v164 offset:3808\n"
    "ds_write_addtid_b32 v165 offset:23936\n"
    "ds_write_addtid_b32 v166 offset:12512\n"
    "v_pk_fma_f32 v[162:163], v[162:163], v[26:27], v[158:159] op_sel:[1,1,0] op_sel_hi:[1,0,1] neg_lo:[0,1,0]\n"
    "v_pk_mul_f32 v[152:153], v[112:113], v[28:29] op_sel_hi:[0,1] neg_hi:[0,1]\n"
    "v_pk_mul_f32 v[154:155], v[130:131], v[30:31] op_sel_hi:[0,1]\n"
    "s_add_u32 s29, s29, 3584\n"
    "buffer_load_dwordx2 v[204:205], v86, s[36:39], s29 offen\n"
    "v_pk_mul_f32 v[156:157], v[120:121], v[32:33] op_sel_hi:[0,1]\n"
    "v_cvt_pk_bf16_f32 v167, v162, v163\n"
    "v_pk_fma_f32 v[112:113], v[112:113], v[28:29], v[152:153] op_sel:[1,1,0] op_sel_hi:[1,0,1]\n"
    "v_pk_fma_f32 v[132:133], v[130:131], v[30:31], v[154:155] op_sel:[1,1,0] op_sel_hi:[1,0,1] neg_lo:[0,1,0]\n"
    "v_pk_fma_f32 v[160:161], v[120:121], v[32:33], v[156:157] op_sel:[1,1,0] op_sel_hi:[1,0,1] neg_lo:[0,1,0]\n"
    "ds_write_addtid_b32 v167 offset:16864\n"
    "v_cvt_pk_bf16_f32 v164, v112, v113\n"
    "v_cvt_pk_bf16_f32 v165, v130, v131\n"
    "s_add_u32 s29, s29, 3584\n"
    "buffer_load_dwordx2 v[206:207], v86, s[36:39], s29 offen\n"
    "v_cvt_pk_bf16_f32 v166, v160, v161\n"
    "v_pk_mul_f32 v[158:159], v[122:123], v[32:33] op_sel_hi:[0,1]\n"
    "ds_write_addtid_b32 v164 offset:4352\n"
    "ds_write_addtid_b32 v165 offset:25024\n"
    "ds_write_addtid_b32 v166 offset:17952\n"
    "v_pk_fma_f32 v[162:163], v[122:123], v[32:33], v[158:159] op_sel:[1,1,0] op_sel_hi:[1,0,1] neg_lo:[0,1,0]\n"
    "v_pk_mul_f32 v[152:153], v[112:113], v[28:29] op_sel_hi:[0,1] neg_hi:[0,1]\n"
    "v_pk_mul_f32 v[154:155], v[132:133], v[30:31] op_sel_hi:[0,1]\n"
    "v_pk_mul_f32 v[156:157], v[160:161], v[32:33] op_sel_hi:[0,1]\n"
    "v_cvt_pk_bf16_f32 v167, v162, v163\n"
    "v_pk_fma_f32 v[112:113], v[112:113], v[28:29], v[152:153] op_sel:[1,1,0] op_sel_hi:[1,0,1]\n"
    "v_pk_fma_f32 v[134:135], v[132:133], v[30:31], v[154:155] op_sel:[1,1,0] op_sel_hi:[1,0,1] neg_lo:[0,1,0]\n"
    "v_pk_fma_f32 v[160:161], v[160:161], v[32:33], v[156:157] op_sel:[1,1,0] op_sel_hi:[1,0,1] neg_lo:[0,1,0]\n"
    "ds_write_addtid_b32 v167 offset:20128\n"
    "v_cvt_pk_bf16_f32 v164, v112, v113\n"
    "v_cvt_pk_bf16_f32 v165, v132, v133\n"
    "v_cvt_pk_bf16_f32 v166, v160, v161\n"
    "v_pk_mul_f32 v[158:159], v[162:163], v[32:33] op_sel_hi:[0,1]\n"
    "ds_write_addtid_b32 v164 offset:4896\n"
    "ds_write_addtid_b32 v165 offset:26112\n"
    "ds_write_addtid_b32 v166 offset:18496\n"
    "v_pk_fma_f32 v[162:163], v[162:163], v[32:33], v[158:159] op_sel:[1,1,0] op_sel_hi:[1,0,1] neg_lo:[0,1,0]\n"
    "v_pk_mul_f32 v[152:153], v[112:113], v[28:29] op_sel_hi:[0,1] neg_hi:[0,1]\n"
    "v_pk_mul_f32 v[154:155], v[134:135], v[30:31] op_sel_hi:[0,1]\n"
    "v_pk_mul_f32 v[156:157], v[160:161], v[32:33] op_sel_hi:[0,1]\n"
    "v_cvt_pk_bf16_f32 v167, v162, v163\n"
    "v_pk_fma_f32 v[112:113], v[112:113], v[28:29], v[152:153] op_sel:[1,1,0] op_sel_hi:[1,0,1]\n"
    "v_pk_fma_f32 v[136:137], v[134:135], v[30:31], v[154:155] op_sel:[1,1,0] op_sel_hi:[1,0,1] neg_lo:[0,1,0]\n"
    "v_pk_fma_f32 v[160:161], v[160:161], v[32:33], v[156:157] op_sel:[1,1,0] op_sel_hi:[1,0,1] neg_lo:[0,1,0]\n"
    "ds_write_addtid_b32 v167 offset:20672\n"
    "v_cvt_pk_bf16_f32 v164, v112, v113\n"
    "v_cvt_pk_bf16_f32 v165, v134, v135\n"
    "v_cvt_pk_bf16_f32 v166, v160, v161\n"
    "v_pk_mul_f32 v[158:159], v[162:163], v[32:33] op_sel_hi:[0,1]\n"
    "ds_write_addtid_b32 v164 offset:5440\n"
    "ds_write_addtid_b32 v165 offset:26656\n"
    "ds_write_addtid_b32 v166 offset:19040\n"
    "v_pk_fma_f32 v[162:163], v[162:163], v[32:33], v[158:159] op_sel:[1,1,0] op_sel_hi:[1,0,1] neg_lo:[0,1,0]\n"
    "v_pk_mul_f32 v[152:153], v[112:113], v[28:29] op_sel_hi:[0,1] neg_hi:[0,1]\n"
    "v_pk_mul_f32 v[154:155], v[136:137], v[30:31] op_sel_hi:[0,1]\n"
    "v_pk_mul_f32 v[156:157], v[124:125], v[34:35] op_sel_hi:[0,1]\n"
    "v_cvt_pk_bf16_f32 v167, v162, v163\n"
    "v_pk_fma_f32 v[112:113], v[112:113], v[28:29], v[152:153] op_sel:[1,1,0] op_sel_hi:[1,0,1]\n"
    "v_pk_fma_f32 v[138:139], v[136:137], v[30:31], v[154:155] op_sel:[1,1,0] op_sel_hi:[1,0,1] neg_lo:[0,1,0]\n"
    "v_pk_fma_f32 v[160:161], v[124:125], v[34:35], v[156:157] op_sel:[1,1,0] op_sel_hi:[1,0,1] neg_lo:[0,1,0]\n"
    "ds_write_addtid_b32 v167 offset:21216\n"
    "v_cvt_pk_bf16_f32 v164, v112, v113\n"
    "v_cvt_pk_bf16_f32 v165, v136, v137\n"
    "v_cvt_pk_bf16_f32 v166, v160, v161\n"
    "v_pk_mul_f32 v[158:159], v[126:127], v[34:35] op_sel_hi:[0,1]\n"
    "ds_write_addtid_b32 v164 offset:5984\n"
    "ds_write_addtid_b32 v165 offset:27200\n"
    "ds_write_addtid_b32 v166 offset:22304\n"
    "v_pk_fma_f32 v[162:163], v[126:127], v[34:35], v[158:159] op_sel:[1,1,0] op_sel_hi:[1,0,1] neg_lo:[0,1,0]\n"
    "v_pk_mul_f32 v[152:153], v[112:113], v[28:29] op_sel_hi:[0,1] neg_hi:[0,1]\n"
    "v_pk_mul_f32 v[154:155], v[138:139], v[30:31] op_sel_hi:[0,1]\n"
    "v_pk_mul_f32 v[156:157], v[128:129], v[34:35] op_sel_hi:[0,1]\n"
    "v_cvt_pk_bf16_f32 v167, v162, v163\n"
    "v_pk_fma_f32 v[112:113], v[112:113], v[28:29], v[152:153] op_sel:[1,1,0] op_sel_hi:[1,0,1]\n"
    "v_pk_fma_f32 v[140:141], v[138:139], v[30:31], v[154:155] op_sel:[1,1,0] op_sel_hi:[1,0,1] neg_lo:[0,1,0]\n"
    "v_pk_fma_f32 v[160:161], v[128:129], v[34:35], v[156:157] op_sel:[1,1,0] op_sel_hi:[1,0,1] neg_lo:[0,1,0]\n"
    "ds_write_addtid_b32 v167 offset:23392\n"
    "v_cvt_pk_bf16_f32 v164, v112, v113\n"
    "v_cvt_pk_bf16_f32 v165, v138, v139\n"
    "v_cvt_pk_bf16_f32 v166, v160, v161\n"
    "v_pk_mul_f32 v[158:159], v[130:131], v[34:35] op_sel_hi:[0,1]\n"
    "ds_write_addtid_b32 v164 offset:6528\n"
    "ds_write_addtid_b32 v165 offset:27744\n"
    "ds_write_addtid_b32 v166 offset:24480\n"
    "v_pk_fma_f32 v[162:163], v[130:131], v[34:35], v[158:159] op_sel:[1,1,0] op_sel_hi:[1,0,1] neg_lo:[0,1,0]\n"
    "v_pk_mul_f32 v[152:153], v[112:113], v[28:29] op_sel_hi:[0,1] neg_hi:[0,1]\n"
    "v_pk_mul_f32 v[154:155], v[140:141], v[30:31] op_sel_hi:[0,1]\n"
    "v_cvt_pk_bf16_f32 v167, v162, v163\n"
    "v_pk_fma_f32 v[112:113], v[112:113], v[28:29], v[152:153] op_sel:[1,1,0] op_sel_hi:[1,0,1]\n"
    "v_pk_fma_f32 v[142:143], v[140:141], v[30:31], v[154:155] op_sel:[1,1,0] op_sel_hi:[1,0,1] neg_lo:[0,1,0]\n"
    "ds_write_addtid_b32 v167 offset:25568\n"
    "v_cvt_pk_bf16_f32 v164, v112, v113\n"
    "v_cvt_pk_bf16_f32 v165, v140, v141\n"
    "ds_write_addtid_b32 v164 offset:7072\n"
    "ds_write_addtid_b32 v165 offset:28288\n"
    "v_pk_mul_f32 v[152:153], v[112:113], v[28:29] op_sel_hi:[0,1] neg_hi:[0,1]\n"
    "v_pk_mul_f32 v[154:155], v[142:143], v[30:31] op_sel_hi:[0,1]\n"
    "v_pk_fma_f32 v[112:113], v[112:113], v[28:29], v[152:153] op_sel:[1,1,0] op_sel_hi:[1,0,1]\n"
    "v_pk_fma_f32 v[144:145], v[142:143], v[30:31], v[154:155] op_sel:[1,1,0] op_sel_hi:[1,0,1] neg_lo:[0,1,0]\n"
    "v_cvt_pk_bf16_f32 v164, v112, v113\n"
    "v_cvt_pk_bf16_f32 v165, v142, v143\n"
    "ds_write_addtid_b32 v164 offset:7616\n"
    "ds_write_addtid_b32 v165 offset:28832\n"
    "v_pk_mul_f32 v[152:153], v[112:113], v[28:29] op_sel_hi:[0,1] neg_hi:[0,1]\n"
    "v_pk_mul_f32 v[154:155], v[144:145], v[30:31] op_sel_hi:[0,1]\n"
    "v_pk_fma_f32 v[112:113], v[112:113], v[28:29], v[152:153] op_sel:[1,1,0] op_sel_hi:[1,0,1]\n"
    "v_pk_fma_f32 v[146:147], v[144:145], v[30:31], v[154:155] op_sel:[1,1,0] op_sel_hi:[1,0,1] neg_lo:[0,1,0]\n"
    "v_cvt_pk_bf16_f32 v164, v112, v113\n"
    "v_cvt_pk_bf16_f32 v165, v144, v145\n"
    "ds_write_addtid_b32 v164 offset:8160\n"
    "ds_write_addtid_b32 v165 offset:29376\n"
    "v_pk_mul_f32 v[154:155], v[146:147], v[30:31] op_sel_hi:[0,1]\n"
    "s_nop 0\n"
    "v_pk_fma_f32 v[148:149], v[146:147], v[30:31], v[154:155] op_sel:[1,1,0] op_sel_hi:[1,0,1] neg_lo:[0,1,0]\n"
    "v_cvt_pk_bf16_f32 v165, v146, v147\n"
    "s_nop 0\n"
    "ds_write_addtid_b32 v165 offset:29920\n"
    "v_cvt_pk_bf16_f32 v165, v148, v149\n"
    "s_nop 0\n"
    "ds_write_addtid_b32 v165 offset:30464\n"
    "s_mov_b32 s4, %[cST]\n"
    "s_add_u32 s5, s4, 4096\n"
    "s_add_u32 s6, s4, 8192\n"
    "s_add_u32 s7, s4, 12288\n"
    "s_lshl4_add_u32 s28, s51, %[cMOD]\n"
    "s_lshl_b32 s24, s90, 10\n"
    "s_add_u32 s24, s24, %[cMOD]\n"
    "s_add_u32 s25, s24, 24576\n"
    "s_add_u32 s26, s25, 24576\n"
    "s_add_u32 s27, s26, 24576\n"
    "buffer_load_dwordx4 v[224:227], v84, s[40:43], s4 offen sc1\n"
    "buffer_load_dwordx4 v[228:231], v84, s[40:43], s6 offen sc1\n"
    "buffer_load_dwordx4 v[232:235], v84, s[40:43], s5 offen sc1\n"
    "buffer_load_dwordx4 v[236:239], v84, s[40:43], s7 offen sc1\n"
    "buffer_load_dwordx4 v[16:19], v24, s[40:43], s28 offen sc1\n"
    "buffer_load_dwordx4 v[208:211], v81, s[40:43], s24 offen sc1\n"
    "buffer_load_dwordx4 v[212:215], v81, s[40:43], s25 offen sc1\n"
    "buffer_load_dwordx4 v[216:219], v81, s[40:43], s26 offen sc1\n"
    "buffer_load_dwordx4 v[220:223], v81, s[40:43], s27 offen sc1\n"
    "s_waitcnt lgkmcnt(0)\n"
    "s_barrier\n"
    "ds_read_b128 v[96:99], v39 offset:0\n"
    "v_sub_u32 v28, 0xff, %[tid]\n"
    "ds_read_b128 v[128:131], v40 offset:8704\n"
    "v_lshlrev_b32 v46, 4, v28\n"
    "ds_read_b128 v[160:163], v39 offset:26112\n"
    "v_lshlrev_b32 v47, 4, %[tid]\n"
    "ds_read_b128 v[100:103], v39 offset:64\n"
    "v_lshlrev_b32 v45, 3, %[tid]\n"
    "ds_read_b128 v[132:135], v40 offset:8768\n"
    "v_add_u32 v46, 62030, v46\n"
    "ds_read_b128 v[164:167], v39 offset:26176\n"
    "v_add_u32 v45, 70848, v45\n"
    "ds_read_b128 v[104:107], v39 offset:128\n"
    "v_cmp_gt_u32 vcc, 32, v28\n"
    "v_cmp_gt_u32 s[70:71], 64, v28\n"
    "v_cmp_gt_u32 s[72:73], 33, v28\n"
    "v_mov_b32 v66, 0x7f\n"
    "ds_read_b128 v[136:139], v40 offset:8832\n"
    "v_cmp_gt_u32 s[68:69], v28, v66\n"
    "v_mov_b32 v4, 0x01000504\n"
    "ds_read_b128 v[168:171], v39 offset:26240\n"
    "v_mov_b32 v5, 0x05040504\n"
    "ds_read_b128 v[108:111], v39 offset:192\n"
    "v_mov_b32 v6, 0x01000100\n"
    "ds_read_b128 v[140:143], v40 offset:8896\n"
    "v_mov_b32 v7, 0x01000302\n"
    "ds_read_b128 v[172:175], v39 offset:26304\n"
    "v_and_b32 v66, 1, v28\n"
    "ds_read_b128 v[112:115], v39 offset:256\n"
    "v_cndmask_b32 v49, v5, v4, s[70:71]\n"
    "ds_read_b128 v[144:147], v40 offset:8960\n"
    "v_cndmask_b32 v51, v4, v6, s[68:69]\n"
    "ds_read_b128 v[176:179], v39 offset:26368\n"
    "v_cmp_eq_u32 s[74:75], 1, v66\n"
    "v_cndmask_b32 v49, v49, v7, vcc\n"
    "v_cndmask_b32 v51, v51, v7, vcc\n"
    "v_cndmask_b32 v52, v6, v7, vcc\n"
    "v_cndmask_b32 v67, v4, v5, s[74:75]\n"
    "v_lshrrev_b32 v48, 2, v65\n"
    "v_cndmask_b32 v50, v52, v67, s[68:69]\n"
    "v_mul_u32_u24 v48, 4160, v48\n"
    "v_and_b32 v66, 3, v65\n"
    "v_lshl_add_u32 v48, v66, 4, v48\n"
    "v_min_u32 v29, 0xdf, %[tid]\n"
    "s_and_b32 s54, s91, 1\n"
    "s_waitcnt lgkmcnt(12)\n"
    "v_mfma_f32_16x16x32_bf16 v[88:91], v[128:131], v[96:99], 0\n"
    "ds_read_b128 v[116:119], v39 offset:320\n"
    "v_mfma_f32_16x16x32_bf16 v[92:95], v[160:163], v[96:99], 0\n"
    "ds_read_b128 v[148:151], v40 offset:9024\n"
    "ds_read_b128 v[180:183], v39 offset:26432\n"
    "s_mul_i32 s54, s54, 56\n"
    "v_mul_u32_u24 v30, 0x493, v29\n"
    "v_lshrrev_b32 v30, 16, v30\n"
    "v_mul_u32_u24 v31, 56, v30\n"
    "v_sub_u32 v31, v29, v31\n"
    "s_waitcnt lgkmcnt(12)\n"
    "v_mfma_f32_16x16x32_bf16 v[88:91], v[132:135], v[100:103], v[88:91]\n"
    "ds_read_b128 v[120:123], v39 offset:384\n"
    "v_mfma_f32_16x16x32_bf16 v[92:95], v[164:167], v[100:103], v[92:95]\n"
    "ds_read_b128 v[152:155], v40 offset:9088\n"
    "ds_read_b128 v[184:187], v39 offset:26496\n"
    "v_add_u32 v66, s54, v31\n"
    "v_lshrrev_b32 v67, 4, v66\n"
    "v_and_b32 v66, 15, v66\n"
    "v_lshlrev_b32 v77, 15, v67\n"
    "v_lshl_add_u32 v77, v66, 4, v77\n"
    "s_waitcnt lgkmcnt(12)\n"
    "v_mfma_f32_16x16x32_bf16 v[88:91], v[136:139], v[104:107], v[88:91]\n"
    "ds_read_b128 v[124:127], v39 offset:448\n"
    "v_mfma_f32_16x16x32_bf16 v[92:95], v[168:171], v[104:107], v[92:95]\n"
    "ds_read_b128 v[156:159], v40 offset:9152\n"
    "ds_read_b128 v[188:191], v39 offset:26560\n"
    "v_lshl_add_u32 v77, v30, 9, v77\n"
    "v_lshl_add_u32 v79, v67, 5, v66\n"
    "v_lshlrev_b32 v79, 2, v79\n"
    "v_mul_u32_u24 v66, 14336, v30\n"
    "v_add_u32 v79, v79, v66\n"
    "s_waitcnt lgkmcnt(12)\n"
    "v_mfma_f32_16x16x32_bf16 v[88:91], v[140:143], v[108:111], v[88:91]\n"
    "v_mfma_f32_16x16x32_bf16 v[92:95], v[172:175], v[108:111], v[92:95]\n"
    "v_and_b32 v66, 3, v31\n"
    "v_and_b32 v67, 60, v31\n"
    "v_mul_u32_u24 v78, 1056, v66\n"
    "v_add_u32 v78, v78, v67\n"
    "v_mul_u32_u24 v76, 4128, v66\n"
    "s_waitcnt lgkmcnt(9)\n"
    "v_mfma_f32_16x16x32_bf16 v[88:91], v[144:147], v[112:115], v[88:91]\n"
    "v_mfma_f32_16x16x32_bf16 v[92:95], v[176:179], v[112:115], v[92:95]\n"
    "v_add_u32 v76, v76, v67\n"
    "v_mad_u32_u24 v76, v30, 56, v76\n"
    "v_add_u32 v76, 81920, v76\n"
    "v_add_u32 v74, 224, v76\n"
    "v_add_u32 v75, 448, v76\n"
    "s_waitcnt lgkmcnt(6)\n"
    "v_mfma_f32_16x16x32_bf16 v[88:91], v[148:151], v[116:119], v[88:91]\n"
    "v_mfma_f32_16x16x32_bf16 v[92:95], v[180:183], v[116:119], v[92:95]\n"
    "v_add_u32 v58, 672, v76\n"
    "v_and_b32 v66, 1, v30\n"
    "v_mul_u32_u24 v66, 4224, v66\n"
    "v_add_u32 v78, v78, v66\n"
    "v_add_u32 v78, 98432, v78\n"
    "s_waitcnt lgkmcnt(3)\n"
    "v_mfma_f32_16x16x32_bf16 v[88:91], v[152:155], v[120:123], v[88:91]\n"
    "v_mfma_f32_16x16x32_bf16 v[92:95], v[184:187], v[120:123], v[92:95]\n"
    "v_mov_b32 v66, 0x7ff00000\n"
    "v_cmp_gt_u32 vcc, 0xe0, %[tid]\n"
    "s_movk_i32 s55, 0x70\n"
    "v_cmp_gt_u32 s[74:75], s55, %[tid]\n"
    "s_lshr_b32 s54, s91, 1\n"
    "s_mul_i32 s92, s54, 229376\n"
    "v_cndmask_b32 v77, v66, v77, vcc\n"
    "v_cndmask_b32 v79, v66, v79, s[74:75]\n"
    "s_waitcnt lgkmcnt(0)\n"
    "v_mfma_f32_16x16x32_bf16 v[88:91], v[156:159], v[124:127], v[88:91]\n"
    "v_mfma_f32_16x16x32_bf16 v[92:95], v[188:191], v[124:127], v[92:95]\n"
    "s_lshl_b32 s55, s90, 13\n"
    "s_add_u32 s92, s92, s55\n"
    "s_lshl_b32 s55, s89, 8\n"
    "s_add_u32 s92, s92, s55\n"
    "s_add_u32 s92, s92, %[cWGU]\n"
    "s_mul_i32 s93, s54, 896\n"
    "s_mul_i32 s55, s90, 28672\n"
    "s_add_u32 s93, s93, s55\n"
    "s_lshl_b32 s55, s89, 6\n"
    "s_add_u32 s93, s93, s55\n"
    "s_add_u32 s93, s93, %[cUV]\n"
    "s_nop 6\n"
    "v_cvt_pk_bf16_f32 v34, v88, v89\n"
    "v_cvt_pk_bf16_f32 v35, v90, v91\n"
    "v_cvt_pk_bf16_f32 v66, v92, v93\n"
    "v_cvt_pk_bf16_f32 v67, v94, v95\n"
    "ds_write_b16 v41, v34\n"
    "ds_write_b16_d16_hi v42, v34\n"
    "ds_write_b16 v43, v35\n"
    "ds_write_b16_d16_hi v44, v35\n"
    "s_mov_b32 exec_hi, 1\n"
    "ds_write_b16 v38, v66 offset:2048\n"
    "s_mov_b32 exec_hi, 0\n"
    "ds_write_b16_d16_hi v38, v66 offset:2304\n"
    "ds_write_b16 v38, v67 offset:2560\n"
    "ds_write_b16_d16_hi v38, v67 offset:2816\n"
    "s_mov_b32 exec_hi, -1\n"
    "s_waitcnt vmcnt(0)\n"
    "v_xor_b32 v66, s30, v225\n"
    "v_xor_b32 v67, s31, v227\n"
    "v_bitop3_b32 v66, v229, s30, v66 bitop3:0xbe\n"
    "v_bitop3_b32 v67, v231, s31, v67 bitop3:0xbe\n"
    "v_bitop3_b32 v66, v233, s30, v66 bitop3:0xbe\n"
    "v_bitop3_b32 v67, v235, s31, v67 bitop3:0xbe\n"
    "v_bitop3_b32 v66, v237, s30, v66 bitop3:0xbe\n"
    "v_bitop3_b32 v67, v239, s31, v67 bitop3:0xbe\n"
    "v_bitop3_b32 v66, v17, s30, v66 bitop3:0xbe\n"
    "v_bitop3_b32 v67, v19, s31, v67 bitop3:0xbe\n"
    "v_bitop3_b32 v66, v209, s30, v66 bitop3:0xbe\n"
    "v_bitop3_b32 v67, v211, s31, v67 bitop3:0xbe\n"
    "v_bitop3_b32 v66, v213, s30, v66 bitop3:0xbe\n"
    "v_bitop3_b32 v67, v215, s31, v67 bitop3:0xbe\n"
    "v_bitop3_b32 v66, v217, s30, v66 bitop3:0xbe\n"
    "v_bitop3_b32 v67, v219, s31, v67 bitop3:0xbe\n"
    "v_bitop3_b32 v66, v221, s30, v66 bitop3:0xbe\n"
    "v_bitop3_b32 v67, v223, s31, v67 bitop3:0xbe\n"
    "v_or_b32 v66, v66, v67\n"
    "v_cmp_ne_u32 vcc, 0, v66\n"
    "s_cbranch_vccz .Lk_goG\n"
    "s_mov_b32 s19, 0x40000\n"
    ".Lk_rlG:\n"
    "s_sleep 4\n"
    "buffer_load_dwordx4 v[224:227], v84, s[40:43], s4 offen sc1\n"
    "buffer_load_dwordx4 v[228:231], v84, s[40:43], s6 offen sc1\n"
    "buffer_load_dwordx4 v[232:235], v84, s[40:43], s5 offen sc1\n"
    "buffer_load_dwordx4 v[236:239], v84, s[40:43], s7 offen sc1\n"
    "buffer_load_dwordx4 v[16:19], v24, s[40:43], s28 offen sc1\n"
    "buffer_load_dwordx4 v[208:211], v81, s[40:43], s24 offen sc1\n"
    "buffer_load_dwordx4 v[212:215], v81, s[40:43], s25 offen sc1\n"
    "buffer_load_dwordx4 v[216:219], v81, s[40:43], s26 offen sc1\n"
    "buffer_load_dwordx4 v[220:223], v81, s[40:43], s27 offen sc1\n"
    "s_waitcnt vmcnt(0)\n"
    "v_xor_b32 v66, s30, v225\n"
    "v_xor_b32 v67, s31, v227\n"
    "v_bitop3_b32 v66, v229, s30, v66 bitop3:0xbe\n"
    "v_bitop3_b32 v67, v231, s31, v67 bitop3:0xbe\n"
    "v_bitop3_b32 v66, v233, s30, v66 bitop3:0xbe\n"
    "v_bitop3_b32 v67, v235, s31, v67 bitop3:0xbe\n"
    "v_bitop3_b32 v66, v237, s30, v66 bitop3:0xbe\n"
    "v_bitop3_b32 v67, v239, s31, v67 bitop3:0xbe\n"
    "v_bitop3_b32 v66, v17, s30, v66 bitop3:0xbe\n"
    "v_bitop3_b32 v67, v19, s31, v67 bitop3:0xbe\n"
    "v_bitop3_b32 v66, v209, s30, v66 bitop3:0xbe\n"
    "v_bitop3_b32 v67, v211, s31, v67 bitop3:0xbe\n"
    "v_bitop3_b32 v66, v213, s30, v66 bitop3:0xbe\n"
    "v_bitop3_b32 v67, v215, s31, v67 bitop3:0xbe\n"
    "v_bitop3_b32 v66, v217, s30, v66 bitop3:0xbe\n"
    "v_bitop3_b32 v67, v219, s31, v67 bitop3:0xbe\n"
    "v_bitop3_b32 v66, v221, s30, v66 bitop3:0xbe\n"
    "v_bitop3_b32 v67, v223, s31, v67 bitop3:0xbe\n"
    "v_or_b32 v66, v66, v67\n"
    "v_cmp_ne_u32 vcc, 0, v66\n"
    "s_cbranch_vccz .Lk_goG\n"
    "s_sub_u32 s19, s19, 1\n"
    "s_cmp_lg_u32 s19, 0\n"
    "s_cbranch_scc1 .Lk_rlG\n"
    ".Lk_goG:\n"
    "buffer_load_dwordx4 v[8:11], v80, s[40:43], s60 offen sc1\n"
    "buffer_load_dwordx4 v[12:15], v80, s[40:43], s61 offen sc1\n"
    "s_waitcnt lgkmcnt(0)\n"
    "s_barrier\n"
    "s_or_b32 s54, %[bid], s48\n"
    "s_cmp_eq_u32 s54, 0\n"
    "s_mov_b32 s55, %[cEP]\n"
    "s_cselect_b32 s55, s55, 0x7ff00000\n"
    "v_mov_b32 v20, s30\n"
    "v_mov_b32 v21, s31\n"
    "s_mov_b64 exec, 1\n"
    "buffer_store_dwordx2 v[20:21], off, s[40:43], s55 sc1\n"
    "s_mov_b64 exec, -1\n"
    "ds_read_b128 v[160:163], v46 offset:2\n"
    "ds_read_u16 v164, v46 offset:18\n"
    "ds_read_u16 v165, v46 offset:0\n"
    "ds_read_b128 v[168:171], v46 offset:4162\n"
    "ds_read_u16 v172, v46 offset:4178\n"
    "ds_read_u16 v173, v46 offset:4160\n"
    "v_and_b32 v26, 15, v65\n"
    "v_lshrrev_b32 v27, 4, v65\n"
    "s_mul_i32 s54, s49, 8320\n"
    "s_lshl_b32 s55, s50, 6\n"
    "s_sub_u32 s54, s54, s55\n"
    "s_add_u32 s54, s54, 3164\n"
    "v_and_b32 v66, 1, v26\n"
    "v_mul_u32_u24 v66, 4160, v66\n"
    "v_and_b32 v67, 14, v26\n"
    "v_lshl_add_u32 v66, v67, 1, v66\n"
    "v_lshl_add_u32 v67, v27, 4, s54\n"
    "v_sub_u32 v67, v67, v66\n"
    "v_add_u32 v53, 4160, v67\n"
    "v_add_u32 v54, 3136, v67\n"
    "v_add_u32 v55, 2112, v67\n"
    "v_add_u32 v56, 1088, v67\n"
    "s_mul_i32 s54, s49, 4608\n"
    "s_sub_u32 s54, s54, s55\n"
    "s_add_u32 s54, s54, 70432\n"
    "v_lshlrev_b32 v57, 5, v26\n"
    "v_lshl_add_u32 v57, v27, 4, v57\n"
    "v_add_u32 v57, s54, v57\n"
    "v_mov_b32 v248, 0\n"
    "v_mov_b32 v249, 0\n"
    "v_mov_b32 v250, 0\n"
    "v_mov_b32 v251, 0\n"
    "s_mov_b64 exec, 0xffff\n"
    "ds_write_b128 v48, v[248:251] offset:4096\n"
    "s_mov_b64 exec, -1\n"
    "s_waitcnt lgkmcnt(4)\n"
    "v_cndmask_b32 v166, v162, v160, s[68:69]\n"
    "v_cndmask_b32 v167, v160, v161, s[70:71]\n"
    "v_perm_b32 v176, v164, v163, v49\n"
    "v_perm_b32 v179, v160, v160, v52\n"
    "v_perm_b32 v177, v164, v166, v50\n"
    "v_perm_b32 v178, v162, v167, v51\n"
    "v_cndmask_b32 v165, v160, v165, s[72:73]\n"
    "v_alignbit_b32 v180, v177, v176, 16\n"
    "ds_write_b128 v47, v[176:179] offset:0\n"
    "v_alignbit_b32 v181, v178, v177, 16\n"
    "v_alignbit_b32 v182, v179, v178, 16\n"
    "v_alignbit_b32 v183, v165, v179, 16\n"
    "ds_write_b128 v47, v[180:183] offset:4160\n"
    "s_waitcnt lgkmcnt(3)\n"
    "v_cndmask_b32 v174, v170, v168, s[68:69]\n"
    "v_cndmask_b32 v175, v168, v169, s[70:71]\n"
    "v_perm_b32 v184, v172, v171, v49\n"
    "v_perm_b32 v187, v168, v168, v52\n"
    "v_perm_b32 v185, v172, v174, v50\n"
    "v_perm_b32 v186, v170, v175, v51\n"
    "v_cndmask_b32 v173, v168, v173, s[72:73]\n"
    "v_alignbit_b32 v188, v185, v184, 16\n"
    "ds_write_b128 v47, v[184:187] offset:8320\n"
    "v_alignbit_b32 v189, v186, v185, 16\n"
    "v_alignbit_b32 v190, v187, v186, 16\n"
    "v_alignbit_b32 v191, v173, v187, 16\n"
    "ds_write_b128 v47, v[188:191] offset:12480\n"
    "s_mov_b32 s6, 0x05040100\n"
    "s_mov_b32 s7, 0x07060302\n"
    "v_perm_b32 v96, v241, v240, s6\n"
    "v_perm_b32 v100, v241, v240, s7\n"
    "v_perm_b32 v97, v243, v242, s6\n"
    "v_perm_b32 v101, v243, v242, s7\n"
    "v_perm_b32 v98, v245, v244, s6\n"
    "v_perm_b32 v102, v245, v244, s7\n"
    "v_perm_b32 v99, v247, v246, s6\n"
    "v_perm_b32 v103, v247, v246, s7\n"
    "ds_write_b128 v82, v[96:99]\n"
    "ds_write_b128 v82, v[100:103] offset:16\n"
    "s_lshl_b32 s54, s94, 16\n"
    "s_and_b32 s55, s94, 0xffff0000\n"
    "v_add_f32_dpp v16, v16, v16 quad_perm:[1,0,3,2] row_mask:0xf bank_mask:0xf\n"
    "v_add_f32_dpp v18, v18, v18 quad_perm:[1,0,3,2] row_mask:0xf bank_mask:0xf\n"
    "v_and_b32 v66, 1, %[tid]\n"
    "v_cmp_eq_u32 s[74:75], 1, v66\n"
    "v_add_f32_dpp v16, v16, v16 quad_perm:[2,3,0,1] row_mask:0xf bank_mask:0xf\n"
    "v_add_f32_dpp v18, v18, v18 quad_perm:[2,3,0,1] row_mask:0xf bank_mask:0xf\n"
    "v_cndmask_b32 v208, v208, v210, s[74:75]\n"
    "v_cvt_pk_bf16_f32 v16, v16, v18\n"
    "v_cndmask_b32 v212, v212, v214, s[74:75]\n"
    "v_lshlrev_b32 v22, 16, v16\n"
    "v_and_b32 v23, 0xffff0000, v16\n"
    "v_cndmask_b32 v216, v216, v218, s[74:75]\n"
    "v_add_f32 v16, 1.0, v22\n"
    "v_add_f32 v17, 1.0, v23\n"
    "v_cndmask_b32 v220, v220, v222, s[74:75]\n"
    "v_cvt_pk_bf16_f32 v16, v16, v17\n"
    "v_readlane_b32 s64, v22, 8\n"
    "v_readlane_b32 s66, v23, 8\n"
    "v_readlane_b32 s6, v22, 0\n"
    "v_readlane_b32 s7, v23, 0\n"
    "v_readlane_b32 s58, v16, 4\n"
    "v_add_f32 v208, v208, v212\n"
    "v_add_f32 v216, v216, v220\n"
    "v_mov_b32 v63, s64\n"
    "v_mov_b32 v64, s66\n"
    "v_mov_b32 v61, s6\n"
    "v_mov_b32 v62, s7\n"
    "s_and_b32 s59, s58, 0xffff0000\n"
    "s_lshl_b32 s58, s58, 16\n"
    "v_add_f32 v208, v208, v216\n"
    "v_mul_f32 v63, s54, v63\n"
    "v_mul_f32 v64, s55, v64\n"
    "v_cvt_pk_bf16_f32 v208, v208, v208\n"
    "s_lshl3_add_u32 s54, s51, %[cGB]\n"
    "v_cmp_gt_u32 vcc, 0x80, %[tid]\n"
    "v_lshlrev_b32 v208, 16, v208\n"
    "s_mov_b64 exec, 0x100000\n"
    "buffer_store_dwordx2 v[22:23], off, s[40:43], s54 sc1\n"
    "s_mov_b64 exec, -1\n"
    "v_add_f32 v209, 1.0, v208\n"
    "v_cvt_pk_bf16_f32 v209, v209, v209\n"
    "v_lshlrev_b32 v209, 16, v209\n"
    "v_cndmask_b32 v208, v208, v209, vcc\n"
    "ds_write_b32 v72, v208\n"
    "v_and_b32 v225, 0xffff0000, v224\n"
    "v_and_b32 v227, 0xffff0000, v226\n"
    "v_and_b32 v229, 0xffff0000, v228\n"
    "v_and_b32 v231, 0xffff0000, v230\n"
    "v_and_b32 v233, 0xffff0000, v232\n"
    "v_and_b32 v235, 0xffff0000, v234\n"
    "v_and_b32 v237, 0xffff0000, v236\n"
    "v_and_b32 v239, 0xffff0000, v238\n"
    "v_rcp_f32 v225, v225\n"
    "v_rcp_f32 v227, v227\n"
    "v_rcp_f32 v229, v229\n"
    "v_rcp_f32 v231, v231\n"
    "v_rcp_f32 v233, v233\n"
    "v_rcp_f32 v235, v235\n"
    "v_rcp_f32 v237, v237\n"
    "v_rcp_f32 v239, v239\n"
    "v_lshlrev_b32 v224, 16, v224\n"
    "v_lshlrev_b32 v226, 16, v226\n"
    "v_lshlrev_b32 v228, 16, v228\n"
    "v_lshlrev_b32 v230, 16, v230\n"
    "v_lshlrev_b32 v232, 16, v232\n"
    "v_lshlrev_b32 v234, 16, v234\n"
    "v_lshlrev_b32 v236, 16, v236\n"
    "v_lshlrev_b32 v238, 16, v238\n"
    "v_mul_f32_e64 v224, -v224, v225\n"
    "v_mul_f32_e64 v226, -v226, v227\n"
    "v_mul_f32_e64 v228, -v228, v229\n"
    "v_mul_f32_e64 v230, -v230, v231\n"
    "v_mul_f32_e64 v232, -v232, v233\n"
    "v_mul_f32_e64 v234, -v234, v235\n"
    "v_mul_f32_e64 v236, -v236, v237\n"
    "v_mul_f32_e64 v238, -v238, v239\n"
    "s_waitcnt vmcnt(3)\n"
    "v_lshlrev_b32 v104, 16, v8\n"
    "v_lshlrev_b32 v105, 16, v9\n"
    "v_lshlrev_b32 v106, 16, v10\n"
    "v_lshlrev_b32 v107, 16, v11\n"
    "v_and_b32 v108, 0xffff0000, v8\n"
    "v_and_b32 v109, 0xffff0000, v9\n"
    "v_and_b32 v110, 0xffff0000, v10\n"
    "v_and_b32 v111, 0xffff0000, v11\n"
    "v_fma_f32 v104, v104, v225, v224\n"
    "v_fma_f32 v105, v105, v227, v226\n"
    "v_fma_f32 v106, v106, v229, v228\n"
    "v_fma_f32 v107, v107, v231, v230\n"
    "v_fma_f32 v108, v108, v225, v224\n"
    "v_fma_f32 v109, v109, v227, v226\n"
    "v_fma_f32 v110, v110, v229, v228\n"
    "v_fma_f32 v111, v111, v231, v230\n"
    "v_fma_f32 v104, v104, s58, v61\n"
    "v_fma_f32 v105, v105, s58, v61\n"
    "v_fma_f32 v106, v106, s58, v61\n"
    "v_fma_f32 v107, v107, s58, v61\n"
    "v_fma_f32 v108, v108, s59, v62\n"
    "v_fma_f32 v109, v109, s59, v62\n"
    "v_fma_f32 v110, v110, s59, v62\n"
    "v_fma_f32 v111, v111, s59, v62\n"
    "v_cvt_pk_bf16_f32 v26, v104, v105\n"
    "v_cvt_pk_bf16_f32 v27, v106, v107\n"
    "v_cvt_pk_bf16_f32 v28, v108, v109\n"
    "v_cvt_pk_bf16_f32 v29, v110, v111\n"
    "ds_write_b64 v45, v[26:27] offset:0\n"
    "ds_write_b64 v45, v[28:29] offset:4608\n"
    "s_waitcnt vmcnt(2)\n"
    "v_lshlrev_b32 v112, 16, v12\n"
    "v_lshlrev_b32 v113, 16, v13\n"
    "v_lshlrev_b32 v114, 16, v14\n"
    "v_lshlrev_b32 v115, 16, v15\n"
    "v_and_b32 v116, 0xffff0000, v12\n"
    "v_and_b32 v117, 0xffff0000, v13\n"
    "v_and_b32 v118, 0xffff0000, v14\n"
    "v_and_b32 v119, 0xffff0000, v15\n"
    "v_fma_f32 v112, v112, v233, v232\n"
    "v_fma_f32 v113, v113, v235, v234\n"
    "v_fma_f32 v114, v114, v237, v236\n"
    "v_fma_f32 v115, v115, v239, v238\n"
    "v_fma_f32 v116, v116, v233, v232\n"
    "v_fma_f32 v117, v117, v235, v234\n"
    "v_fma_f32 v118, v118, v237, v236\n"
    "v_fma_f32 v119, v119, v239, v238\n"
    "v_fma_f32 v112, v112, s58, v61\n"
    "v_fma_f32 v113, v113, s58, v61\n"
    "v_fma_f32 v114, v114, s58, v61\n"
    "v_fma_f32 v115, v115, s58, v61\n"
    "v_fma_f32 v116, v116, s59, v62\n"
    "v_fma_f32 v117, v117, s59, v62\n"
    "v_fma_f32 v118, v118, s59, v62\n"
    "v_fma_f32 v119, v119, s59, v62\n"
    "v_cvt_pk_bf16_f32 v30, v112, v113\n"
    "v_cvt_pk_bf16_f32 v31, v114, v115\n"
    "v_cvt_pk_bf16_f32 v32, v116, v117\n"
    "v_cvt_pk_bf16_f32 v33, v118, v119\n"
    "ds_write_b64 v45, v[30:31] offset:2048\n"
    "ds_write_b64 v45, v[32:33] offset:6656\n"
    "s_waitcnt lgkmcnt(0)\n"
    "s_barrier\n"
    "ds_read_b128 v[160:163], v73 offset:0\n"
    "ds_read_b128 v[164:167], v73 offset:16\n"
    "ds_read_b128 v[168:171], v73 offset:512\n"
    "ds_read_b128 v[172:175], v73 offset:528\n"
    "ds_read_b128 v[240:243], v87 offset:0\n"
    "ds_read_b128 v[244:247], v87 offset:3584\n"
    "v_mov_b32 v66, 0x3f803f80\n"
    "v_mov_b32 v88, 0\n"
    "v_mov_b32 v89, 0\n"
    "v_mov_b32 v90, 0\n"
    "v_mov_b32 v91, 0\n"
    "s_lshl_b32 s54, s48, 8\n"
    "s_add_u32 m0, s54, 49152\n"
    "v_lshlrev_b32 v176, 16, v192\n"
    "v_and_b32 v177, 0xffff0000, v192\n"
    "v_lshlrev_b32 v178, 16, v194\n"
    "v_and_b32 v179, 0xffff0000, v194\n"
    "ds_read_b128 v[128:131], v57 offset:384\n"
    "v_lshlrev_b32 v180, 16, v193\n"
    "v_and_b32 v181, 0xffff0000, v193\n"
    "v_lshlrev_b32 v182, 16, v195\n"
    "v_and_b32 v183, 0xffff0000, v195\n"
    "ds_read_b128 v[132:135], v57 offset:896\n"
    "s_waitcnt lgkmcnt(2)\n"
    "buffer_store_dwordx4 v[240:243], v83, s[40:43], s34 offen offset:0 sc1\n"
    "buffer_store_dwordx4 v[244:247], v83, s[40:43], s34 offen offset:3584 sc1\n"
    "v_pk_mul_f32 v[184:185], v[160:161], v[176:177] op_sel_hi:[0,1]\n"
    "v_pk_mul_f32 v[186:187], v[160:161], v[178:179] op_sel:[1,0]\n"
    "v_pk_mul_f32 v[188:189], v[160:161], v[180:181] op_sel_hi:[0,1]\n"
    "v_pk_mul_f32 v[190:191], v[160:161], v[182:183] op_sel:[1,0]\n"
    "ds_read_b128 v[136:139], v57 offset:1408\n"
    "v_pk_fma_f32 v[92:93], v[168:169], v[176:177], 0 op_sel_hi:[0,1,0]\n"
    "v_pk_fma_f32 v[94:95], v[168:169], v[180:181], 0 op_sel_hi:[0,1,0]\n"
    "v_cvt_pk_bf16_f32 v208, v184, v186\n"
    "v_cvt_pk_bf16_f32 v212, v185, v187\n"
    "ds_read_b128 v[140:143], v57 offset:1920\n"
    "v_cvt_pk_bf16_f32 v216, v188, v190\n"
    "v_cvt_pk_bf16_f32 v220, v189, v191\n"
    "v_pk_fma_f32 v[92:93], v[168:169], v[178:179], v[92:93] op_sel:[1,0,0]\n"
    "v_pk_fma_f32 v[94:95], v[168:169], v[182:183], v[94:95] op_sel:[1,0,0]\n"
    "ds_read_b128 v[144:147], v57 offset:2432\n"
    "v_dot2c_f32_bf16 v88, v66, v208\n"
    "v_dot2c_f32_bf16 v89, v66, v212\n"
    "v_dot2c_f32_bf16 v90, v66, v216\n"
    "v_dot2c_f32_bf16 v91, v66, v220\n"
    "ds_read_b128 v[148:151], v57 offset:2944\n"
    "ds_write_addtid_b32 v208 offset:32768\n"
    "ds_write_addtid_b32 v212 offset:36896\n"
    "ds_write_addtid_b32 v216 offset:41024\n"
    "ds_write_addtid_b32 v220 offset:45152\n"
    "v_lshlrev_b32 v176, 16, v196\n"
    "v_and_b32 v177, 0xffff0000, v196\n"
    "v_lshlrev_b32 v178, 16, v198\n"
    "v_and_b32 v179, 0xffff0000, v198\n"
    "ds_read_b128 v[152:155], v57 offset:3456\n"
    "v_lshlrev_b32 v180, 16, v197\n"
    "v_and_b32 v181, 0xffff0000, v197\n"
    "v_lshlrev_b32 v182, 16, v199\n"
    "v_and_b32 v183, 0xffff0000, v199\n"
    "ds_read_b128 v[156:159], v57 offset:3968\n"
    "v_pk_mul_f32 v[184:185], v[162:163], v[176:177] op_sel_hi:[0,1]\n"
    "v_pk_mul_f32 v[186:187], v[162:163], v[178:179] op_sel:[1,0]\n"
    "v_pk_mul_f32 v[188:189], v[162:163], v[180:181] op_sel_hi:[0,1]\n"
    "v_pk_mul_f32 v[190:191], v[162:163], v[182:183] op_sel:[1,0]\n"
    "ds_read2_b32 v[96:97], v53 offset0:224 offset1:225\n"
    "v_pk_fma_f32 v[92:93], v[170:171], v[176:177], v[92:93] op_sel_hi:[0,1,1]\n"
    "v_pk_fma_f32 v[94:95], v[170:171], v[180:181], v[94:95] op_sel_hi:[0,1,1]\n"
    "v_cvt_pk_bf16_f32 v210, v184, v186\n"
    "v_cvt_pk_bf16_f32 v214, v185, v187\n"
    "ds_read2_b32 v[98:99], v53 offset0:226 offset1:227\n"
    "v_cvt_pk_bf16_f32 v218, v188, v190\n"
    "v_cvt_pk_bf16_f32 v222, v189, v191\n"
    "v_pk_fma_f32 v[92:93], v[170:171], v[178:179], v[92:93] op_sel:[1,0,0]\n"
    "v_pk_fma_f32 v[94:95], v[170:171], v[182:183], v[94:95] op_sel:[1,0,0]\n"
    "ds_read2_b32 v[100:101], v53 offset0:96 offset1:97\n"
    "v_dot2c_f32_bf16 v88, v66, v210\n"
    "v_dot2c_f32_bf16 v89, v66, v214\n"
    "v_dot2c_f32_bf16 v90, v66, v218\n"
    "v_dot2c_f32_bf16 v91, v66, v222\n"
    "ds_read2_b32 v[102:103], v53 offset0:98 offset1:99\n"
    "ds_write_addtid_b32 v210 offset:34816\n"
    "ds_write_addtid_b32 v214 offset:38944\n"
    "ds_write_addtid_b32 v218 offset:43072\n"
    "ds_write_addtid_b32 v222 offset:47200\n"
    "v_lshlrev_b32 v176, 16, v200\n"
    "v_and_b32 v177, 0xffff0000, v200\n"
    "v_lshlrev_b32 v178, 16, v202\n"
    "v_and_b32 v179, 0xffff0000, v202\n"
    "ds_read2_b32 v[104:105], v54 offset0:224 offset1:225\n"
    "v_lshlrev_b32 v180, 16, v201\n"
    "v_and_b32 v181, 0xffff0000, v201\n"
    "v_lshlrev_b32 v182, 16, v203\n"
    "v_and_b32 v183, 0xffff0000, v203\n"
    "ds_read2_b32 v[106:107], v54 offset0:226 offset1:227\n"
    "v_pk_mul_f32 v[184:185], v[164:165], v[176:177] op_sel_hi:[0,1]\n"
    "v_pk_mul_f32 v[186:187], v[164:165], v[178:179] op_sel:[1,0]\n"
    "v_pk_mul_f32 v[188:189], v[164:165], v[180:181] op_sel_hi:[0,1]\n"
    "v_pk_mul_f32 v[190:191], v[164:165], v[182:183] op_sel:[1,0]\n"
    "ds_read2_b32 v[108:109], v54 offset0:96 offset1:97\n"
    "v_pk_fma_f32 v[92:93], v[172:173], v[176:177], v[92:93] op_sel_hi:[0,1,1]\n"
    "v_pk_fma_f32 v[94:95], v[172:173], v[180:181], v[94:95] op_sel_hi:[0,1,1]\n"
    "v_cvt_pk_bf16_f32 v209, v184, v186\n"
    "v_cvt_pk_bf16_f32 v213, v185, v187\n"
    "ds_read2_b32 v[110:111], v54 offset0:98 offset1:99\n"
    "v_cvt_pk_bf16_f32 v217, v188, v190\n"
    "v_cvt_pk_bf16_f32 v221, v189, v191\n"
    "v_pk_fma_f32 v[92:93], v[172:173], v[178:179], v[92:93] op_sel:[1,0,0]\n"
    "v_pk_fma_f32 v[94:95], v[172:173], v[182:183], v[94:95] op_sel:[1,0,0]\n"
    "ds_read2_b32 v[112:113], v55 offset0:224 offset1:225\n"
    "v_dot2c_f32_bf16 v88, v66, v209\n"
    "v_dot2c_f32_bf16 v89, v66, v213\n"
    "v_dot2c_f32_bf16 v90, v66, v217\n"
    "v_dot2c_f32_bf16 v91, v66, v221\n"
    "ds_read2_b32 v[114:115], v55 offset0:226 offset1:227\n"
    "ds_write_addtid_b32 v209 offset:33792\n"
    "ds_write_addtid_b32 v213 offset:37920\n"
    "ds_write_addtid_b32 v217 offset:42048\n"
    "ds_write_addtid_b32 v221 offset:46176\n"
    "v_lshlrev_b32 v176, 16, v204\n"
    "v_and_b32 v177, 0xffff0000, v204\n"
    "v_lshlrev_b32 v178, 16, v206\n"
    "v_and_b32 v179, 0xffff0000, v206\n"
    "ds_read2_b32 v[116:117], v55 offset0:96 offset1:97\n"
    "v_lshlrev_b32 v180, 16, v205\n"
    "v_and_b32 v181, 0xffff0000, v205\n"
    "v_lshlrev_b32 v182, 16, v207\n"
    "v_and_b32 v183, 0xffff0000, v207\n"
    "ds_read2_b32 v[118:119], v55 offset0:98 offset1:99\n"
    "v_pk_mul_f32 v[184:185], v[166:167], v[176:177] op_sel_hi:[0,1]\n"
    "v_pk_mul_f32 v[186:187], v[166:167], v[178:179] op_sel:[1,0]\n"
    "v_pk_mul_f32 v[188:189], v[166:167], v[180:181] op_sel_hi:[0,1]\n"
    "v_pk_mul_f32 v[190:191], v[166:167], v[182:183] op_sel:[1,0]\n"
    "ds_read2_b32 v[120:121], v56 offset0:224 offset1:225\n"
    "v_pk_fma_f32 v[92:93], v[174:175], v[176:177], v[92:93] op_sel_hi:[0,1,1]\n"
    "v_pk_fma_f32 v[94:95], v[174:175], v[180:181], v[94:95] op_sel_hi:[0,1,1]\n"
    "v_cvt_pk_bf16_f32 v211, v184, v186\n"
    "v_cvt_pk_bf16_f32 v215, v185, v187\n"
    "ds_read2_b32 v[122:123], v56 offset0:226 offset1:227\n"
    "v_cvt_pk_bf16_f32 v219, v188, v190\n"
    "v_cvt_pk_bf16_f32 v223, v189, v191\n"
    "v_pk_fma_f32 v[92:93], v[174:175], v[178:179], v[92:93] op_sel:[1,0,0]\n"
    "v_pk_fma_f32 v[94:95], v[174:175], v[182:183], v[94:95] op_sel:[1,0,0]\n"
    "ds_read2_b32 v[124:125], v56 offset0:96 offset1:97\n"
    "v_dot2c_f32_bf16 v88, v66, v211\n"
    "v_dot2c_f32_bf16 v89, v66, v215\n"
    "v_dot2c_f32_bf16 v90, v66, v219\n"
    "v_dot2c_f32_bf16 v91, v66, v223\n"
    "ds_read2_b32 v[126:127], v56 offset0:98 offset1:99\n"
    "ds_write_addtid_b32 v211 offset:35840\n"
    "ds_write_addtid_b32 v215 offset:39968\n"
    "ds_write_addtid_b32 v219 offset:44096\n"
    "ds_write_addtid_b32 v223 offset:48224\n"
    "ds_write_addtid_b32 v92 offset:53504\n"
    "ds_write_addtid_b32 v93 offset:54560\n"
    "ds_write_addtid_b32 v94 offset:55616\n"
    "ds_write_addtid_b32 v95 offset:56672\n"
    "ds_write_addtid_b32 v88 offset:49280\n"
    "ds_write_addtid_b32 v89 offset:50336\n"
    "ds_write_addtid_b32 v90 offset:51392\n"
    "ds_write_addtid_b32 v91 offset:52448\n"
    "s_waitcnt lgkmcnt(15)\n"
    "v_mfma_f32_16x16x32_bf16 v[224:227], v[96:99], v[128:131], 0\n"
    "v_mfma_f32_16x16x32_bf16 v[228:231], v[96:99], v[132:135], 0\n"
    "v_mfma_f32_16x16x32_bf16 v[232:235], v[96:99], v[136:139], 0\n"
    "ds_read_b128 v[192:195], v57 offset:256\n"
    "v_mfma_f32_16x16x32_bf16 v[236:239], v[96:99], v[140:143], 0\n"
    "v_mfma_f32_16x16x32_bf16 v[240:243], v[96:99], v[144:147], 0\n"
    "ds_read2_b32 v[160:161], v53 offset0:192 offset1:193\n"
    "v_mfma_f32_16x16x32_bf16 v[244:247], v[96:99], v[148:151], 0\n"
    "ds_read2_b32 v[162:163], v53 offset0:194 offset1:195\n"
    "v_mfma_f32_16x16x32_bf16 v[248:251], v[96:99], v[152:155], 0\n"
    "ds_read2_b32 v[164:165], v53 offset0:64 offset1:65\n"
    "s_waitcnt lgkmcnt(4)\n"
    "s_barrier\n"
    "v_mfma_f32_16x16x32_bf16 v[252:255], v[96:99], v[156:159], 0\n"
    "ds_read2st64_b32 v[36:37], v76 offset0:0 offset1:4\n"
    "ds_read2st64_b32 v[38:39], v76 offset0:8 offset1:12\n"
    "v_mfma_f32_16x16x32_bf16 v[228:231], v[100:103], v[128:131], v[228:231]\n"
    "ds_read2_b32 v[166:167], v53 offset0:66 offset1:67\n"
    "ds_read2st64_b32 v[40:41], v74 offset0:0 offset1:4\n"
    "v_mfma_f32_16x16x32_bf16 v[232:235], v[100:103], v[132:135], v[232:235]\n"
    "ds_read2_b32 v[168:169], v54 offset0:192 offset1:193\n"
    "v_mfma_f32_16x16x32_bf16 v[236:239], v[100:103], v[136:139], v[236:239]\n"
    "ds_read2_b32 v[170:171], v54 offset0:194 offset1:195\n"
    "ds_read2st64_b32 v[42:43], v74 offset0:8 offset1:12\n"
    "v_mfma_f32_16x16x32_bf16 v[240:243], v[100:103], v[140:143], v[240:243]\n"
    "ds_read2st64_b32 v[44:45], v75 offset0:0 offset1:4\n"
    "ds_read2st64_b32 v[46:47], v75 offset0:8 offset1:12\n"
    "v_mfma_f32_16x16x32_bf16 v[244:247], v[100:103], v[144:147], v[244:247]\n"
    "ds_read2_b32 v[172:173], v54 offset0:64 offset1:65\n"
    "ds_read2st64_b32 v[48:49], v58 offset0:0 offset1:4\n"
    "v_mfma_f32_16x16x32_bf16 v[248:251], v[100:103], v[148:151], v[248:251]\n"
    "ds_read2_b32 v[174:175], v54 offset0:66 offset1:67\n"
    "v_mfma_f32_16x16x32_bf16 v[252:255], v[100:103], v[152:155], v[252:255]\n"
    "ds_read2_b32 v[176:177], v55 offset0:192 offset1:193\n"
    "ds_read2st64_b32 v[50:51], v58 offset0:8 offset1:12\n"
    "v_mfma_f32_16x16x32_bf16 v[232:235], v[104:107], v[128:131], v[232:235]\n"
    "s_waitcnt lgkmcnt(12)\n"
    "buffer_store_dwordx4 v[36:39], v77, s[40:43], s92 offen offset:0 sc1\n"
    "s_waitcnt lgkmcnt(7)\n"
    "buffer_store_dwordx4 v[40:43], v77, s[40:43], s92 offen offset:2048 sc1\n"
    "v_mfma_f32_16x16x32_bf16 v[236:239], v[104:107], v[132:135], v[236:239]\n"
    "ds_read2_b32 v[178:179], v55 offset0:194 offset1:195\n"
    "s_add_u32 s92, s92, 4096\n"
    "v_mfma_f32_16x16x32_bf16 v[240:243], v[104:107], v[136:139], v[240:243]\n"
    "ds_read2_b32 v[180:181], v55 offset0:64 offset1:65\n"
    "v_mfma_f32_16x16x32_bf16 v[244:247], v[104:107], v[140:143], v[244:247]\n"
    "ds_read2_b32 v[182:183], v55 offset0:66 offset1:67\n"
    "s_waitcnt lgkmcnt(8)\n"
    "buffer_store_dwordx4 v[44:47], v77, s[40:43], s92 offen offset:0 sc1\n"
    "v_mfma_f32_16x16x32_bf16 v[248:251], v[104:107], v[144:147], v[248:251]\n"
    "s_waitcnt lgkmcnt(3)\n"
    "buffer_store_dwordx4 v[48:51], v77, s[40:43], s92 offen offset:2048 sc1\n"
    "ds_read2_b32 v[88:89], v78 offset0:0 offset1:14\n"
    "v_mfma_f32_16x16x32_bf16 v[252:255], v[104:107], v[148:151], v[252:255]\n"
    "ds_read2_b32 v[184:185], v56 offset0:192 offset1:193\n"
    "ds_read2_b32 v[90:91], v78 offset0:28 offset1:42\n"
    "v_mfma_f32_16x16x32_bf16 v[236:239], v[108:111], v[128:131], v[236:239]\n"
    "ds_read2_b32 v[186:187], v56 offset0:194 offset1:195\n"
    "v_mfma_f32_16x16x32_bf16 v[240:243], v[108:111], v[132:135], v[240:243]\n"
    "ds_read2_b32 v[188:189], v56 offset0:64 offset1:65\n"
    "ds_read2_b32 v[92:93], v78 offset0:56 offset1:70\n"
    "v_mfma_f32_16x16x32_bf16 v[244:247], v[108:111], v[136:139], v[244:247]\n"
    "ds_read2_b32 v[94:95], v78 offset0:84 offset1:98\n"
    "s_waitcnt lgkmcnt(0)\n"
    "v_add_f32 v88, v88, v92\n"
    "v_mfma_f32_16x16x32_bf16 v[248:251], v[108:111], v[140:143], v[248:251]\n"
    "ds_read2_b32 v[190:191], v56 offset0:66 offset1:67\n"
    "v_add_f32 v89, v89, v93\n"
    "v_mfma_f32_16x16x32_bf16 v[252:255], v[108:111], v[144:147], v[252:255]\n"
    "ds_read_b128 v[196:199], v57 offset:768\n"
    "v_mfma_f32_16x16x32_bf16 v[240:243], v[112:115], v[128:131], v[240:243]\n"
    "ds_read_b128 v[200:203], v57 offset:1280\n"
    "v_add_f32 v90, v90, v94\n"
    "v_mfma_f32_16x16x32_bf16 v[244:247], v[112:115], v[132:135], v[244:247]\n"
    "v_add_f32 v91, v91, v95\n"
    "v_add_f32 v88, v88, v90\n"
    "v_mfma_f32_16x16x32_bf16 v[248:251], v[112:115], v[136:139], v[248:251]\n"
    "ds_read_b128 v[204:207], v57 offset:1792\n"
    "v_add_f32 v89, v89, v91\n"
    "v_mfma_f32_16x16x32_bf16 v[252:255], v[112:115], v[140:143], v[252:255]\n"
    "ds_read_b128 v[208:211], v57 offset:2304\n"
    "v_mfma_f32_16x16x32_bf16 v[244:247], v[116:119], v[128:131], v[244:247]\n"
    "ds_read_b128 v[212:215], v57 offset:2816\n"
    "v_add_f32 v4, v88, v89\n"
    "v_mfma_f32_16x16x32_bf16 v[248:251], v[116:119], v[132:135], v[248:251]\n"
    "ds_read2_b32 v[88:89], v78 offset0:112 offset1:126\n"
    "ds_read2_b32 v[90:91], v78 offset0:140 offset1:154\n"
    "v_mfma_f32_16x16x32_bf16 v[252:255], v[116:119], v[136:139], v[252:255]\n"
    "ds_read_b128 v[216:219], v57 offset:3328\n"
    "ds_read2_b32 v[92:93], v78 offset0:168 offset1:182\n"
    "v_mfma_f32_16x16x32_bf16 v[248:251], v[120:123], v[128:131], v[248:251]\n"
    "ds_read_b128 v[220:223], v57 offset:3840\n"
    "v_mfma_f32_16x16x32_bf16 v[252:255], v[120:123], v[132:135], v[252:255]\n"
    "ds_read2_b32 v[94:95], v78 offset0:196 offset1:210\n"
    "s_waitcnt lgkmcnt(0)\n"
    "v_add_f32 v88, v88, v92\n"
    "v_mfma_f32_16x16x32_bf16 v[252:255], v[124:127], v[128:131], v[252:255]\n"
    "v_add_f32 v89, v89, v93\n"
    "v_add_f32 v90, v90, v94\n"
    "v_mfma_f32_16x16x32_bf16 v[224:227], v[160:163], v[192:195], v[224:227]\n"
    "v_add_f32 v91, v91, v95\n"
    "v_add_f32 v88, v88, v90\n"
    "v_mfma_f32_16x16x32_bf16 v[228:231], v[164:167], v[192:195], v[228:231]\n"
    "v_add_f32 v89, v89, v91\n"
    "v_add_f32 v5, v88, v89\n"
    "v_mfma_f32_16x16x32_bf16 v[232:235], v[168:171], v[192:195], v[232:235]\n"
    "ds_read_b128 v[128:131], v57 offset:128\n"
    "v_add_f32 v4, v4, v5\n"
    "v_mfma_f32_16x16x32_bf16 v[236:239], v[172:175], v[192:195], v[236:239]\n"
    "buffer_store_dword v4, v79, s[40:43], s93 offen sc1\n"
    "v_mfma_f32_16x16x32_bf16 v[240:243], v[176:179], v[192:195], v[240:243]\n"
    "ds_read2_b32 v[96:97], v53 offset0:160 offset1:161\n"
    "v_mfma_f32_16x16x32_bf16 v[244:247], v[180:183], v[192:195], v[244:247]\n"
    "ds_read2_b32 v[98:99], v53 offset0:162 offset1:163\n"
    "v_mfma_f32_16x16x32_bf16 v[248:251], v[184:187], v[192:195], v[248:251]\n"
    "ds_read2_b32 v[100:101], v53 offset0:32 offset1:33\n"
    "v_mfma_f32_16x16x32_bf16 v[252:255], v[188:191], v[192:195], v[252:255]\n"
    "v_and_b32 v1, 15, v65\n"
    "v_lshrrev_b32 v2, 4, v65\n"
    "v_mfma_f32_16x16x32_bf16 v[228:231], v[160:163], v[196:199], v[228:231]\n"
    "ds_read2_b32 v[102:103], v53 offset0:34 offset1:35\n"
    "v_mfma_f32_16x16x32_bf16 v[232:235], v[164:167], v[196:199], v[232:235]\n"
    "ds_read2_b32 v[104:105], v54 offset0:160 offset1:161\n"
    "v_mfma_f32_16x16x32_bf16 v[236:239], v[168:171], v[196:199], v[236:239]\n"
    "ds_read2_b32 v[106:107], v54 offset0:162 offset1:163\n"
    "v_mfma_f32_16x16x32_bf16 v[240:243], v[172:175], v[196:199], v[240:243]\n"
    "v_lshl_add_u32 v6, v1, 2, v2\n"
    "v_bfe_u32 v7, v6, 3, 2\n"
    "v_mfma_f32_16x16x32_bf16 v[244:247], v[176:179], v[196:199], v[244:247]\n"
    "ds_read2_b32 v[108:109], v54 offset0:32 offset1:33\n"
    "v_mfma_f32_16x16x32_bf16 v[248:251], v[180:183], v[196:199], v[248:251]\n"
    "ds_read2_b32 v[110:111], v54 offset0:34 offset1:35\n"
    "v_mfma_f32_16x16x32_bf16 v[252:255], v[184:187], v[196:199], v[252:255]\n"
    "ds_read2_b32 v[112:113], v55 offset0:160 offset1:161\n"
    "v_mfma_f32_16x16x32_bf16 v[232:235], v[160:163], v[200:203], v[232:235]\n"
    "v_xor_b32 v6, v6, v7\n"
    "v_mfma_f32_16x16x32_bf16 v[236:239], v[164:167], v[200:203], v[236:239]\n"
    "ds_read2_b32 v[114:115], v55 offset0:162 offset1:163\n"
    "v_mfma_f32_16x16x32_bf16 v[240:243], v[168:171], v[200:203], v[240:243]\n"
    "ds_read2_b32 v[116:117], v55 offset0:32 offset1:33\n"
    "v_mfma_f32_16x16x32_bf16 v[244:247], v[172:175], v[200:203], v[244:247]\n"
    "ds_read2_b32 v[118:119], v55 offset0:34 offset1:35\n"
    "v_mfma_f32_16x16x32_bf16 v[248:251], v[176:179], v[200:203], v[248:251]\n"
    "s_lshl_b32 s56, s48, 13\n"
    "v_mfma_f32_16x16x32_bf16 v[252:255], v[180:183], v[200:203], v[252:255]\n"
    "ds_read2_b32 v[120:121], v56 offset0:160 offset1:161\n"
    "v_mfma_f32_16x16x32_bf16 v[236:239], v[160:163], v[204:207], v[236:239]\n"
    "ds_read2_b32 v[122:123], v56 offset0:162 offset1:163\n"
    "v_mfma_f32_16x16x32_bf16 v[240:243], v[164:167], v[204:207], v[240:243]\n"
    "ds_read2_b32 v[124:125], v56 offset0:32 offset1:33\n"
    "v_mfma_f32_16x16x32_bf16 v[244:247], v[168:171], v[204:207], v[244:247]\n"
    "s_add_u32 s56, s56, 16640\n"
    "v_mfma_f32_16x16x32_bf16 v[248:251], v[172:175], v[204:207], v[248:251]\n"
    "ds_read2_b32 v[126:127], v56 offset0:34 offset1:35\n"
    "v_mfma_f32_16x16x32_bf16 v[252:255], v[176:179], v[204:207], v[252:255]\n"
    "ds_read_b128 v[132:135], v57 offset:640\n"
    "v_mfma_f32_16x16x32_bf16 v[240:243], v[160:163], v[208:211], v[240:243]\n"
    "ds_read_b128 v[136:139], v57 offset:1152\n"
    "v_mfma_f32_16x16x32_bf16 v[244:247], v[164:167], v[208:211], v[244:247]\n"
    "v_lshl_add_u32 v69, v6, 4, s56\n"
    "v_mfma_f32_16x16x32_bf16 v[248:251], v[168:171], v[208:211], v[248:251]\n"
    "ds_read_b128 v[140:143], v57 offset:1664\n"
    "v_mfma_f32_16x16x32_bf16 v[252:255], v[172:175], v[208:211], v[252:255]\n"
    "ds_read_b128 v[144:147], v57 offset:2176\n"
    "v_mfma_f32_16x16x32_bf16 v[244:247], v[160:163], v[212:215], v[244:247]\n"
    "ds_read_b128 v[148:151], v57 offset:2688\n"
    "v_mfma_f32_16x16x32_bf16 v[248:251], v[164:167], v[212:215], v[248:251]\n"
    "v_bfe_u32 v7, %[tid], 3, 2\n"
    "v_mfma_f32_16x16x32_bf16 v[252:255], v[168:171], v[212:215], v[252:255]\n"
    "ds_read_b128 v[152:155], v57 offset:3200\n"
    "v_mfma_f32_16x16x32_bf16 v[248:251], v[160:163], v[216:219], v[248:251]\n"
    "ds_read_b128 v[156:159], v57 offset:3712\n"
    "v_mfma_f32_16x16x32_bf16 v[252:255], v[164:167], v[216:219], v[252:255]\n"
    "v_xor_b32 v6, %[tid], v7\n"
    "v_mfma_f32_16x16x32_bf16 v[252:255], v[160:163], v[220:223], v[252:255]\n"
    "v_lshlrev_b32 v59, 4, v6\n"
    "s_waitcnt vmcnt(0)\n"
    "v_mov_b32 v24, 1\n"
    "s_lshl2_add_u32 s54, %[bid], s48\n"
    "s_lshl2_add_u32 s54, s54, %[cFW]\n"
    "s_mov_b64 exec, 1\n"
    "buffer_store_dword v24, off, s[40:43], s54 sc1\n"
    "s_mov_b64 exec, -1\n"
    "s_waitcnt lgkmcnt(7)\n"
    "v_mfma_f32_16x16x32_bf16 v[224:227], v[96:99], v[128:131], v[224:227]\n"
    "v_lshrrev_b32 v6, 3, %[tid]\n"
    "v_and_b32 v7, 7, %[tid]\n"
    "v_mfma_f32_16x16x32_bf16 v[228:231], v[100:103], v[128:131], v[228:231]\n"
    "v_lshlrev_b32 v6, 15, v6\n"
    "v_lshl_add_u32 v60, v7, 4, v6\n"
    "v_mfma_f32_16x16x32_bf16 v[232:235], v[104:107], v[128:131], v[232:235]\n"
    "ds_read_b128 v[192:195], v57 offset:0\n"
    "v_mfma_f32_16x16x32_bf16 v[236:239], v[108:111], v[128:131], v[236:239]\n"
    "s_lshl_b32 s62, s51, 7\n"
    "s_add_u32 s62, s62, %[cX2]\n"
    "v_mfma_f32_16x16x32_bf16 v[240:243], v[112:115], v[128:131], v[240:243]\n"
    "ds_read2_b32 v[160:161], v53 offset0:128 offset1:129\n"
    "v_mfma_f32_16x16x32_bf16 v[244:247], v[116:119], v[128:131], v[244:247]\n"
    "ds_read2_b32 v[162:163], v53 offset0:130 offset1:131\n"
    "v_mfma_f32_16x16x32_bf16 v[248:251], v[120:123], v[128:131], v[248:251]\n"
    "ds_read2_b32 v[164:165], v53 offset0:0 offset1:1\n"
    "v_mfma_f32_16x16x32_bf16 v[252:255], v[124:127], v[128:131], v[252:255]\n"
    "s_add_u32 s63, s62, 1048576\n"
    "v_lshlrev_b32 v72, 16, v8\n"
    "s_waitcnt lgkmcnt(4)\n"
    "v_mfma_f32_16x16x32_bf16 v[228:231], v[96:99], v[132:135], v[228:231]\n"
    "ds_read2_b32 v[166:167], v53 offset0:2 offset1:3\n"
    "v_mfma_f32_16x16x32_bf16 v[232:235], v[100:103], v[132:135], v[232:235]\n"
    "ds_read2_b32 v[168:169], v54 offset0:128 offset1:129\n"
    "v_mfma_f32_16x16x32_bf16 v[236:239], v[104:107], v[132:135], v[236:239]\n"
    "ds_read2_b32 v[170:171], v54 offset0:130 offset1:131\n"
    "v_mfma_f32_16x16x32_bf16 v[240:243], v[108:111], v[132:135], v[240:243]\n"
    "v_lshlrev_b32 v73, 16, v9\n"
    "v_and_b32 v80, 0xffff0000, v8\n"
    "v_mfma_f32_16x16x32_bf16 v[244:247], v[112:115], v[132:135], v[244:247]\n"
    "ds_read2_b32 v[172:173], v54 offset0:0 offset1:1\n"
    "v_mfma_f32_16x16x32_bf16 v[248:251], v[116:119], v[132:135], v[248:251]\n"
    "ds_read2_b32 v[174:175], v54 offset0:2 offset1:3\n"
    "v_mfma_f32_16x16x32_bf16 v[252:255], v[120:123], v[132:135], v[252:255]\n"
    "ds_read2_b32 v[176:177], v55 offset0:128 offset1:129\n"
    "v_mfma_f32_16x16x32_bf16 v[232:235], v[96:99], v[136:139], v[232:235]\n"
    "v_and_b32 v81, 0xffff0000, v9\n"
    "v_lshlrev_b32 v88, 16, v26\n"
    "v_mfma_f32_16x16x32_bf16 v[236:239], v[100:103], v[136:139], v[236:239]\n"
    "ds_read2_b32 v[178:179], v55 offset0:130 offset1:131\n"
    "v_mfma_f32_16x16x32_bf16 v[240:243], v[104:107], v[136:139], v[240:243]\n"
    "ds_read2_b32 v[180:181], v55 offset0:0 offset1:1\n"
    "v_mfma_f32_16x16x32_bf16 v[244:247], v[108:111], v[136:139], v[244:247]\n"
    "ds_read2_b32 v[182:183], v55 offset0:2 offset1:3\n"
    "v_mfma_f32_16x16x32_bf16 v[248:251], v[112:115], v[136:139], v[248:251]\n"
    "v_and_b32 v89, 0xffff0000, v26\n"
    "v_lshlrev_b32 v90, 16, v28\n"
    "v_mfma_f32_16x16x32_bf16 v[252:255], v[116:119], v[136:139], v[252:255]\n"
    "ds_read2_b32 v[184:185], v56 offset0:128 offset1:129\n"
    "v_mfma_f32_16x16x32_bf16 v[236:239], v[96:99], v[140:143], v[236:239]\n"
    "ds_read2_b32 v[186:187], v56 offset0:130 offset1:131\n"
    "v_mfma_f32_16x16x32_bf16 v[240:243], v[100:103], v[140:143], v[240:243]\n"
    "ds_read2_b32 v[188:189], v56 offset0:0 offset1:1\n"
    "v_mfma_f32_16x16x32_bf16 v[244:247], v[104:107], v[140:143], v[244:247]\n"
    "v_and_b32 v91, 0xffff0000, v28\n"
    "v_fmac_f32 v72, v63, v88\n"
    "v_mfma_f32_16x16x32_bf16 v[248:251], v[108:111], v[140:143], v[248:251]\n"
    "ds_read2_b32 v[190:191], v56 offset0:2 offset1:3\n"
    "v_mfma_f32_16x16x32_bf16 v[252:255], v[112:115], v[140:143], v[252:255]\n"
    "ds_read_b128 v[196:199], v57 offset:512\n"
    "v_mfma_f32_16x16x32_bf16 v[240:243], v[96:99], v[144:147], v[240:243]\n"
    "ds_read_b128 v[200:203], v57 offset:1024\n"
    "v_mfma_f32_16x16x32_bf16 v[244:247], v[100:103], v[144:147], v[244:247]\n"
    "v_fmac_f32 v73, v63, v89\n"
    "v_fmac_f32 v80, v64, v90\n"
    "v_mfma_f32_16x16x32_bf16 v[248:251], v[104:107], v[144:147], v[248:251]\n"
    "ds_read_b128 v[204:207], v57 offset:1536\n"
    "v_mfma_f32_16x16x32_bf16 v[252:255], v[108:111], v[144:147], v[252:255]\n"
    "ds_read_b128 v[208:211], v57 offset:2048\n"
    "v_mfma_f32_16x16x32_bf16 v[244:247], v[96:99], v[148:151], v[244:247]\n"
    "ds_read_b128 v[212:215], v57 offset:2560\n"
    "v_mfma_f32_16x16x32_bf16 v[248:251], v[100:103], v[148:151], v[248:251]\n"
    "v_fmac_f32 v81, v64, v91\n"
    "v_lshlrev_b32 v74, 16, v10\n"
    "v_mfma_f32_16x16x32_bf16 v[252:255], v[104:107], v[148:151], v[252:255]\n"
    "ds_read_b128 v[216:219], v57 offset:3072\n"
    "v_mfma_f32_16x16x32_bf16 v[248:251], v[96:99], v[152:155], v[248:251]\n"
    "ds_read_b128 v[220:223], v57 offset:3584\n"
    "v_mfma_f32_16x16x32_bf16 v[252:255], v[100:103], v[152:155], v[252:255]\n"
    "v_lshlrev_b32 v75, 16, v11\n"
    "v_and_b32 v82, 0xffff0000, v10\n"
    "v_mfma_f32_16x16x32_bf16 v[252:255], v[96:99], v[156:159], v[252:255]\n"
    "v_and_b32 v83, 0xffff0000, v11\n"
    "v_lshlrev_b32 v88, 16, v27\n"
    "s_waitcnt lgkmcnt(0)\n"
    "v_mfma_f32_16x16x32_bf16 v[252:255], v[188:191], v[192:195], v[252:255]\n"
    "v_and_b32 v89, 0xffff0000, v27\n"
    "v_lshlrev_b32 v90, 16, v29\n"
    "v_mfma_f32_16x16x32_bf16 v[252:255], v[184:187], v[196:199], v[252:255]\n"
    "v_and_b32 v91, 0xffff0000, v29\n"
    "v_fmac_f32 v74, v63, v88\n"
    "v_mfma_f32_16x16x32_bf16 v[252:255], v[180:183], v[200:203], v[252:255]\n"
    "v_fmac_f32 v75, v63, v89\n"
    "v_fmac_f32 v82, v64, v90\n"
    "v_mfma_f32_16x16x32_bf16 v[252:255], v[176:179], v[204:207], v[252:255]\n"
    "v_fmac_f32 v83, v64, v91\n"
    "v_lshlrev_b32 v76, 16, v12\n"
    "v_mfma_f32_16x16x32_bf16 v[252:255], v[172:175], v[208:211], v[252:255]\n"
    "v_lshlrev_b32 v77, 16, v13\n"
    "v_and_b32 v84, 0xffff0000, v12\n"
    "v_mfma_f32_16x16x32_bf16 v[252:255], v[168:171], v[212:215], v[252:255]\n"
    "v_and_b32 v85, 0xffff0000, v13\n"
    "v_lshlrev_b32 v88, 16, v30\n"
    "v_mfma_f32_16x16x32_bf16 v[252:255], v[164:167], v[216:219], v[252:255]\n"
    "v_and_b32 v89, 0xffff0000, v30\n"
    "v_lshlrev_b32 v90, 16, v32\n"
    "v_mfma_f32_16x16x32_bf16 v[252:255], v[160:163], v[220:223], v[252:255]\n"
    "v_and_b32 v91, 0xffff0000, v32\n"
    "v_fmac_f32 v76, v63, v88\n"
    "v_mfma_f32_16x16x32_bf16 v[248:251], v[184:187], v[192:195], v[248:251]\n"
    "v_fmac_f32 v77, v63, v89\n"
    "v_fmac_f32 v84, v64, v90\n"
    "v_mfma_f32_16x16x32_bf16 v[248:251], v[180:183], v[196:199], v[248:251]\n"
    "v_fmac_f32 v85, v64, v91\n"
    "v_lshlrev_b32 v78, 16, v14\n"
    "v_mfma_f32_16x16x32_bf16 v[248:251], v[176:179], v[200:203], v[248:251]\n"
    "v_lshlrev_b32 v79, 16, v15\n"
    "v_and_b32 v86, 0xffff0000, v14\n"
    "v_mfma_f32_16x16x32_bf16 v[248:251], v[172:175], v[204:207], v[248:251]\n"
    "v_and_b32 v87, 0xffff0000, v15\n"
    "v_lshlrev_b32 v88, 16, v31\n"
    "v_mfma_f32_16x16x32_bf16 v[248:251], v[168:171], v[208:211], v[248:251]\n"
    "v_and_b32 v89, 0xffff0000, v31\n"
    "v_lshlrev_b32 v90, 16, v33\n"
    "v_mfma_f32_16x16x32_bf16 v[248:251], v[164:167], v[212:215], v[248:251]\n"
    "v_and_b32 v91, 0xffff0000, v33\n"
    "v_fmac_f32 v78, v63, v88\n"
    "v_mfma_f32_16x16x32_bf16 v[248:251], v[160:163], v[216:219], v[248:251]\n"
    "v_fmac_f32 v79, v63, v89\n"
    "v_mfma_f32_16x16x32_bf16 v[244:247], v[180:183], v[192:195], v[244:247]\n"
    "v_fmac_f32 v86, v64, v90\n"
    "ds_write_b128 v69, v[252:255] offset:7168\n"
    "v_mfma_f32_16x16x32_bf16 v[244:247], v[176:179], v[196:199], v[244:247]\n"
    "v_fmac_f32 v87, v64, v91\n"
    "v_mfma_f32_16x16x32_bf16 v[244:247], v[172:175], v[200:203], v[244:247]\n"
    "v_mfma_f32_16x16x32_bf16 v[244:247], v[168:171], v[204:207], v[244:247]\n"
    "v_mfma_f32_16x16x32_bf16 v[244:247], v[164:167], v[208:211], v[244:247]\n"
    "v_mfma_f32_16x16x32_bf16 v[244:247], v[160:163], v[212:215], v[244:247]\n"
    "v_mfma_f32_16x16x32_bf16 v[240:243], v[176:179], v[192:195], v[240:243]\n"
    "ds_write_b128 v69, v[248:251] offset:6144\n"
    "v_mfma_f32_16x16x32_bf16 v[240:243], v[172:175], v[196:199], v[240:243]\n"
    "v_mfma_f32_16x16x32_bf16 v[240:243], v[168:171], v[200:203], v[240:243]\n"
    "v_mfma_f32_16x16x32_bf16 v[240:243], v[164:167], v[204:207], v[240:243]\n"
    "v_mfma_f32_16x16x32_bf16 v[240:243], v[160:163], v[208:211], v[240:243]\n"
    "v_mfma_f32_16x16x32_bf16 v[236:239], v[172:175], v[192:195], v[236:239]\n"
    "v_mfma_f32_16x16x32_bf16 v[236:239], v[168:171], v[196:199], v[236:239]\n"
    "ds_write_b128 v69, v[244:247] offset:5120\n"
    "v_mfma_f32_16x16x32_bf16 v[236:239], v[164:167], v[200:203], v[236:239]\n"
    "v_mfma_f32_16x16x32_bf16 v[236:239], v[160:163], v[204:207], v[236:239]\n"
    "v_mfma_f32_16x16x32_bf16 v[232:235], v[168:171], v[192:195], v[232:235]\n"
    "v_mfma_f32_16x16x32_bf16 v[232:235], v[164:167], v[196:199], v[232:235]\n"
    "v_mfma_f32_16x16x32_bf16 v[232:235], v[160:163], v[200:203], v[232:235]\n"
    "ds_write_b128 v69, v[240:243] offset:4096\n"
    "v_mfma_f32_16x16x32_bf16 v[228:231], v[164:167], v[192:195], v[228:231]\n"
    "v_mfma_f32_16x16x32_bf16 v[228:231], v[160:163], v[196:199], v[228:231]\n"
    "v_mfma_f32_16x16x32_bf16 v[224:227], v[160:163], v[192:195], v[224:227]\n"
    "s_nop 0\n"
    "ds_write_b128 v69, v[236:239] offset:3072\n"
    "s_nop 2\n"
    "ds_write_b128 v69, v[232:235] offset:2048\n"
    "s_nop 0\n"
    "ds_write_b128 v69, v[228:231] offset:1024\n"
    "ds_write_b128 v69, v[224:227] offset:0\n"
    "s_waitcnt lgkmcnt(0)\n"
    "s_barrier\n"
    "ds_read_b128 v[96:99], v59 offset:16640\n"
    "ds_read_b128 v[100:103], v59 offset:24832\n"
    "ds_read_b128 v[104:107], v59 offset:33024\n"
    "ds_read_b128 v[108:111], v59 offset:41216\n"
    "ds_read_b128 v[112:115], v59 offset:20736\n"
    "ds_read_b128 v[116:119], v59 offset:28928\n"
    "ds_read_b128 v[120:123], v59 offset:37120\n"
    "ds_read_b128 v[124:127], v59 offset:45312\n"
    "v_lshrrev_b32 v7, 2, v68\n"
    "s_lshl_b32 s56, s76, 8\n"
    "s_add_u32 s56, s56, %[cFW]\n"
    "buffer_load_dword v6, v7, s[40:43], s56 offen sc1\n"
    "s_waitcnt lgkmcnt(6)\n"
    "v_pk_add_f32 v[96:97], v[96:97], v[100:101]\n"
    "v_pk_add_f32 v[98:99], v[98:99], v[102:103]\n"
    "s_waitcnt lgkmcnt(4)\n"
    "v_pk_add_f32 v[104:105], v[104:105], v[108:109]\n"
    "v_pk_add_f32 v[106:107], v[106:107], v[110:111]\n"
    "v_pk_fma_f32 v[96:97], s[64:65], v[96:97], v[72:73] op_sel_hi:[0,1,1]\n"
    "v_pk_fma_f32 v[98:99], s[64:65], v[98:99], v[74:75] op_sel_hi:[0,1,1]\n"
    "v_pk_fma_f32 v[104:105], s[66:67], v[104:105], v[80:81] op_sel_hi:[0,1,1]\n"
    "v_pk_fma_f32 v[106:107], s[66:67], v[106:107], v[82:83] op_sel_hi:[0,1,1]\n"
    "s_nop 0\n"
    "v_cvt_pk_bf16_f32 v128, v96, v104\n"
    "v_cvt_pk_bf16_f32 v129, v97, v105\n"
    "v_cvt_pk_bf16_f32 v130, v98, v106\n"
    "v_cvt_pk_bf16_f32 v131, v99, v107\n"
    "buffer_store_dwordx4 v[128:131], v60, s[40:43], s62 offen sc1\n"
    "s_waitcnt lgkmcnt(2)\n"
    "v_pk_add_f32 v[112:113], v[112:113], v[116:117]\n"
    "v_pk_add_f32 v[114:115], v[114:115], v[118:119]\n"
    "s_waitcnt lgkmcnt(0)\n"
    "v_pk_add_f32 v[120:121], v[120:121], v[124:125]\n"
    "v_pk_add_f32 v[122:123], v[122:123], v[126:127]\n"
    "v_pk_fma_f32 v[112:113], s[64:65], v[112:113], v[76:77] op_sel_hi:[0,1,1]\n"
    "v_pk_fma_f32 v[114:115], s[64:65], v[114:115], v[78:79] op_sel_hi:[0,1,1]\n"
    "v_pk_fma_f32 v[120:121], s[66:67], v[120:121], v[84:85] op_sel_hi:[0,1,1]\n"
    "v_pk_fma_f32 v[122:123], s[66:67], v[122:123], v[86:87] op_sel_hi:[0,1,1]\n"
    "s_nop 0\n"
    "v_cvt_pk_bf16_f32 v132, v112, v120\n"
    "v_cvt_pk_bf16_f32 v133, v113, v121\n"
    "v_cvt_pk_bf16_f32 v134, v114, v122\n"
    "v_cvt_pk_bf16_f32 v135, v115, v123\n"
    "buffer_store_dwordx4 v[132:135], v60, s[40:43], s63 offen sc1\n"
    "v_mov_b32 v246, v68\n"
    "s_mov_b32 s44, s22\n"
    "s_and_b32 s45, s23, 0xffff\n"
    "s_mov_b32 s46, 2097152\n"
    "s_mov_b32 s47, 0x20000\n"
    "s_lshr_b32 s54, s53, 3\n"
    "s_lshl2_add_u32 s85, s52, s54\n"
    "s_and_b32 s86, s53, 7\n"
    "s_lshl1_add_u32 s87, s86, s49\n"
    "s_lshl1_add_u32 s88, s85, s50\n"
    "s_mul_i32 s55, s48, 28672\n"
    "s_add_u32 s55, s55, %[cWD]\n"
    "s_mul_i32 s78, s87, 114688\n"
    "s_xor_b32 s54, s87, 1\n"
    "s_mul_i32 s79, s54, 114688\n"
    "s_add_u32 s78, s78, s55\n"
    "s_add_u32 s79, s79, s55\n"
    "s_add_u32 s80, s78, 4096\n"
    "s_add_u32 s81, s79, 4096\n"
    "s_mul_i32 s82, s77, 114688\n"
    "s_mul_i32 s54, s76, 7168\n"
    "s_add_u32 s82, s82, s54\n"
    "s_add_u32 s82, s82, %[cHH]\n"
    "s_add_u32 s83, s82, 4096\n"
    "v_add_u32 v243, 7168, v68\n"
    "v_add_u32 v244, 14336, v68\n"
    "v_add_u32 v245, 21504, v68\n"
    "s_lshl_b32 s10, s48, 13\n"
    "s_mul_i32 s9, s76, 229376\n"
    "s_add_u32 s9, s9, s10\n"
    "s_add_u32 s9, s9, %[cWGU]\n"
    "s_lshl_b32 s8, s77, 15\n"
    "s_add_u32 s8, s8, %[cX2]\n"
    "s_mul_i32 s17, s76, 896\n"
    "s_add_u32 s17, s17, %[cUV]\n"
    "v_lshlrev_b32 v208, 2, %[tid]\n"
    "v_add_u32 v209, 0x20380, v208\n"
    "v_cmp_gt_u32 vcc, 224, %[tid]\n"
    "v_add_u32 v210, 0x20000, v208\n"
    "v_lshlrev_b32 v8, 3, v246\n"
    "v_cndmask_b32 v209, v209, v210, vcc\n"
    "v_cndmask_b32 v208, 0, v208, vcc\n"
    "s_add_u32 s55, s8, s10\n"
    "s_barrier\n"
    "s_waitcnt vmcnt(2)\n"
    "v_cmp_ne_u32 vcc, 1, v6\n"
    "s_cbranch_vccz .Lk_goW\n"
    "s_mov_b32 s19, 0x40000\n"
    ".Lk_rlW:\n"
    "s_sleep 4\n"
    "buffer_load_dword v6, v7, s[40:43], s56 offen sc1\n"
    "s_waitcnt vmcnt(0)\n"
    "v_cmp_ne_u32 vcc, 1, v6\n"
    "s_cbranch_vccz .Lk_goW\n"
    "s_sub_u32 s19, s19, 1\n"
    "s_cmp_lg_u32 s19, 0\n"
    "s_cbranch_scc1 .Lk_rlW\n"
    ".Lk_goW:\n"
    "s_add_u32 s12, s9, 0\n"
    "s_add_u32 m0, s10, 98304\n"
    "s_nop 0\n"
    "buffer_load_dwordx4 v246, s[40:43], s12 offen offset:0 sc1 lds\n"
    "buffer_load_dwordx4 v246, s[40:43], s12 offen offset:1024 sc1 lds\n"
    "buffer_load_dwordx4 v246, s[40:43], s12 offen offset:2048 sc1 lds\n"
    "buffer_load_dwordx4 v246, s[40:43], s12 offen offset:3072 sc1 lds\n"
    "s_add_u32 s12, s9, 4096\n"
    "s_add_u32 m0, s10, 102400\n"
    "s_nop 0\n"
    "buffer_load_dwordx4 v246, s[40:43], s12 offen offset:0 sc1 lds\n"
    "buffer_load_dwordx4 v246, s[40:43], s12 offen offset:1024 sc1 lds\n"
    "buffer_load_dwordx4 v246, s[40:43], s12 offen offset:2048 sc1 lds\n"
    "buffer_load_dwordx4 v246, s[40:43], s12 offen offset:3072 sc1 lds\n"
    "s_add_u32 s12, s9, 32768\n"
    "s_add_u32 m0, s10, 65536\n"
    "s_nop 0\n"
    "buffer_load_dwordx4 v246, s[40:43], s12 offen offset:0 sc1 lds\n"
    "buffer_load_dwordx4 v246, s[40:43], s12 offen offset:1024 sc1 lds\n"
    "buffer_load_dwordx4 v246, s[40:43], s12 offen offset:2048 sc1 lds\n"
    "buffer_load_dwordx4 v246, s[40:43], s12 offen offset:3072 sc1 lds\n"
    "s_add_u32 s12, s9, 36864\n"
    "s_add_u32 m0, s10, 69632\n"
    "s_nop 0\n"
    "buffer_load_dwordx4 v246, s[40:43], s12 offen offset:0 sc1 lds\n"
    "buffer_load_dwordx4 v246, s[40:43], s12 offen offset:1024 sc1 lds\n"
    "buffer_load_dwordx4 v246, s[40:43], s12 offen offset:2048 sc1 lds\n"
    "buffer_load_dwordx4 v246, s[40:43], s12 offen offset:3072 sc1 lds\n"
    "s_add_u32 s18, s17, 0\n"
    "buffer_load_dword v200, v208, s[40:43], s18 offen sc1\n"
    "s_add_u32 s18, s17, 14336\n"
    "buffer_load_dword v201, v208, s[40:43], s18 offen sc1\n"
    "s_add_u32 s18, s17, 28672\n"
    "buffer_load_dword v202, v208, s[40:43], s18 offen sc1\n"
    "s_add_u32 s18, s17, 43008\n"
    "buffer_load_dword v203, v208, s[40:43], s18 offen sc1\n"
    "s_add_u32 s18, s17, 57344\n"
    "buffer_load_dword v204, v208, s[40:43], s18 offen sc1\n"
    "s_add_u32 s18, s17, 71680\n"
    "buffer_load_dword v205, v208, s[40:43], s18 offen sc1\n"
    "s_add_u32 s18, s17, 86016\n"
    "buffer_load_dword v206, v208, s[40:43], s18 offen sc1\n"
    "s_add_u32 s18, s17, 100352\n"
    "buffer_load_dword v207, v208, s[40:43], s18 offen sc1\n"
    "s_add_u32 s11, s8, 0\n"
    "s_add_u32 m0, s10, 0\n"
    "s_nop 0\n"
    "buffer_load_dwordx4 v246, s[40:43], s11 offen offset:0 sc1 lds\n"
    "buffer_load_dwordx4 v246, s[40:43], s11 offen offset:1024 sc1 lds\n"
    "buffer_load_dwordx4 v246, s[40:43], s11 offen offset:2048 sc1 lds\n"
    "buffer_load_dwordx4 v246, s[40:43], s11 offen offset:3072 sc1 lds\n"
    "s_add_u32 s11, s8, 4096\n"
    "s_add_u32 m0, s10, 4096\n"
    "s_nop 0\n"
    "buffer_load_dwordx4 v246, s[40:43], s11 offen offset:0 sc1 lds\n"
    "buffer_load_dwordx4 v246, s[40:43], s11 offen offset:1024 sc1 lds\n"
    "s_mul_i32 s18, s48, 28672\n"
    "s_add_u32 s18, s18, %[cHH]\n"
    "s_mul_i32 s13, s88, 114688\n"
    "s_xor_b32 s54, s88, 1\n"
    "s_mul_i32 s14, s54, 114688\n"
    "s_add_u32 s13, s13, s18\n"
    "s_add_u32 s14, s14, s18\n"
    "s_add_u32 s15, s13, 4096\n"
    "s_add_u32 s16, s14, 4096\n"
    "s_mov_b32 s20, 0x3f803f80\n"
    "v_add_u32 v242, 0x10000, v246\n"
    "v_mov_b32 v236, 0\n"
    "v_mov_b32 v237, 0\n"
    "v_and_b32 v248, 31, v65\n"
    "v_lshrrev_b32 v251, 5, v65\n"
    "v_lshlrev_b32 v252, 2, v248\n"
    "v_lshl_add_u32 v252, v251, 9, v252\n"
    "v_add_u32 v252, s10, v252\n"
    "v_add_u32 v254, 0x80, v252\n"
    "v_lshlrev_b32 v249, 4, v251\n"
    "v_and_b32 v210, 16, %[tid]\n"
    "v_cmp_eq_u32 vcc, 0, v210\n"
    "v_mov_b32 v210, 0xbfb8aa3b\n"
    "v_add_u32 v249, 0x20000, v249\n"
    "v_cndmask_b32 v210, 1.0, v210, vcc\n"
    "s_lshl_b32 s54, s85, 5\n"
    "s_lshl2_add_u32 s54, s48, s54\n"
    "s_lshl_b32 s54, s54, 7\n"
    "s_add_u32 s54, s54, %[cFL]\n"
    "v_and_b32 v250, 3, v65\n"
    "v_bfe_u32 v247, v65, 2, 1\n"
    "v_lshl_add_u32 v250, v247, 4, v250\n"
    "v_lshlrev_b32 v250, 7, v250\n"
    "v_add_u32 v250, s54, v250\n"
    "s_waitcnt vmcnt(6)\n"
    "v_add_f32 v200, v200, v202\n"
    "v_add_f32 v201, v201, v203\n"
    "v_add_f32 v204, v204, v206\n"
    "v_add_f32 v205, v205, v207\n"
    "v_add_f32 v200, v200, v204\n"
    "v_add_f32 v201, v201, v205\n"
    "v_mul_f32 v200, v210, v200\n"
    "v_mul_f32 v201, v210, v201\n"
    "ds_write2_b32 v209, v200, v201 offset1:224\n"
    "s_waitcnt lgkmcnt(0)\n"
    "s_barrier\n"
    "ds_read_b128 v[176:179], v242 offset:32768\n"
    "ds_read_b128 v[180:183], v242 offset:0\n"
    "ds_read_b128 v[184:187], v242 offset:33792\n"
    "ds_read_b128 v[188:191], v242 offset:1024\n"
    "ds_read_b128 v[192:195], v242 offset:34816\n"
    "ds_read_b128 v[196:199], v242 offset:2048\n"
    "s_waitcnt vmcnt(4)\n"
    "ds_read2st64_b32 v[0:1], v252 offset0:0 offset1:1\n"
    "ds_read2st64_b32 v[2:3], v254 offset0:0 offset1:1\n"
    "ds_read2st64_b32 v[4:5], v252 offset0:4 offset1:5\n"
    "ds_read2st64_b32 v[6:7], v254 offset0:4 offset1:5\n"
    "s_waitcnt lgkmcnt(0)\n"
    "v_max3_u32 v251, v0, v1, v2\n"
    "v_max3_u32 v247, v4, v5, v6\n"
    "v_max3_u32 v251, v251, v3, v7\n"
    "v_max_u32 v251, v251, v247\n"
    "v_cmp_eq_u32 vcc, -1, v251\n"
    "s_cbranch_vccnz .Lk_rex0\n"
    ".Lk_gox0:\n"
    "v_mfma_f32_32x32x16_bf16 v[128:143], v[176:179], v[0:3], 0\n"
    "ds_read_b128 v[176:179], v242 offset:35840\n"
    "s_waitcnt vmcnt(2)\n"
    "ds_read2st64_b32 v[8:9], v252 offset0:8 offset1:9\n"
    "ds_read2st64_b32 v[10:11], v254 offset0:8 offset1:9\n"
    "ds_read2st64_b32 v[12:13], v252 offset0:12 offset1:13\n"
    "ds_read2st64_b32 v[14:15], v254 offset0:12 offset1:13\n"
    "v_mfma_f32_32x32x16_bf16 v[144:159], v[180:183], v[0:3], 0\n"
    "ds_read_b128 v[180:183], v242 offset:3072\n"
    "buffer_load_dwordx4 v246, s[40:43], s11 offen offset:2048 sc1 lds\n"
    "buffer_load_dwordx4 v246, s[40:43], s11 offen offset:3072 sc1 lds\n"
    "v_mfma_f32_32x32x16_bf16 v[128:143], v[184:187], v[4:7], v[128:143]\n"
    "ds_read_b128 v[184:187], v242 offset:36864\n"
    "s_add_u32 s11, s8, 8192\n"
    "s_add_u32 m0, s10, 32768\n"
    "s_nop 0\n"
    "buffer_load_dwordx4 v246, s[40:43], s11 offen offset:0 sc1 lds\n"
    "buffer_load_dwordx4 v246, s[40:43], s11 offen offset:1024 sc1 lds\n"
    "v_mfma_f32_32x32x16_bf16 v[144:159], v[188:191], v[4:7], v[144:159]\n"
    "ds_read_b128 v[188:191], v242 offset:4096\n"
    "s_waitcnt lgkmcnt(3)\n"
    "v_max3_u32 v251, v8, v9, v10\n"
    "v_max3_u32 v247, v12, v13, v14\n"
    "v_max3_u32 v251, v251, v11, v15\n"
    "v_max_u32 v251, v251, v247\n"
    "v_cmp_eq_u32 vcc, -1, v251\n"
    "s_cbranch_vccnz .Lk_rex1\n"
    ".Lk_gox1:\n"
    "v_dot2c_f32_bf16 v236, s20, v0\n"
    "v_dot2c_f32_bf16 v237, v0, v0\n"
    "v_dot2c_f32_bf16 v236, s20, v1\n"
    "v_dot2c_f32_bf16 v237, v1, v1\n"
    "v_dot2c_f32_bf16 v236, s20, v2\n"
    "v_dot2c_f32_bf16 v237, v2, v2\n"
    "v_dot2c_f32_bf16 v236, s20, v3\n"
    "v_dot2c_f32_bf16 v237, v3, v3\n"
    "v_dot2c_f32_bf16 v236, s20, v4\n"
    "v_dot2c_f32_bf16 v237, v4, v4\n"
    "v_dot2c_f32_bf16 v236, s20, v5\n"
    "v_dot2c_f32_bf16 v237, v5, v5\n"
    "v_dot2c_f32_bf16 v236, s20, v6\n"
    "v_dot2c_f32_bf16 v237, v6, v6\n"
    "v_dot2c_f32_bf16 v236, s20, v7\n"
    "v_dot2c_f32_bf16 v237, v7, v7\n"
    "v_mfma_f32_32x32x16_bf16 v[128:143], v[192:195], v[8:11], v[128:143]\n"
    "ds_read_b128 v[192:195], v242 offset:37888\n"
    "s_waitcnt vmcnt(4)\n"
    "ds_read2st64_b32 v[16:17], v252 offset0:16 offset1:17\n"
    "ds_read2st64_b32 v[18:19], v254 offset0:16 offset1:17\n"
    "ds_read2st64_b32 v[20:21], v252 offset0:20 offset1:21\n"
    "ds_read2st64_b32 v[22:23], v254 offset0:20 offset1:21\n"
    "v_mfma_f32_32x32x16_bf16 v[144:159], v[196:199], v[8:11], v[144:159]\n"
    "ds_read_b128 v[196:199], v242 offset:5120\n"
    "buffer_load_dwordx4 v246, s[40:43], s11 offen offset:2048 sc1 lds\n"
    "buffer_load_dwordx4 v246, s[40:43], s11 offen offset:3072 sc1 lds\n"
    "s_waitcnt lgkmcnt(8)\n"
    "v_mfma_f32_32x32x16_bf16 v[128:143], v[176:179], v[12:15], v[128:143]\n"
    "ds_read_b128 v[176:179], v242 offset:38912\n"
    "s_add_u32 s11, s8, 12288\n"
    "s_add_u32 m0, s10, 36864\n"
    "s_nop 0\n"
    "buffer_load_dwordx4 v246, s[40:43], s11 offen offset:0 sc1 lds\n"
    "buffer_load_dwordx4 v246, s[40:43], s11 offen offset:1024 sc1 lds\n"
    "v_mfma_f32_32x32x16_bf16 v[144:159], v[180:183], v[12:15], v[144:159]\n"
    "ds_read_b128 v[180:183], v242 offset:6144\n"
    "s_waitcnt lgkmcnt(3)\n"
    "v_max3_u32 v251, v16, v17, v18\n"
    "v_max3_u32 v247, v20, v21, v22\n"
    "v_max3_u32 v251, v251, v19, v23\n"
    "v_max_u32 v251, v251, v247\n"
    "v_cmp_eq_u32 vcc, -1, v251\n"
    "s_cbranch_vccnz .Lk_rex2\n"
    ".Lk_gox2:\n"
    "v_dot2c_f32_bf16 v236, s20, v8\n"
    "v_dot2c_f32_bf16 v237, v8, v8\n"
    "v_dot2c_f32_bf16 v236, s20, v9\n"
    "v_dot2c_f32_bf16 v237, v9, v9\n"
    "v_dot2c_f32_bf16 v236, s20, v10\n"
    "v_dot2c_f32_bf16 v237, v10, v10\n"
    "v_dot2c_f32_bf16 v236, s20, v11\n"
    "v_dot2c_f32_bf16 v237, v11, v11\n"
    "v_dot2c_f32_bf16 v236, s20, v12\n"
    "v_dot2c_f32_bf16 v237, v12, v12\n"
    "v_dot2c_f32_bf16 v236, s20, v13\n"
    "v_dot2c_f32_bf16 v237, v13, v13\n"
    "v_dot2c_f32_bf16 v236, s20, v14\n"
    "v_dot2c_f32_bf16 v237, v14, v14\n"
    "v_dot2c_f32_bf16 v236, s20, v15\n"
    "v_dot2c_f32_bf16 v237, v15, v15\n"
    "v_mfma_f32_32x32x16_bf16 v[128:143], v[184:187], v[16:19], v[128:143]\n"
    "ds_read_b128 v[184:187], v242 offset:39936\n"
    "s_waitcnt vmcnt(6)\n"
    "ds_read2st64_b32 v[24:25], v252 offset0:24 offset1:25\n"
    "ds_read2st64_b32 v[26:27], v254 offset0:24 offset1:25\n"
    "ds_read2st64_b32 v[28:29], v252 offset0:28 offset1:29\n"
    "ds_read2st64_b32 v[30:31], v254 offset0:28 offset1:29\n"
    "v_mfma_f32_32x32x16_bf16 v[144:159], v[188:191], v[16:19], v[144:159]\n"
    "ds_read_b128 v[188:191], v242 offset:7168\n"
    "buffer_load_dwordx4 v246, s[40:43], s11 offen offset:2048 sc1 lds\n"
    "buffer_load_dwordx4 v246, s[40:43], s11 offen offset:3072 sc1 lds\n"
    "s_waitcnt lgkmcnt(8)\n"
    "v_mfma_f32_32x32x16_bf16 v[128:143], v[192:195], v[20:23], v[128:143]\n"
    "ds_read_b128 v[192:195], v242 offset:40960\n"
    "s_add_u32 s11, s8, 16384\n"
    "s_add_u32 m0, s10, 0\n"
    "s_nop 0\n"
    "buffer_load_dwordx4 v246, s[40:43], s11 offen offset:0 sc1 lds\n"
    "buffer_load_dwordx4 v246, s[40:43], s11 offen offset:1024 sc1 lds\n"
    "v_mfma_f32_32x32x16_bf16 v[144:159], v[196:199], v[20:23], v[144:159]\n"
    "ds_read_b128 v[196:199], v242 offset:8192\n"
    "s_waitcnt lgkmcnt(3)\n"
    "v_max3_u32 v251, v24, v25, v26\n"
    "v_max3_u32 v247, v28, v29, v30\n"
    "v_max3_u32 v251, v251, v27, v31\n"
    "v_max_u32 v251, v251, v247\n"
    "v_cmp_eq_u32 vcc, -1, v251\n"
    "s_cbranch_vccnz .Lk_rex3\n"
    ".Lk_gox3:\n"
    "v_dot2c_f32_bf16 v236, s20, v16\n"
    "v_dot2c_f32_bf16 v237, v16, v16\n"
    "v_dot2c_f32_bf16 v236, s20, v17\n"
    "v_dot2c_f32_bf16 v237, v17, v17\n"
    "v_dot2c_f32_bf16 v236, s20, v18\n"
    "v_dot2c_f32_bf16 v237, v18, v18\n"
    "v_dot2c_f32_bf16 v236, s20, v19\n"
    "v_dot2c_f32_bf16 v237, v19, v19\n"
    "v_dot2c_f32_bf16 v236, s20, v20\n"
    "v_dot2c_f32_bf16 v237, v20, v20\n"
    "v_dot2c_f32_bf16 v236, s20, v21\n"
    "v_dot2c_f32_bf16 v237, v21, v21\n"
    "v_dot2c_f32_bf16 v236, s20, v22\n"
    "v_dot2c_f32_bf16 v237, v22, v22\n"
    "v_dot2c_f32_bf16 v236, s20, v23\n"
    "v_dot2c_f32_bf16 v237, v23, v23\n"
    "v_mfma_f32_32x32x16_bf16 v[128:143], v[176:179], v[24:27], v[128:143]\n"
    "ds_read_b128 v[176:179], v242 offset:41984\n"
    "s_waitcnt vmcnt(8)\n"
    "ds_read2st64_b32 v[32:33], v252 offset0:128 offset1:129\n"
    "ds_read2st64_b32 v[34:35], v254 offset0:128 offset1:129\n"
    "ds_read2st64_b32 v[36:37], v252 offset0:132 offset1:133\n"
    "ds_read2st64_b32 v[38:39], v254 offset0:132 offset1:133\n"
    "v_mfma_f32_32x32x16_bf16 v[144:159], v[180:183], v[24:27], v[144:159]\n"
    "ds_read_b128 v[180:183], v242 offset:9216\n"
    "buffer_load_dwordx4 v246, s[40:43], s11 offen offset:2048 sc1 lds\n"
    "s_waitcnt lgkmcnt(8)\n"
    "v_mfma_f32_32x32x16_bf16 v[128:143], v[184:187], v[28:31], v[128:143]\n"
    "ds_read_b128 v[184:187], v242 offset:43008\n"
    "buffer_load_dwordx4 v246, s[40:43], s11 offen offset:3072 sc1 lds\n"
    "v_mfma_f32_32x32x16_bf16 v[144:159], v[188:191], v[28:31], v[144:159]\n"
    "ds_read_b128 v[188:191], v242 offset:10240\n"
    "s_waitcnt lgkmcnt(3)\n"
    "v_max3_u32 v251, v32, v33, v34\n"
    "v_max3_u32 v247, v36, v37, v38\n"
    "v_max3_u32 v251, v251, v35, v39\n"
    "v_max_u32 v251, v251, v247\n"
    "v_cmp_eq_u32 vcc, -1, v251\n"
    "s_cbranch_vccnz .Lk_rex4\n"
    ".Lk_gox4:\n"
    "v_dot2c_f32_bf16 v236, s20, v24\n"
    "v_dot2c_f32_bf16 v237, v24, v24\n"
    "v_dot2c_f32_bf16 v236, s20, v25\n"
    "v_dot2c_f32_bf16 v237, v25, v25\n"
    "v_dot2c_f32_bf16 v236, s20, v26\n"
    "v_dot2c_f32_bf16 v237, v26, v26\n"
    "v_dot2c_f32_bf16 v236, s20, v27\n"
    "v_dot2c_f32_bf16 v237, v27, v27\n"
    "v_dot2c_f32_bf16 v236, s20, v28\n"
    "v_dot2c_f32_bf16 v237, v28, v28\n"
    "v_dot2c_f32_bf16 v236, s20, v29\n"
    "v_dot2c_f32_bf16 v237, v29, v29\n"
    "v_dot2c_f32_bf16 v236, s20, v30\n"
    "v_dot2c_f32_bf16 v237, v30, v30\n"
    "v_dot2c_f32_bf16 v236, s20, v31\n"
    "v_dot2c_f32_bf16 v237, v31, v31\n"
    "v_mfma_f32_32x32x16_bf16 v[128:143], v[192:195], v[32:35], v[128:143]\n"
    "ds_read_b128 v[192:195], v242 offset:44032\n"
    "s_waitcnt vmcnt(8)\n"
    "ds_read2st64_b32 v[40:41], v252 offset0:136 offset1:137\n"
    "ds_read2st64_b32 v[42:43], v254 offset0:136 offset1:137\n"
    "ds_read2st64_b32 v[44:45], v252 offset0:140 offset1:141\n"
    "ds_read2st64_b32 v[46:47], v254 offset0:140 offset1:141\n"
    "v_mfma_f32_32x32x16_bf16 v[144:159], v[196:199], v[32:35], v[144:159]\n"
    "ds_read_b128 v[196:199], v242 offset:11264\n"
    "s_add_u32 s11, s8, 20480\n"
    "s_add_u32 m0, s10, 4096\n"
    "s_nop 0\n"
    "buffer_load_dwordx4 v246, s[40:43], s11 offen offset:0 sc1 lds\n"
    "s_waitcnt lgkmcnt(8)\n"
    "v_mfma_f32_32x32x16_bf16 v[128:143], v[176:179], v[36:39], v[128:143]\n"
    "ds_read_b128 v[176:179], v242 offset:45056\n"
    "buffer_load_dwordx4 v246, s[40:43], s11 offen offset:1024 sc1 lds\n"
    "v_mfma_f32_32x32x16_bf16 v[144:159], v[180:183], v[36:39], v[144:159]\n"
    "ds_read_b128 v[180:183], v242 offset:12288\n"
    "s_waitcnt lgkmcnt(3)\n"
    "v_max3_u32 v251, v40, v41, v42\n"
    "v_max3_u32 v247, v44, v45, v46\n"
    "v_max3_u32 v251, v251, v43, v47\n"
    "v_max_u32 v251, v251, v247\n"
    "v_cmp_eq_u32 vcc, -1, v251\n"
    "s_cbranch_vccnz .Lk_rex5\n"
    ".Lk_gox5:\n"
    "v_dot2c_f32_bf16 v236, s20, v32\n"
    "v_dot2c_f32_bf16 v237, v32, v32\n"
    "v_dot2c_f32_bf16 v236, s20, v33\n"
    "v_dot2c_f32_bf16 v237, v33, v33\n"
    "v_dot2c_f32_bf16 v236, s20, v34\n"
    "v_dot2c_f32_bf16 v237, v34, v34\n"
    "v_dot2c_f32_bf16 v236, s20, v35\n"
    "v_dot2c_f32_bf16 v237, v35, v35\n"
    "v_dot2c_f32_bf16 v236, s20, v36\n"
    "v_dot2c_f32_bf16 v237, v36, v36\n"
    "v_dot2c_f32_bf16 v236, s20, v37\n"
    "v_dot2c_f32_bf16 v237, v37, v37\n"
    "v_dot2c_f32_bf16 v236, s20, v38\n"
    "v_dot2c_f32_bf16 v237, v38, v38\n"
    "v_dot2c_f32_bf16 v236, s20, v39\n"
    "v_dot2c_f32_bf16 v237, v39, v39\n"
    "v_mfma_f32_32x32x16_bf16 v[128:143], v[184:187], v[40:43], v[128:143]\n"
    "ds_read_b128 v[184:187], v242 offset:46080\n"
    "s_waitcnt vmcnt(8)\n"
    "ds_read2st64_b32 v[48:49], v252 offset0:144 offset1:145\n"
    "ds_read2st64_b32 v[50:51], v254 offset0:144 offset1:145\n"
    "ds_read2st64_b32 v[52:53], v252 offset0:148 offset1:149\n"
    "ds_read2st64_b32 v[54:55], v254 offset0:148 offset1:149\n"
    "v_mfma_f32_32x32x16_bf16 v[144:159], v[188:191], v[40:43], v[144:159]\n"
    "ds_read_b128 v[188:191], v242 offset:13312\n"
    "buffer_load_dwordx4 v246, s[40:43], s11 offen offset:2048 sc1 lds\n"
    "s_waitcnt lgkmcnt(8)\n"
    "v_mfma_f32_32x32x16_bf16 v[128:143], v[192:195], v[44:47], v[128:143]\n"
    "ds_read_b128 v[192:195], v242 offset:47104\n"
    "buffer_load_dwordx4 v246, s[40:43], s11 offen offset:3072 sc1 lds\n"
    "v_mfma_f32_32x32x16_bf16 v[144:159], v[196:199], v[44:47], v[144:159]\n"
    "ds_read_b128 v[196:199], v242 offset:14336\n"
    "s_waitcnt lgkmcnt(3)\n"
    "v_max3_u32 v251, v48, v49, v50\n"
    "v_max3_u32 v247, v52, v53, v54\n"
    "v_max3_u32 v251, v251, v51, v55\n"
    "v_max_u32 v251, v251, v247\n"
    "v_cmp_eq_u32 vcc, -1, v251\n"
    "s_cbranch_vccnz .Lk_rex6\n"
    ".Lk_gox6:\n"
    "v_dot2c_f32_bf16 v236, s20, v40\n"
    "v_dot2c_f32_bf16 v237, v40, v40\n"
    "v_dot2c_f32_bf16 v236, s20, v41\n"
    "v_dot2c_f32_bf16 v237, v41, v41\n"
    "v_dot2c_f32_bf16 v236, s20, v42\n"
    "v_dot2c_f32_bf16 v237, v42, v42\n"
    "v_dot2c_f32_bf16 v236, s20, v43\n"
    "v_dot2c_f32_bf16 v237, v43, v43\n"
    "v_dot2c_f32_bf16 v236, s20, v44\n"
    "v_dot2c_f32_bf16 v237, v44, v44\n"
    "v_dot2c_f32_bf16 v236, s20, v45\n"
    "v_dot2c_f32_bf16 v237, v45, v45\n"
    "v_dot2c_f32_bf16 v236, s20, v46\n"
    "v_dot2c_f32_bf16 v237, v46, v46\n"
    "v_dot2c_f32_bf16 v236, s20, v47\n"
    "v_dot2c_f32_bf16 v237, v47, v47\n"
    "v_mfma_f32_32x32x16_bf16 v[128:143], v[176:179], v[48:51], v[128:143]\n"
    "ds_read_b128 v[176:179], v242 offset:48128\n"
    "s_waitcnt vmcnt(8)\n"
    "ds_read2st64_b32 v[56:57], v252 offset0:152 offset1:153\n"
    "ds_read2st64_b32 v[58:59], v254 offset0:152 offset1:153\n"
    "ds_read2st64_b32 v[60:61], v252 offset0:156 offset1:157\n"
    "ds_read2st64_b32 v[62:63], v254 offset0:156 offset1:157\n"
    "v_mfma_f32_32x32x16_bf16 v[144:159], v[180:183], v[48:51], v[144:159]\n"
    "ds_read_b128 v[180:183], v242 offset:15360\n"
    "s_add_u32 s11, s8, 24576\n"
    "s_add_u32 m0, s10, 32768\n"
    "s_nop 0\n"
    "buffer_load_dwordx4 v246, s[40:43], s11 offen offset:0 sc1 lds\n"
    "s_waitcnt lgkmcnt(8)\n"
    "v_mfma_f32_32x32x16_bf16 v[128:143], v[184:187], v[52:55], v[128:143]\n"
    "ds_read_b128 v[184:187], v242 offset:49152\n"
    "buffer_load_dwordx4 v246, s[40:43], s11 offen offset:1024 sc1 lds\n"
    "v_mfma_f32_32x32x16_bf16 v[144:159], v[188:191], v[52:55], v[144:159]\n"
    "ds_read_b128 v[188:191], v242 offset:16384\n"
    "s_waitcnt lgkmcnt(3)\n"
    "v_max3_u32 v251, v56, v57, v58\n"
    "v_max3_u32 v247, v60, v61, v62\n"
    "v_max3_u32 v251, v251, v59, v63\n"
    "v_max_u32 v251, v251, v247\n"
    "v_cmp_eq_u32 vcc, -1, v251\n"
    "s_cbranch_vccnz .Lk_rex7\n"
    ".Lk_gox7:\n"
    "v_dot2c_f32_bf16 v236, s20, v48\n"
    "v_dot2c_f32_bf16 v237, v48, v48\n"
    "v_dot2c_f32_bf16 v236, s20, v49\n"
    "v_dot2c_f32_bf16 v237, v49, v49\n"
    "v_dot2c_f32_bf16 v236, s20, v50\n"
    "v_dot2c_f32_bf16 v237, v50, v50\n"
    "v_dot2c_f32_bf16 v236, s20, v51\n"
    "v_dot2c_f32_bf16 v237, v51, v51\n"
    "v_dot2c_f32_bf16 v236, s20, v52\n"
    "v_dot2c_f32_bf16 v237, v52, v52\n"
    "v_dot2c_f32_bf16 v236, s20, v53\n"
    "v_dot2c_f32_bf16 v237, v53, v53\n"
    "v_dot2c_f32_bf16 v236, s20, v54\n"
    "v_dot2c_f32_bf16 v237, v54, v54\n"
    "v_dot2c_f32_bf16 v236, s20, v55\n"
    "v_dot2c_f32_bf16 v237, v55, v55\n"
    "v_mfma_f32_32x32x16_bf16 v[128:143], v[192:195], v[56:59], v[128:143]\n"
    "ds_read_b128 v[192:195], v242 offset:50176\n"
    "s_waitcnt vmcnt(8)\n"
    "ds_read2st64_b32 v[64:65], v252 offset0:0 offset1:1\n"
    "ds_read2st64_b32 v[66:67], v254 offset0:0 offset1:1\n"
    "ds_read2st64_b32 v[68:69], v252 offset0:4 offset1:5\n"
    "ds_read2st64_b32 v[70:71], v254 offset0:4 offset1:5\n"
    "v_mfma_f32_32x32x16_bf16 v[144:159], v[196:199], v[56:59], v[144:159]\n"
    "ds_read_b128 v[196:199], v242 offset:17408\n"
    "buffer_load_dwordx4 v246, s[40:43], s11 offen offset:2048 sc1 lds\n"
    "s_waitcnt lgkmcnt(8)\n"
    "v_mfma_f32_32x32x16_bf16 v[128:143], v[176:179], v[60:63], v[128:143]\n"
    "ds_read_b128 v[176:179], v242 offset:51200\n"
    "buffer_load_dwordx4 v246, s[40:43], s11 offen offset:3072 sc1 lds\n"
    "v_mfma_f32_32x32x16_bf16 v[144:159], v[180:183], v[60:63], v[144:159]\n"
    "ds_read_b128 v[180:183], v242 offset:18432\n"
    "s_waitcnt lgkmcnt(3)\n"
    "v_max3_u32 v251, v64, v65, v66\n"
    "v_max3_u32 v247, v68, v69, v70\n"
    "v_max3_u32 v251, v251, v67, v71\n"
    "v_max_u32 v251, v251, v247\n"
    "v_cmp_eq_u32 vcc, -1, v251\n"
    "s_cbranch_vccnz .Lk_rex8\n"
    ".Lk_gox8:\n"
    "v_dot2c_f32_bf16 v236, s20, v56\n"
    "v_dot2c_f32_bf16 v237, v56, v56\n"
    "v_dot2c_f32_bf16 v236, s20, v57\n"
    "v_dot2c_f32_bf16 v237, v57, v57\n"
    "v_dot2c_f32_bf16 v236, s20, v58\n"
    "v_dot2c_f32_bf16 v237, v58, v58\n"
    "v_dot2c_f32_bf16 v236, s20, v59\n"
    "v_dot2c_f32_bf16 v237, v59, v59\n"
    "v_dot2c_f32_bf16 v236, s20, v60\n"
    "v_dot2c_f32_bf16 v237, v60, v60\n"
    "v_dot2c_f32_bf16 v236, s20, v61\n"
    "v_dot2c_f32_bf16 v237, v61, v61\n"
    "v_dot2c_f32_bf16 v236, s20, v62\n"
    "v_dot2c_f32_bf16 v237, v62, v62\n"
    "v_dot2c_f32_bf16 v236, s20, v63\n"
    "v_dot2c_f32_bf16 v237, v63, v63\n"
    "v_mfma_f32_32x32x16_bf16 v[128:143], v[184:187], v[64:67], v[128:143]\n"
    "ds_read_b128 v[184:187], v242 offset:52224\n"
    "s_waitcnt vmcnt(8)\n"
    "ds_read2st64_b32 v[72:73], v252 offset0:8 offset1:9\n"
    "ds_read2st64_b32 v[74:75], v254 offset0:8 offset1:9\n"
    "ds_read2st64_b32 v[76:77], v252 offset0:12 offset1:13\n"
    "ds_read2st64_b32 v[78:79], v254 offset0:12 offset1:13\n"
    "v_mfma_f32_32x32x16_bf16 v[144:159], v[188:191], v[64:67], v[144:159]\n"
    "ds_read_b128 v[188:191], v242 offset:19456\n"
    "s_add_u32 s11, s8, 28672\n"
    "s_add_u32 m0, s10, 36864\n"
    "s_nop 0\n"
    "buffer_load_dwordx4 v246, s[40:43], s11 offen offset:0 sc1 lds\n"
    "s_waitcnt lgkmcnt(8)\n"
    "v_mfma_f32_32x32x16_bf16 v[128:143], v[192:195], v[68:71], v[128:143]\n"
    "ds_read_b128 v[192:195], v242 offset:53248\n"
    "buffer_load_dwordx4 v246, s[40:43], s11 offen offset:1024 sc1 lds\n"
    "v_mfma_f32_32x32x16_bf16 v[144:159], v[196:199], v[68:71], v[144:159]\n"
    "ds_read_b128 v[196:199], v242 offset:20480\n"
    "s_waitcnt lgkmcnt(3)\n"
    "v_max3_u32 v251, v72, v73, v74\n"
    "v_max3_u32 v247, v76, v77, v78\n"
    "v_max3_u32 v251, v251, v75, v79\n"
    "v_max_u32 v251, v251, v247\n"
    "v_cmp_eq_u32 vcc, -1, v251\n"
    "s_cbranch_vccnz .Lk_rex9\n"
    ".Lk_gox9:\n"
    "v_dot2c_f32_bf16 v236, s20, v64\n"
    "v_dot2c_f32_bf16 v237, v64, v64\n"
    "v_dot2c_f32_bf16 v236, s20, v65\n"
    "v_dot2c_f32_bf16 v237, v65, v65\n"
    "v_dot2c_f32_bf16 v236, s20, v66\n"
    "v_dot2c_f32_bf16 v237, v66, v66\n"
    "v_dot2c_f32_bf16 v236, s20, v67\n"
    "v_dot2c_f32_bf16 v237, v67, v67\n"
    "v_dot2c_f32_bf16 v236, s20, v68\n"
    "v_dot2c_f32_bf16 v237, v68, v68\n"
    "v_dot2c_f32_bf16 v236, s20, v69\n"
    "v_dot2c_f32_bf16 v237, v69, v69\n"
    "v_dot2c_f32_bf16 v236, s20, v70\n"
    "v_dot2c_f32_bf16 v237, v70, v70\n"
    "v_dot2c_f32_bf16 v236, s20, v71\n"
    "v_dot2c_f32_bf16 v237, v71, v71\n"
    "v_mfma_f32_32x32x16_bf16 v[128:143], v[176:179], v[72:75], v[128:143]\n"
    "ds_read_b128 v[176:179], v242 offset:54272\n"
    "s_waitcnt vmcnt(8)\n"
    "ds_read2st64_b32 v[80:81], v252 offset0:16 offset1:17\n"
    "ds_read2st64_b32 v[82:83], v254 offset0:16 offset1:17\n"
    "ds_read2st64_b32 v[84:85], v252 offset0:20 offset1:21\n"
    "ds_read2st64_b32 v[86:87], v254 offset0:20 offset1:21\n"
    "v_mfma_f32_32x32x16_bf16 v[144:159], v[180:183], v[72:75], v[144:159]\n"
    "ds_read_b128 v[180:183], v242 offset:21504\n"
    "buffer_load_dwordx4 v246, s[40:43], s11 offen offset:2048 sc1 lds\n"
    "s_waitcnt lgkmcnt(8)\n"
    "v_mfma_f32_32x32x16_bf16 v[128:143], v[184:187], v[76:79], v[128:143]\n"
    "ds_read_b128 v[184:187], v242 offset:55296\n"
    "buffer_load_dwordx4 v246, s[40:43], s11 offen offset:3072 sc1 lds\n"
    "v_mfma_f32_32x32x16_bf16 v[144:159], v[188:191], v[76:79], v[144:159]\n"
    "ds_read_b128 v[188:191], v242 offset:22528\n"
    "s_waitcnt lgkmcnt(3)\n"
    "v_max3_u32 v251, v80, v81, v82\n"
    "v_max3_u32 v247, v84, v85, v86\n"
    "v_max3_u32 v251, v251, v83, v87\n"
    "v_max_u32 v251, v251, v247\n"
    "v_cmp_eq_u32 vcc, -1, v251\n"
    "s_cbranch_vccnz .Lk_rex10\n"
    ".Lk_gox10:\n"
    "v_dot2c_f32_bf16 v236, s20, v72\n"
    "v_dot2c_f32_bf16 v237, v72, v72\n"
    "v_dot2c_f32_bf16 v236, s20, v73\n"
    "v_dot2c_f32_bf16 v237, v73, v73\n"
    "v_dot2c_f32_bf16 v236, s20, v74\n"
    "v_dot2c_f32_bf16 v237, v74, v74\n"
    "v_dot2c_f32_bf16 v236, s20, v75\n"
    "v_dot2c_f32_bf16 v237, v75, v75\n"
    "v_dot2c_f32_bf16 v236, s20, v76\n"
    "v_dot2c_f32_bf16 v237, v76, v76\n"
    "v_dot2c_f32_bf16 v236, s20, v77\n"
    "v_dot2c_f32_bf16 v237, v77, v77\n"
    "v_dot2c_f32_bf16 v236, s20, v78\n"
    "v_dot2c_f32_bf16 v237, v78, v78\n"
    "v_dot2c_f32_bf16 v236, s20, v79\n"
    "v_dot2c_f32_bf16 v237, v79, v79\n"
    "v_mfma_f32_32x32x16_bf16 v[128:143], v[192:195], v[80:83], v[128:143]\n"
    "ds_read_b128 v[192:195], v242 offset:56320\n"
    "s_waitcnt vmcnt(8)\n"
    "ds_read2st64_b32 v[88:89], v252 offset0:24 offset1:25\n"
    "ds_read2st64_b32 v[90:91], v254 offset0:24 offset1:25\n"
    "ds_read2st64_b32 v[92:93], v252 offset0:28 offset1:29\n"
    "ds_read2st64_b32 v[94:95], v254 offset0:28 offset1:29\n"
    "v_mfma_f32_32x32x16_bf16 v[144:159], v[196:199], v[80:83], v[144:159]\n"
    "ds_read_b128 v[196:199], v242 offset:23552\n"
    "s_add_u32 s12, s9, 65536\n"
    "s_add_u32 m0, s10, 0\n"
    "s_nop 0\n"
    "buffer_load_dwordx4 v246, s[40:43], s12 offen offset:0 sc1 lds\n"
    "s_waitcnt lgkmcnt(8)\n"
    "v_mfma_f32_32x32x16_bf16 v[128:143], v[176:179], v[84:87], v[128:143]\n"
    "ds_read_b128 v[176:179], v242 offset:57344\n"
    "buffer_load_dwordx4 v246, s[40:43], s12 offen offset:1024 sc1 lds\n"
    "v_mfma_f32_32x32x16_bf16 v[144:159], v[180:183], v[84:87], v[144:159]\n"
    "ds_read_b128 v[180:183], v242 offset:24576\n"
    "s_waitcnt lgkmcnt(3)\n"
    "v_max3_u32 v251, v88, v89, v90\n"
    "v_max3_u32 v247, v92, v93, v94\n"
    "v_max3_u32 v251, v251, v91, v95\n"
    "v_max_u32 v251, v251, v247\n"
    "v_cmp_eq_u32 vcc, -1, v251\n"
    "s_cbranch_vccnz .Lk_rex11\n"
    ".Lk_gox11:\n"
    "v_dot2c_f32_bf16 v236, s20, v80\n"
    "v_dot2c_f32_bf16 v237, v80, v80\n"
    "v_dot2c_f32_bf16 v236, s20, v81\n"
    "v_dot2c_f32_bf16 v237, v81, v81\n"
    "v_dot2c_f32_bf16 v236, s20, v82\n"
    "v_dot2c_f32_bf16 v237, v82, v82\n"
    "v_dot2c_f32_bf16 v236, s20, v83\n"
    "v_dot2c_f32_bf16 v237, v83, v83\n"
    "v_dot2c_f32_bf16 v236, s20, v84\n"
    "v_dot2c_f32_bf16 v237, v84, v84\n"
    "v_dot2c_f32_bf16 v236, s20, v85\n"
    "v_dot2c_f32_bf16 v237, v85, v85\n"
    "v_dot2c_f32_bf16 v236, s20, v86\n"
    "v_dot2c_f32_bf16 v237, v86, v86\n"
    "v_dot2c_f32_bf16 v236, s20, v87\n"
    "v_dot2c_f32_bf16 v237, v87, v87\n"
    "v_mfma_f32_32x32x16_bf16 v[128:143], v[184:187], v[88:91], v[128:143]\n"
    "ds_read_b128 v[184:187], v242 offset:58368\n"
    "s_waitcnt vmcnt(8)\n"
    "ds_read2st64_b32 v[96:97], v252 offset0:128 offset1:129\n"
    "ds_read2st64_b32 v[98:99], v254 offset0:128 offset1:129\n"
    "ds_read2st64_b32 v[100:101], v252 offset0:132 offset1:133\n"
    "ds_read2st64_b32 v[102:103], v254 offset0:132 offset1:133\n"
    "v_mfma_f32_32x32x16_bf16 v[144:159], v[188:191], v[88:91], v[144:159]\n"
    "ds_read_b128 v[188:191], v242 offset:25600\n"
    "buffer_load_dwordx4 v246, s[40:43], s12 offen offset:2048 sc1 lds\n"
    "s_waitcnt lgkmcnt(8)\n"
    "v_mfma_f32_32x32x16_bf16 v[128:143], v[192:195], v[92:95], v[128:143]\n"
    "ds_read_b128 v[192:195], v242 offset:59392\n"
    "buffer_load_dwordx4 v246, s[40:43], s12 offen offset:3072 sc1 lds\n"
    "v_mfma_f32_32x32x16_bf16 v[144:159], v[196:199], v[92:95], v[144:159]\n"
    "ds_read_b128 v[196:199], v242 offset:26624\n"
    "v_mov_b32 v232, -1\n"
    "v_mov_b32 v233, -1\n"
    "v_mov_b32 v234, -1\n"
    "v_mov_b32 v235, -1\n"
    "buffer_store_dwordx4 v[232:235], v246, s[40:43], s82 offen offset:3072 sc1\n"
    "s_waitcnt lgkmcnt(3)\n"
    "v_max3_u32 v251, v96, v97, v98\n"
    "v_max3_u32 v247, v100, v101, v102\n"
    "v_max3_u32 v251, v251, v99, v103\n"
    "v_max_u32 v251, v251, v247\n"
    "v_cmp_eq_u32 vcc, -1, v251\n"
    "s_cbranch_vccnz .Lk_rex12\n"
    ".Lk_gox12:\n"
    "v_dot2c_f32_bf16 v236, s20, v88\n"
    "v_dot2c_f32_bf16 v237, v88, v88\n"
    "v_dot2c_f32_bf16 v236, s20, v89\n"
    "v_dot2c_f32_bf16 v237, v89, v89\n"
    "v_dot2c_f32_bf16 v236, s20, v90\n"
    "v_dot2c_f32_bf16 v237, v90, v90\n"
    "v_dot2c_f32_bf16 v236, s20, v91\n"
    "v_dot2c_f32_bf16 v237, v91, v91\n"
    "v_dot2c_f32_bf16 v236, s20, v92\n"
    "v_dot2c_f32_bf16 v237, v92, v92\n"
    "v_dot2c_f32_bf16 v236, s20, v93\n"
    "v_dot2c_f32_bf16 v237, v93, v93\n"
    "v_dot2c_f32_bf16 v236, s20, v94\n"
    "v_dot2c_f32_bf16 v237, v94, v94\n"
    "v_dot2c_f32_bf16 v236, s20, v95\n"
    "v_dot2c_f32_bf16 v237, v95, v95\n"
    "v_mfma_f32_32x32x16_bf16 v[128:143], v[176:179], v[96:99], v[128:143]\n"
    "ds_read_b128 v[176:179], v242 offset:60416\n"
    "s_waitcnt vmcnt(9)\n"
    "ds_read2st64_b32 v[104:105], v252 offset0:136 offset1:137\n"
    "ds_read2st64_b32 v[106:107], v254 offset0:136 offset1:137\n"
    "ds_read2st64_b32 v[108:109], v252 offset0:140 offset1:141\n"
    "ds_read2st64_b32 v[110:111], v254 offset0:140 offset1:141\n"
    "v_mfma_f32_32x32x16_bf16 v[144:159], v[180:183], v[96:99], v[144:159]\n"
    "ds_read_b128 v[180:183], v242 offset:27648\n"
    "s_add_u32 s12, s9, 69632\n"
    "s_add_u32 m0, s10, 4096\n"
    "s_nop 0\n"
    "buffer_load_dwordx4 v246, s[40:43], s12 offen offset:0 sc1 lds\n"
    "s_waitcnt lgkmcnt(8)\n"
    "v_mfma_f32_32x32x16_bf16 v[128:143], v[184:187], v[100:103], v[128:143]\n"
    "ds_read_b128 v[184:187], v242 offset:61440\n"
    "buffer_load_dwordx4 v246, s[40:43], s12 offen offset:1024 sc1 lds\n"
    "v_mfma_f32_32x32x16_bf16 v[144:159], v[188:191], v[100:103], v[144:159]\n"
    "ds_read_b128 v[188:191], v242 offset:28672\n"
    "buffer_store_dwordx4 v[232:235], v246, s[40:43], s83 offen offset:0 sc1\n"
    "s_waitcnt lgkmcnt(3)\n"
    "v_max3_u32 v251, v104, v105, v106\n"
    "v_max3_u32 v247, v108, v109, v110\n"
    "v_max3_u32 v251, v251, v107, v111\n"
    "v_max_u32 v251, v251, v247\n"
    "v_cmp_eq_u32 vcc, -1, v251\n"
    "s_cbranch_vccnz .Lk_rex13\n"
    ".Lk_gox13:\n"
    "v_dot2c_f32_bf16 v236, s20, v96\n"
    "v_dot2c_f32_bf16 v237, v96, v96\n"
    "v_dot2c_f32_bf16 v236, s20, v97\n"
    "v_dot2c_f32_bf16 v237, v97, v97\n"
    "v_dot2c_f32_bf16 v236, s20, v98\n"
    "v_dot2c_f32_bf16 v237, v98, v98\n"
    "v_dot2c_f32_bf16 v236, s20, v99\n"
    "v_dot2c_f32_bf16 v237, v99, v99\n"
    "v_dot2c_f32_bf16 v236, s20, v100\n"
    "v_dot2c_f32_bf16 v237, v100, v100\n"
    "v_dot2c_f32_bf16 v236, s20, v101\n"
    "v_dot2c_f32_bf16 v237, v101, v101\n"
    "v_dot2c_f32_bf16 v236, s20, v102\n"
    "v_dot2c_f32_bf16 v237, v102, v102\n"
    "v_dot2c_f32_bf16 v236, s20, v103\n"
    "v_dot2c_f32_bf16 v237, v103, v103\n"
    "v_mfma_f32_32x32x16_bf16 v[128:143], v[192:195], v[104:107], v[128:143]\n"
    "ds_read_b128 v[192:195], v242 offset:62464\n"
    "s_waitcnt vmcnt(10)\n"
    "ds_read2st64_b32 v[112:113], v252 offset0:144 offset1:145\n"
    "ds_read2st64_b32 v[114:115], v254 offset0:144 offset1:145\n"
    "ds_read2st64_b32 v[116:117], v252 offset0:148 offset1:149\n"
    "ds_read2st64_b32 v[118:119], v254 offset0:148 offset1:149\n"
    "v_mfma_f32_32x32x16_bf16 v[144:159], v[196:199], v[104:107], v[144:159]\n"
    "ds_read_b128 v[196:199], v242 offset:29696\n"
    "buffer_load_dwordx4 v246, s[40:43], s12 offen offset:2048 sc1 lds\n"
    "s_waitcnt lgkmcnt(8)\n"
    "v_mfma_f32_32x32x16_bf16 v[128:143], v[176:179], v[108:111], v[128:143]\n"
    "ds_read_b128 v[176:179], v242 offset:63488\n"
    "buffer_load_dwordx4 v246, s[40:43], s12 offen offset:3072 sc1 lds\n"
    "v_mfma_f32_32x32x16_bf16 v[144:159], v[180:183], v[108:111], v[144:159]\n"
    "ds_read_b128 v[180:183], v242 offset:30720\n"
    "buffer_store_dwordx4 v[232:235], v246, s[40:43], s83 offen offset:1024 sc1\n"
    "s_waitcnt lgkmcnt(3)\n"
    "v_max3_u32 v251, v112, v113, v114\n"
    "v_max3_u32 v247, v116, v117, v118\n"
    "v_max3_u32 v251, v251, v115, v119\n"
    "v_max_u32 v251, v251, v247\n"
    "v_cmp_eq_u32 vcc, -1, v251\n"
    "s_cbranch_vccnz .Lk_rex14\n"
    ".Lk_gox14:\n"
    "v_dot2c_f32_bf16 v236, s20, v104\n"
    "v_dot2c_f32_bf16 v237, v104, v104\n"
    "v_dot2c_f32_bf16 v236, s20, v105\n"
    "v_dot2c_f32_bf16 v237, v105, v105\n"
    "v_dot2c_f32_bf16 v236, s20, v106\n"
    "v_dot2c_f32_bf16 v237, v106, v106\n"
    "v_dot2c_f32_bf16 v236, s20, v107\n"
    "v_dot2c_f32_bf16 v237, v107, v107\n"
    "v_dot2c_f32_bf16 v236, s20, v108\n"
    "v_dot2c_f32_bf16 v237, v108, v108\n"
    "v_dot2c_f32_bf16 v236, s20, v109\n"
    "v_dot2c_f32_bf16 v237, v109, v109\n"
    "v_dot2c_f32_bf16 v236, s20, v110\n"
    "v_dot2c_f32_bf16 v237, v110, v110\n"
    "v_dot2c_f32_bf16 v236, s20, v111\n"
    "v_dot2c_f32_bf16 v237, v111, v111\n"
    "v_mfma_f32_32x32x16_bf16 v[128:143], v[184:187], v[112:115], v[128:143]\n"
    "ds_read_b128 v[184:187], v242 offset:64512\n"
    "s_waitcnt vmcnt(11)\n"
    "ds_read2st64_b32 v[120:121], v252 offset0:152 offset1:153\n"
    "ds_read2st64_b32 v[122:123], v254 offset0:152 offset1:153\n"
    "ds_read2st64_b32 v[124:125], v252 offset0:156 offset1:157\n"
    "ds_read2st64_b32 v[126:127], v254 offset0:156 offset1:157\n"
    "v_mfma_f32_32x32x16_bf16 v[144:159], v[188:191], v[112:115], v[144:159]\n"
    "ds_read_b128 v[188:191], v242 offset:31744\n"
    "s_add_u32 s12, s9, 98304\n"
    "s_add_u32 m0, s10, 32768\n"
    "s_nop 0\n"
    "buffer_load_dwordx4 v246, s[40:43], s12 offen offset:0 sc1 lds\n"
    "s_waitcnt vmcnt(2)\n"
    "s_barrier\n"
    "s_waitcnt lgkmcnt(8)\n"
    "v_mfma_f32_32x32x16_bf16 v[128:143], v[192:195], v[116:119], v[128:143]\n"
    "buffer_load_dwordx4 v246, s[40:43], s12 offen offset:1024 sc1 lds\n"
    "ds_read_b128 v[192:195], v246 offset:0\n"
    "v_mfma_f32_32x32x16_bf16 v[144:159], v[196:199], v[116:119], v[144:159]\n"
    "buffer_store_dwordx4 v[232:235], v246, s[40:43], s83 offen offset:2048 sc1\n"
    "s_waitcnt lgkmcnt(2)\n"
    "v_max3_u32 v251, v120, v121, v122\n"
    "v_max3_u32 v247, v124, v125, v126\n"
    "v_max3_u32 v251, v251, v123, v127\n"
    "v_max_u32 v251, v251, v247\n"
    "v_cmp_eq_u32 vcc, -1, v251\n"
    "s_cbranch_vccnz .Lk_rex15\n"
    ".Lk_gox15:\n"
    "v_dot2c_f32_bf16 v236, s20, v112\n"
    "v_dot2c_f32_bf16 v237, v112, v112\n"
    "v_dot2c_f32_bf16 v236, s20, v113\n"
    "v_dot2c_f32_bf16 v237, v113, v113\n"
    "v_dot2c_f32_bf16 v236, s20, v114\n"
    "v_dot2c_f32_bf16 v237, v114, v114\n"
    "v_dot2c_f32_bf16 v236, s20, v115\n"
    "v_dot2c_f32_bf16 v237, v115, v115\n"
    "v_dot2c_f32_bf16 v236, s20, v116\n"
    "v_dot2c_f32_bf16 v237, v116, v116\n"
    "v_dot2c_f32_bf16 v236, s20, v117\n"
    "v_dot2c_f32_bf16 v237, v117, v117\n"
    "v_dot2c_f32_bf16 v236, s20, v118\n"
    "v_dot2c_f32_bf16 v237, v118, v118\n"
    "v_dot2c_f32_bf16 v236, s20, v119\n"
    "v_dot2c_f32_bf16 v237, v119, v119\n"
    "ds_read_b128 v[196:199], v246 offset:1024\n"
    "v_mfma_f32_32x32x16_bf16 v[128:143], v[176:179], v[120:123], v[128:143]\n"
    "ds_read_b128 v[176:179], v246 offset:2048\n"
    "v_mfma_f32_32x32x16_bf16 v[144:159], v[180:183], v[120:123], v[144:159]\n"
    "buffer_load_dwordx4 v246, s[40:43], s12 offen offset:2048 sc1 lds\n"
    "ds_read_b128 v[180:183], v246 offset:3072\n"
    "s_waitcnt lgkmcnt(4)\n"
    "v_mfma_f32_32x32x16_bf16 v[128:143], v[184:187], v[124:127], v[128:143]\n"
    "buffer_load_dwordx4 v246, s[40:43], s12 offen offset:3072 sc1 lds\n"
    "ds_read_b128 v[184:187], v246 offset:4096\n"
    "v_mfma_f32_32x32x16_bf16 v[144:159], v[188:191], v[124:127], v[144:159]\n"
    "v_dot2c_f32_bf16 v236, s20, v120\n"
    "v_dot2c_f32_bf16 v237, v120, v120\n"
    "v_dot2c_f32_bf16 v236, s20, v121\n"
    "v_dot2c_f32_bf16 v237, v121, v121\n"
    "v_dot2c_f32_bf16 v236, s20, v122\n"
    "v_dot2c_f32_bf16 v237, v122, v122\n"
    "v_dot2c_f32_bf16 v236, s20, v123\n"
    "v_dot2c_f32_bf16 v237, v123, v123\n"
    "v_dot2c_f32_bf16 v236, s20, v124\n"
    "v_dot2c_f32_bf16 v237, v124, v124\n"
    "v_dot2c_f32_bf16 v236, s20, v125\n"
    "v_dot2c_f32_bf16 v237, v125, v125\n"
    "v_dot2c_f32_bf16 v236, s20, v126\n"
    "v_dot2c_f32_bf16 v237, v126, v126\n"
    "v_dot2c_f32_bf16 v236, s20, v127\n"
    "v_dot2c_f32_bf16 v237, v127, v127\n"
    "ds_read_b128 v[188:191], v246 offset:5120\n"
    "s_waitcnt lgkmcnt(4)\n"
    "v_mfma_f32_32x32x16_bf16 v[160:175], v[192:195], v[0:3], 0\n"
    "ds_read_b128 v[192:195], v246 offset:6144\n"
    "s_add_u32 s12, s9, 102400\n"
    "s_add_u32 m0, s10, 36864\n"
    "s_nop 0\n"
    "buffer_load_dwordx4 v246, s[40:43], s12 offen offset:0 sc1 lds\n"
    "ds_read_b128 v[200:203], v249 offset:0\n"
    "ds_read_b128 v[204:207], v249 offset:896\n"
    "ds_read_b128 v[208:211], v249 offset:64\n"
    "ds_read_b128 v[212:215], v249 offset:960\n"
    "v_mfma_f32_32x32x16_bf16 v[160:175], v[196:199], v[4:7], v[160:175]\n"
    "ds_read_b128 v[196:199], v246 offset:7168\n"
    "buffer_load_dwordx4 v246, s[40:43], s12 offen offset:1024 sc1 lds\n"
    "s_nop 2\n"
    "v_mov_b32 v238, v236\n"
    "v_mov_b32 v239, v237\n"
    "s_nop 1\n"
    "v_permlane32_swap_b32 v236, v238\n"
    "s_waitcnt lgkmcnt(8)\n"
    "v_mfma_f32_32x32x16_bf16 v[160:175], v[176:179], v[8:11], v[160:175]\n"
    "ds_read_b128 v[176:179], v246 offset:8192\n"
    "buffer_load_dwordx4 v246, s[40:43], s12 offen offset:2048 sc1 lds\n"
    "v_permlane32_swap_b32 v237, v239\n"
    "v_add_f32 v236, v236, v238\n"
    "v_add_f32 v237, v237, v239\n"
    "v_mul_f32 v236, 0x3b000000, v236\n"
    "v_mfma_f32_32x32x16_bf16 v[160:175], v[180:183], v[12:15], v[160:175]\n"
    "ds_read_b128 v[180:183], v246 offset:9216\n"
    "buffer_load_dwordx4 v246, s[40:43], s12 offen offset:3072 sc1 lds\n"
    "v_mul_f32 v237, 0x3b000000, v237\n"
    "v_fma_f32 v237, -v236, v236, v237\n"
    "v_max_f32 v237, 0, v237\n"
    "v_cvt_pk_bf16_f32 v237, v237, v237\n"
    "v_lshlrev_b32 v237, 16, v237\n"
    "s_waitcnt lgkmcnt(8)\n"
    "v_mfma_f32_32x32x16_bf16 v[160:175], v[184:187], v[16:19], v[160:175]\n"
    "ds_read_b128 v[184:187], v246 offset:10240\n"
    "v_add_f32 v237, 0x3727c5ac, v237\n"
    "v_cvt_pk_bf16_f32 v237, v237, v237\n"
    "v_lshlrev_b32 v237, 16, v237\n"
    "v_sqrt_f32 v237, v237\n"
    "v_cvt_pk_bf16_f32 v236, v236, v236\n"
    "v_mfma_f32_32x32x16_bf16 v[160:175], v[188:191], v[20:23], v[160:175]\n"
    "ds_read_b128 v[188:191], v246 offset:11264\n"
    "buffer_load_dwordx4 a[0:3], v246, s[40:43], s78 offen offset:0 sc1\n"
    "v_lshlrev_b32 v236, 16, v236\n"
    "v_cvt_pk_bf16_f32 v237, v237, v237\n"
    "v_lshlrev_b32 v237, 16, v237\n"
    "v_rcp_f32 v238, v237\n"
    "s_nop 0\n"
    "s_waitcnt lgkmcnt(4)\n"
    "v_mfma_f32_32x32x16_bf16 v[160:175], v[192:195], v[24:27], v[160:175]\n"
    "ds_read_b128 v[192:195], v246 offset:12288\n"
    "v_mul_f32 v239, 0xbfb8aa3b, v238\n"
    "v_mul_f32_e64 v240, -v236, v238\n"
    "v_fmac_f32 v204, v240, v200\n"
    "v_fmac_f32 v205, v240, v201\n"
    "v_mfma_f32_32x32x16_bf16 v[160:175], v[196:199], v[28:31], v[160:175]\n"
    "ds_read_b128 v[196:199], v246 offset:13312\n"
    "buffer_load_dwordx4 a[4:7], v246, s[40:43], s79 offen offset:0 sc1\n"
    "v_fmac_f32 v206, v240, v202\n"
    "v_fmac_f32 v207, v240, v203\n"
    "ds_read_b128 v[216:219], v249 offset:32\n"
    "ds_read_b128 v[220:223], v249 offset:928\n"
    "ds_read_b128 v[224:227], v249 offset:96\n"
    "s_barrier\n"
    "s_waitcnt lgkmcnt(7)\n"
    "v_mfma_f32_32x32x16_bf16 v[160:175], v[176:179], v[32:35], v[160:175]\n"
    "ds_read_b128 v[176:179], v246 offset:14336\n"
    "ds_read_b128 v[228:231], v249 offset:992\n"
    "v_fmac_f32 v204, v239, v128\n"
    "v_fmac_f32 v205, v239, v129\n"
    "v_fmac_f32 v206, v239, v130\n"
    "v_fmac_f32 v207, v239, v131\n"
    "v_mfma_f32_32x32x16_bf16 v[160:175], v[180:183], v[36:39], v[160:175]\n"
    "ds_read_b128 v[180:183], v246 offset:15360\n"
    "buffer_load_dwordx4 a[8:11], v243, s[40:43], s78 offen offset:0 sc1\n"
    "v_exp_f32 v204, v204\n"
    "v_exp_f32 v205, v205\n"
    "s_waitcnt lgkmcnt(8)\n"
    "v_mfma_f32_32x32x16_bf16 v[160:175], v[184:187], v[40:43], v[160:175]\n"
    "ds_read_b128 v[184:187], v246 offset:16384\n"
    "s_add_u32 s12, s9, 131072\n"
    "s_add_u32 m0, s10, 98304\n"
    "s_nop 0\n"
    "buffer_load_dwordx4 v246, s[40:43], s12 offen offset:0 sc1 lds\n"
    "v_exp_f32 v206, v206\n"
    "v_exp_f32 v207, v207\n"
    "v_mfma_f32_32x32x16_bf16 v[160:175], v[188:191], v[44:47], v[160:175]\n"
    "ds_read_b128 v[188:191], v246 offset:17408\n"
    "v_fmac_f32 v212, v240, v208\n"
    "v_fmac_f32 v213, v240, v209\n"
    "v_fmac_f32 v214, v240, v210\n"
    "v_fmac_f32 v215, v240, v211\n"
    "v_add_f32 v204, 1.0, v204\n"
    "v_add_f32 v205, 1.0, v205\n"
    "s_waitcnt lgkmcnt(8)\n"
    "v_mfma_f32_32x32x16_bf16 v[160:175], v[192:195], v[48:51], v[160:175]\n"
    "ds_read_b128 v[192:195], v246 offset:18432\n"
    "buffer_load_dwordx4 v246, s[40:43], s12 offen offset:1024 sc1 lds\n"
    "v_add_f32 v206, 1.0, v206\n"
    "v_add_f32 v207, 1.0, v207\n"
    "v_rcp_f32 v204, v204\n"
    "v_mfma_f32_32x32x16_bf16 v[160:175], v[196:199], v[52:55], v[160:175]\n"
    "ds_read_b128 v[196:199], v246 offset:19456\n"
    "v_rcp_f32 v205, v205\n"
    "v_rcp_f32 v206, v206\n"
    "v_rcp_f32 v207, v207\n"
    "s_waitcnt lgkmcnt(4)\n"
    "v_mfma_f32_32x32x16_bf16 v[160:175], v[176:179], v[56:59], v[160:175]\n"
    "ds_read_b128 v[176:179], v246 offset:20480\n"
    "buffer_load_dwordx4 v246, s[40:43], s12 offen offset:2048 sc1 lds\n"
    "v_fmac_f32 v212, v238, v136\n"
    "v_fmac_f32 v213, v238, v137\n"
    "v_fmac_f32 v214, v238, v138\n"
    "v_fmac_f32 v215, v238, v139\n"
    "v_mfma_f32_32x32x16_bf16 v[160:175], v[180:183], v[60:63], v[160:175]\n"
    "ds_read_b128 v[180:183], v246 offset:21504\n"
    "v_mul_f32 v212, v212, v204\n"
    "v_mul_f32 v213, v213, v205\n"
    "v_mul_f32 v214, v214, v206\n"
    "v_mul_f32 v215, v215, v207\n"
    "v_cvt_pk_bf16_f32 v232, v212, v213\n"
    "v_cvt_pk_bf16_f32 v233, v214, v215\n"
    "s_waitcnt lgkmcnt(4)\n"
    "v_mfma_f32_32x32x16_bf16 v[160:175], v[184:187], v[64:67], v[160:175]\n"
    "ds_read_b128 v[184:187], v246 offset:22528\n"
    "buffer_load_dwordx4 v246, s[40:43], s12 offen offset:3072 sc1 lds\n"
    "v_fmac_f32 v220, v240, v216\n"
    "v_fmac_f32 v221, v240, v217\n"
    "v_fmac_f32 v222, v240, v218\n"
    "v_mfma_f32_32x32x16_bf16 v[160:175], v[188:191], v[68:71], v[160:175]\n"
    "ds_read_b128 v[188:191], v246 offset:23552\n"
    "v_fmac_f32 v223, v240, v219\n"
    "v_fmac_f32 v220, v239, v132\n"
    "v_fmac_f32 v221, v239, v133\n"
    "v_fmac_f32 v222, v239, v134\n"
    "v_fmac_f32 v223, v239, v135\n"
    "ds_read_b128 v[200:203], v249 offset:128\n"
    "s_waitcnt lgkmcnt(5)\n"
    "v_mfma_f32_32x32x16_bf16 v[160:175], v[192:195], v[72:75], v[160:175]\n"
    "ds_read_b128 v[192:195], v246 offset:24576\n"
    "s_add_u32 s12, s9, 135168\n"
    "s_add_u32 m0, s10, 102400\n"
    "s_nop 0\n"
    "buffer_load_dwordx4 v246, s[40:43], s12 offen offset:0 sc1 lds\n"
    "ds_read_b128 v[204:207], v249 offset:1024\n"
    "ds_read_b128 v[208:211], v249 offset:192\n"
    "ds_read_b128 v[212:215], v249 offset:1088\n"
    "v_mfma_f32_32x32x16_bf16 v[160:175], v[196:199], v[76:79], v[160:175]\n"
    "ds_read_b128 v[196:199], v246 offset:25600\n"
    "v_exp_f32 v220, v220\n"
    "v_exp_f32 v221, v221\n"
    "v_exp_f32 v222, v222\n"
    "s_waitcnt lgkmcnt(8)\n"
    "v_mfma_f32_32x32x16_bf16 v[160:175], v[176:179], v[80:83], v[160:175]\n"
    "ds_read_b128 v[176:179], v246 offset:26624\n"
    "buffer_load_dwordx4 v246, s[40:43], s12 offen offset:1024 sc1 lds\n"
    "v_exp_f32 v223, v223\n"
    "v_fmac_f32 v228, v240, v224\n"
    "v_fmac_f32 v229, v240, v225\n"
    "v_mfma_f32_32x32x16_bf16 v[160:175], v[180:183], v[84:87], v[160:175]\n"
    "ds_read_b128 v[180:183], v246 offset:27648\n"
    "v_fmac_f32 v230, v240, v226\n"
    "v_fmac_f32 v231, v240, v227\n"
    "v_add_f32 v220, 1.0, v220\n"
    "v_add_f32 v221, 1.0, v221\n"
    "v_add_f32 v222, 1.0, v222\n"
    "v_add_f32 v223, 1.0, v223\n"
    "s_waitcnt lgkmcnt(8)\n"
    "v_mfma_f32_32x32x16_bf16 v[160:175], v[184:187], v[88:91], v[160:175]\n"
    "ds_read_b128 v[184:187], v246 offset:28672\n"
    "buffer_load_dwordx4 v246, s[40:43], s12 offen offset:2048 sc1 lds\n"
    "v_rcp_f32 v220, v220\n"
    "v_rcp_f32 v221, v221\n"
    "v_mfma_f32_32x32x16_bf16 v[160:175], v[188:191], v[92:95], v[160:175]\n"
    "ds_read_b128 v[188:191], v246 offset:29696\n"
    "v_rcp_f32 v222, v222\n"
    "v_rcp_f32 v223, v223\n"
    "v_fmac_f32 v228, v238, v140\n"
    "v_fmac_f32 v229, v238, v141\n"
    "s_waitcnt lgkmcnt(4)\n"
    "v_mfma_f32_32x32x16_bf16 v[160:175], v[192:195], v[96:99], v[160:175]\n"
    "ds_read_b128 v[192:195], v246 offset:30720\n"
    "buffer_load_dwordx4 v246, s[40:43], s12 offen offset:3072 sc1 lds\n"
    "v_fmac_f32 v230, v238, v142\n"
    "v_fmac_f32 v231, v238, v143\n"
    "v_mul_f32 v228, v228, v220\n"
    "v_mul_f32 v229, v229, v221\n"
    "s_waitcnt vmcnt(11)\n"
    "s_barrier\n"
    "v_mfma_f32_32x32x16_bf16 v[160:175], v[196:199], v[100:103], v[160:175]\n"
    "ds_read_b128 v[196:199], v246 offset:31744\n"
    "v_mul_f32 v230, v230, v222\n"
    "v_mul_f32 v231, v231, v223\n"
    "v_cvt_pk_bf16_f32 v234, v228, v229\n"
    "v_cvt_pk_bf16_f32 v235, v230, v231\n"
    "buffer_store_dwordx4 v[232:235], v246, s[40:43], s82 offen offset:0 sc1\n"
    "s_waitcnt lgkmcnt(4)\n"
    "v_mfma_f32_32x32x16_bf16 v[160:175], v[176:179], v[104:107], v[160:175]\n"
    "ds_read_b128 v[176:179], v246 offset:32768\n"
    "buffer_load_dwordx4 a[12:15], v243, s[40:43], s79 offen offset:0 sc1\n"
    "v_fmac_f32 v204, v240, v200\n"
    "v_fmac_f32 v205, v240, v201\n"
    "v_fmac_f32 v206, v240, v202\n"
    "v_mfma_f32_32x32x16_bf16 v[160:175], v[180:183], v[108:111], v[160:175]\n"
    "ds_read_b128 v[180:183], v246 offset:33792\n"
    "v_fmac_f32 v207, v240, v203\n"
    "ds_read_b128 v[216:219], v249 offset:160\n"
    "ds_read_b128 v[220:223], v249 offset:1056\n"
    "ds_read_b128 v[224:227], v249 offset:224\n"
    "ds_read_b128 v[228:231], v249 offset:1120\n"
    "v_fmac_f32 v204, v239, v144\n"
    "s_waitcnt lgkmcnt(8)\n"
    "v_mfma_f32_32x32x16_bf16 v[160:175], v[184:187], v[112:115], v[160:175]\n"
    "ds_read_b128 v[184:187], v246 offset:34816\n"
    "buffer_load_dwordx4 a[16:19], v244, s[40:43], s78 offen offset:0 sc1\n"
    "v_fmac_f32 v205, v239, v145\n"
    "v_fmac_f32 v206, v239, v146\n"
    "v_fmac_f32 v207, v239, v147\n"
    "v_mfma_f32_32x32x16_bf16 v[160:175], v[188:191], v[116:119], v[160:175]\n"
    "ds_read_b128 v[188:191], v246 offset:35840\n"
    "v_exp_f32 v204, v204\n"
    "v_exp_f32 v205, v205\n"
    "v_exp_f32 v206, v206\n"
    "s_waitcnt lgkmcnt(8)\n"
    "v_mfma_f32_32x32x16_bf16 v[160:175], v[192:195], v[120:123], v[160:175]\n"
    "ds_read_b128 v[192:195], v246 offset:36864\n"
    "buffer_load_dwordx4 a[20:23], v244, s[40:43], s79 offen offset:0 sc1\n"
    "v_exp_f32 v207, v207\n"
    "v_fmac_f32 v212, v240, v208\n"
    "v_fmac_f32 v213, v240, v209\n"
    "v_mfma_f32_32x32x16_bf16 v[160:175], v[196:199], v[124:127], v[160:175]\n"
    "ds_read_b128 v[196:199], v246 offset:37888\n"
    "v_fmac_f32 v214, v240, v210\n"
    "v_fmac_f32 v215, v240, v211\n"
    "v_add_f32 v204, 1.0, v204\n"
    "v_add_f32 v205, 1.0, v205\n"
    "v_add_f32 v206, 1.0, v206\n"
    "v_add_f32 v207, 1.0, v207\n"
    "s_waitcnt lgkmcnt(8)\n"
    "v_mfma_f32_32x32x16_bf16 v[128:143], v[176:179], v[0:3], 0\n"
    "ds_read_b128 v[176:179], v246 offset:38912\n"
    "s_add_u32 s12, s9, 163840\n"
    "s_add_u32 m0, s10, 65536\n"
    "s_nop 0\n"
    "buffer_load_dwordx4 v246, s[40:43], s12 offen offset:0 sc1 lds\n"
    "v_rcp_f32 v204, v204\n"
    "v_rcp_f32 v205, v205\n"
    "v_mfma_f32_32x32x16_bf16 v[128:143], v[180:183], v[4:7], v[128:143]\n"
    "ds_read_b128 v[180:183], v246 offset:39936\n"
    "buffer_load_dwordx4 a[24:27], v245, s[40:43], s78 offen offset:0 sc1\n"
    "v_rcp_f32 v206, v206\n"
    "v_rcp_f32 v207, v207\n"
    "v_fmac_f32 v212, v238, v152\n"
    "s_waitcnt lgkmcnt(4)\n"
    "v_mfma_f32_32x32x16_bf16 v[128:143], v[184:187], v[8:11], v[128:143]\n"
    "ds_read_b128 v[184:187], v246 offset:40960\n"
    "buffer_load_dwordx4 v246, s[40:43], s12 offen offset:1024 sc1 lds\n"
    "v_fmac_f32 v213, v238, v153\n"
    "v_fmac_f32 v214, v238, v154\n"
    "v_fmac_f32 v215, v238, v155\n"
    "v_mul_f32 v212, v212, v204\n"
    "v_mfma_f32_32x32x16_bf16 v[128:143], v[188:191], v[12:15], v[128:143]\n"
    "ds_read_b128 v[188:191], v246 offset:41984\n"
    "buffer_load_dwordx4 a[28:31], v245, s[40:43], s79 offen offset:0 sc1\n"
    "v_mul_f32 v213, v213, v205\n"
    "v_mul_f32 v214, v214, v206\n"
    "v_mul_f32 v215, v215, v207\n"
    "v_cvt_pk_bf16_f32 v232, v212, v213\n"
    "v_cvt_pk_bf16_f32 v233, v214, v215\n"
    "s_waitcnt lgkmcnt(4)\n"
    "v_mfma_f32_32x32x16_bf16 v[128:143], v[192:195], v[16:19], v[128:143]\n"
    "ds_read_b128 v[192:195], v246 offset:43008\n"
    "buffer_load_dwordx4 v246, s[40:43], s12 offen offset:2048 sc1 lds\n"
    "v_fmac_f32 v220, v240, v216\n"
    "v_fmac_f32 v221, v240, v217\n"
    "v_fmac_f32 v222, v240, v218\n"
    "v_mfma_f32_32x32x16_bf16 v[128:143], v[196:199], v[20:23], v[128:143]\n"
    "ds_read_b128 v[196:199], v246 offset:44032\n"
    "buffer_load_dwordx4 a[32:35], v246, s[40:43], s78 offen offset:1024 sc1\n"
    "v_fmac_f32 v223, v240, v219\n"
    "v_fmac_f32 v220, v239, v148\n"
    "v_fmac_f32 v221, v239, v149\n"
    "v_fmac_f32 v222, v239, v150\n"
    "v_fmac_f32 v223, v239, v151\n"
    "s_waitcnt lgkmcnt(4)\n"
    "v_mfma_f32_32x32x16_bf16 v[128:143], v[176:179], v[24:27], v[128:143]\n"
    "ds_read_b128 v[176:179], v246 offset:45056\n"
    "buffer_load_dwordx4 v246, s[40:43], s12 offen offset:3072 sc1 lds\n"
    "ds_read_b128 v[200:203], v249 offset:256\n"
    "ds_read_b128 v[204:207], v249 offset:1152\n"
    "ds_read_b128 v[208:211], v249 offset:320\n"
    "ds_read_b128 v[212:215], v249 offset:1216\n"
    "v_mfma_f32_32x32x16_bf16 v[128:143], v[180:183], v[28:31], v[128:143]\n"
    "ds_read_b128 v[180:183], v246 offset:46080\n"
    "buffer_load_dwordx4 a[36:39], v246, s[40:43], s79 offen offset:1024 sc1\n"
    "v_exp_f32 v220, v220\n"
    "v_exp_f32 v221, v221\n"
    "s_waitcnt lgkmcnt(8)\n"
    "v_mfma_f32_32x32x16_bf16 v[128:143], v[184:187], v[32:35], v[128:143]\n"
    "ds_read_b128 v[184:187], v246 offset:47104\n"
    "s_add_u32 s12, s9, 167936\n"
    "s_add_u32 m0, s10, 69632\n"
    "s_nop 0\n"
    "buffer_load_dwordx4 v246, s[40:43], s12 offen offset:0 sc1 lds\n"
    "v_exp_f32 v222, v222\n"
    "v_exp_f32 v223, v223\n"
    "v_mfma_f32_32x32x16_bf16 v[128:143], v[188:191], v[36:39], v[128:143]\n"
    "ds_read_b128 v[188:191], v246 offset:48128\n"
    "buffer_load_dwordx4 a[40:43], v243, s[40:43], s78 offen offset:1024 sc1\n"
    "v_fmac_f32 v228, v240, v224\n"
    "v_fmac_f32 v229, v240, v225\n"
    "v_fmac_f32 v230, v240, v226\n"
    "v_fmac_f32 v231, v240, v227\n"
    "v_add_f32 v220, 1.0, v220\n"
    "s_waitcnt lgkmcnt(8)\n"
    "v_mfma_f32_32x32x16_bf16 v[128:143], v[192:195], v[40:43], v[128:143]\n"
    "ds_read_b128 v[192:195], v246 offset:49152\n"
    "buffer_load_dwordx4 v246, s[40:43], s12 offen offset:1024 sc1 lds\n"
    "v_add_f32 v221, 1.0, v221\n"
    "v_add_f32 v222, 1.0, v222\n"
    "v_add_f32 v223, 1.0, v223\n"
    "v_mfma_f32_32x32x16_bf16 v[128:143], v[196:199], v[44:47], v[128:143]\n"
    "ds_read_b128 v[196:199], v246 offset:50176\n"
    "buffer_load_dwordx4 a[44:47], v243, s[40:43], s79 offen offset:1024 sc1\n"
    "v_rcp_f32 v220, v220\n"
    "v_rcp_f32 v221, v221\n"
    "s_waitcnt lgkmcnt(4)\n"
    "v_mfma_f32_32x32x16_bf16 v[128:143], v[176:179], v[48:51], v[128:143]\n"
    "ds_read_b128 v[176:179], v246 offset:51200\n"
    "buffer_load_dwordx4 v246, s[40:43], s12 offen offset:2048 sc1 lds\n"
    "v_rcp_f32 v222, v222\n"
    "v_rcp_f32 v223, v223\n"
    "v_mfma_f32_32x32x16_bf16 v[128:143], v[180:183], v[52:55], v[128:143]\n"
    "ds_read_b128 v[180:183], v246 offset:52224\n"
    "buffer_load_dwordx4 a[48:51], v244, s[40:43], s78 offen offset:1024 sc1\n"
    "v_fmac_f32 v228, v238, v156\n"
    "v_fmac_f32 v229, v238, v157\n"
    "v_fmac_f32 v230, v238, v158\n"
    "v_fmac_f32 v231, v238, v159\n"
    "v_mul_f32 v228, v228, v220\n"
    "s_waitcnt lgkmcnt(4)\n"
    "v_mfma_f32_32x32x16_bf16 v[128:143], v[184:187], v[56:59], v[128:143]\n"
    "ds_read_b128 v[184:187], v246 offset:53248\n"
    "buffer_load_dwordx4 v246, s[40:43], s12 offen offset:3072 sc1 lds\n"
    "v_mul_f32 v229, v229, v221\n"
    "v_mul_f32 v230, v230, v222\n"
    "v_mul_f32 v231, v231, v223\n"
    "v_cvt_pk_bf16_f32 v234, v228, v229\n"
    "v_mfma_f32_32x32x16_bf16 v[128:143], v[188:191], v[60:63], v[128:143]\n"
    "ds_read_b128 v[188:191], v246 offset:54272\n"
    "buffer_load_dwordx4 a[52:55], v244, s[40:43], s79 offen offset:1024 sc1\n"
    "v_cvt_pk_bf16_f32 v235, v230, v231\n"
    "buffer_store_dwordx4 v[232:235], v246, s[40:43], s82 offen offset:1024 sc1\n"
    "v_fmac_f32 v204, v240, v200\n"
    "v_fmac_f32 v205, v240, v201\n"
    "s_waitcnt lgkmcnt(4)\n"
    "v_mfma_f32_32x32x16_bf16 v[128:143], v[192:195], v[64:67], v[128:143]\n"
    "ds_read_b128 v[192:195], v246 offset:55296\n"
    "buffer_load_dwordx4 a[56:59], v245, s[40:43], s78 offen offset:1024 sc1\n"
    "v_fmac_f32 v206, v240, v202\n"
    "v_fmac_f32 v207, v240, v203\n"
    "ds_read_b128 v[216:219], v249 offset:288\n"
    "ds_read_b128 v[220:223], v249 offset:1184\n"
    "v_mfma_f32_32x32x16_bf16 v[128:143], v[196:199], v[68:71], v[128:143]\n"
    "ds_read_b128 v[196:199], v246 offset:56320\n"
    "ds_read_b128 v[224:227], v249 offset:352\n"
    "ds_read_b128 v[228:231], v249 offset:1248\n"
    "v_fmac_f32 v204, v239, v160\n"
    "v_fmac_f32 v205, v239, v161\n"
    "v_fmac_f32 v206, v239, v162\n"
    "v_fmac_f32 v207, v239, v163\n"
    "s_waitcnt lgkmcnt(8)\n"
    "v_mfma_f32_32x32x16_bf16 v[128:143], v[176:179], v[72:75], v[128:143]\n"
    "ds_read_b128 v[176:179], v246 offset:57344\n"
    "buffer_load_dwordx4 a[60:63], v245, s[40:43], s79 offen offset:1024 sc1\n"
    "v_exp_f32 v204, v204\n"
    "v_exp_f32 v205, v205\n"
    "v_mfma_f32_32x32x16_bf16 v[128:143], v[180:183], v[76:79], v[128:143]\n"
    "ds_read_b128 v[180:183], v246 offset:58368\n"
    "v_exp_f32 v206, v206\n"
    "v_exp_f32 v207, v207\n"
    "v_fmac_f32 v212, v240, v208\n"
    "v_fmac_f32 v213, v240, v209\n"
    "s_waitcnt lgkmcnt(8)\n"
    "v_mfma_f32_32x32x16_bf16 v[128:143], v[184:187], v[80:83], v[128:143]\n"
    "ds_read_b128 v[184:187], v246 offset:59392\n"
    "buffer_load_dwordx4 a[64:67], v246, s[40:43], s78 offen offset:2048 sc1\n"
    "v_fmac_f32 v214, v240, v210\n"
    "v_fmac_f32 v215, v240, v211\n"
    "v_add_f32 v204, 1.0, v204\n"
    "v_add_f32 v205, 1.0, v205\n"
    "v_mfma_f32_32x32x16_bf16 v[128:143], v[188:191], v[84:87], v[128:143]\n"
    "ds_read_b128 v[188:191], v246 offset:60416\n"
    "v_add_f32 v206, 1.0, v206\n"
    "v_add_f32 v207, 1.0, v207\n"
    "v_rcp_f32 v204, v204\n"
    "v_rcp_f32 v205, v205\n"
    "s_waitcnt lgkmcnt(6)\n"
    "v_mfma_f32_32x32x16_bf16 v[128:143], v[192:195], v[88:91], v[128:143]\n"
    "ds_read_b128 v[192:195], v246 offset:61440\n"
    "buffer_load_dwordx4 a[68:71], v246, s[40:43], s79 offen offset:2048 sc1\n"
    "v_rcp_f32 v206, v206\n"
    "v_rcp_f32 v207, v207\n"
    "v_mfma_f32_32x32x16_bf16 v[128:143], v[196:199], v[92:95], v[128:143]\n"
    "ds_read_b128 v[196:199], v246 offset:62464\n"
    "v_fmac_f32 v212, v238, v168\n"
    "v_fmac_f32 v213, v238, v169\n"
    "v_fmac_f32 v214, v238, v170\n"
    "v_fmac_f32 v215, v238, v171\n"
    "v_mul_f32 v212, v212, v204\n"
    "v_mul_f32 v213, v213, v205\n"
    "s_waitcnt lgkmcnt(4)\n"
    "v_mfma_f32_32x32x16_bf16 v[128:143], v[176:179], v[96:99], v[128:143]\n"
    "ds_read_b128 v[176:179], v246 offset:63488\n"
    "buffer_load_dwordx4 a[72:75], v243, s[40:43], s78 offen offset:2048 sc1\n"
    "v_mul_f32 v214, v214, v206\n"
    "v_mul_f32 v215, v215, v207\n"
    "v_cvt_pk_bf16_f32 v232, v212, v213\n"
    "v_cvt_pk_bf16_f32 v233, v214, v215\n"
    "s_waitcnt vmcnt(26)\n"
    "s_barrier\n"
    "v_mfma_f32_32x32x16_bf16 v[128:143], v[180:183], v[100:103], v[128:143]\n"
    "ds_read_b128 v[180:183], v246 offset:64512\n"
    "v_fmac_f32 v220, v240, v216\n"
    "v_fmac_f32 v221, v240, v217\n"
    "v_fmac_f32 v222, v240, v218\n"
    "v_fmac_f32 v223, v240, v219\n"
    "v_fmac_f32 v220, v239, v164\n"
    "s_waitcnt lgkmcnt(4)\n"
    "v_mfma_f32_32x32x16_bf16 v[128:143], v[184:187], v[104:107], v[128:143]\n"
    "ds_read_b128 v[184:187], v242 offset:32768\n"
    "buffer_load_dwordx4 a[76:79], v243, s[40:43], s79 offen offset:2048 sc1\n"
    "v_fmac_f32 v221, v239, v165\n"
    "v_fmac_f32 v222, v239, v166\n"
    "v_fmac_f32 v223, v239, v167\n"
    "ds_read_b128 v[200:203], v249 offset:384\n"
    "v_mfma_f32_32x32x16_bf16 v[128:143], v[188:191], v[108:111], v[128:143]\n"
    "ds_read_b128 v[188:191], v242 offset:33792\n"
    "ds_read_b128 v[204:207], v249 offset:1280\n"
    "ds_read_b128 v[208:211], v249 offset:448\n"
    "ds_read_b128 v[212:215], v249 offset:1344\n"
    "v_exp_f32 v220, v220\n"
    "s_waitcnt lgkmcnt(8)\n"
    "v_mfma_f32_32x32x16_bf16 v[128:143], v[192:195], v[112:115], v[128:143]\n"
    "ds_read_b128 v[192:195], v242 offset:34816\n"
    "buffer_load_dwordx4 a[80:83], v244, s[40:43], s78 offen offset:2048 sc1\n"
    "v_exp_f32 v221, v221\n"
    "v_exp_f32 v222, v222\n"
    "v_mfma_f32_32x32x16_bf16 v[128:143], v[196:199], v[116:119], v[128:143]\n"
    "ds_read_b128 v[196:199], v242 offset:35840\n"
    "v_exp_f32 v223, v223\n"
    "v_fmac_f32 v228, v240, v224\n"
    "v_fmac_f32 v229, v240, v225\n"
    "v_fmac_f32 v230, v240, v226\n"
    "v_fmac_f32 v231, v240, v227\n"
    "s_waitcnt lgkmcnt(8)\n"
    "v_mfma_f32_32x32x16_bf16 v[128:143], v[176:179], v[120:123], v[128:143]\n"
    "ds_read_b128 v[176:179], v242 offset:36864\n"
    "buffer_load_dwordx4 a[84:87], v244, s[40:43], s79 offen offset:2048 sc1\n"
    "v_add_f32 v220, 1.0, v220\n"
    "v_add_f32 v221, 1.0, v221\n"
    "v_add_f32 v222, 1.0, v222\n"
    "v_add_f32 v223, 1.0, v223\n"
    "v_mfma_f32_32x32x16_bf16 v[128:143], v[180:183], v[124:127], v[128:143]\n"
    "ds_read_b128 v[180:183], v242 offset:37888\n"
    "v_rcp_f32 v220, v220\n"
    "v_rcp_f32 v221, v221\n"
    "v_rcp_f32 v222, v222\n"
    "s_waitcnt lgkmcnt(7)\n"
    "v_mfma_f32_32x32x16_bf16 v[144:159], v[184:187], v[0:3], 0\n"
    "ds_read_b128 v[184:187], v242 offset:38912\n"
    "s_add_u32 s12, s9, 196608\n"
    "s_add_u32 m0, s10, 0\n"
    "s_nop 0\n"
    "buffer_load_dwordx4 v246, s[40:43], s12 offen offset:0 sc1 lds\n"
    "v_rcp_f32 v223, v223\n"
    "v_fmac_f32 v228, v238, v172\n"
    "v_mfma_f32_32x32x16_bf16 v[144:159], v[188:191], v[4:7], v[144:159]\n"
    "ds_read_b128 v[188:191], v242 offset:39936\n"
    "v_fmac_f32 v229, v238, v173\n"
    "v_fmac_f32 v230, v238, v174\n"
    "v_fmac_f32 v231, v238, v175\n"
    "v_mul_f32 v228, v228, v220\n"
    "v_mul_f32 v229, v229, v221\n"
    "s_waitcnt lgkmcnt(4)\n"
    "v_mfma_f32_32x32x16_bf16 v[144:159], v[192:195], v[8:11], v[144:159]\n"
    "ds_read_b128 v[192:195], v242 offset:40960\n"
    "buffer_load_dwordx4 v246, s[40:43], s12 offen offset:1024 sc1 lds\n"
    "v_mul_f32 v230, v230, v222\n"
    "v_mul_f32 v231, v231, v223\n"
    "v_cvt_pk_bf16_f32 v234, v228, v229\n"
    "v_mfma_f32_32x32x16_bf16 v[144:159], v[196:199], v[12:15], v[144:159]\n"
    "ds_read_b128 v[196:199], v242 offset:41984\n"
    "v_cvt_pk_bf16_f32 v235, v230, v231\n"
    "buffer_store_dwordx4 v[232:235], v246, s[40:43], s82 offen offset:2048 sc1\n"
    "v_fmac_f32 v204, v240, v200\n"
    "v_fmac_f32 v205, v240, v201\n"
    "s_waitcnt lgkmcnt(4)\n"
    "v_mfma_f32_32x32x16_bf16 v[144:159], v[176:179], v[16:19], v[144:159]\n"
    "ds_read_b128 v[176:179], v242 offset:43008\n"
    "buffer_load_dwordx4 v246, s[40:43], s12 offen offset:2048 sc1 lds\n"
    "v_fmac_f32 v206, v240, v202\n"
    "v_fmac_f32 v207, v240, v203\n"
    "ds_read_b128 v[216:219], v249 offset:416\n"
    "v_mfma_f32_32x32x16_bf16 v[144:159], v[180:183], v[20:23], v[144:159]\n"
    "ds_read_b128 v[180:183], v242 offset:44032\n"
    "ds_read_b128 v[220:223], v249 offset:1312\n"
    "ds_read_b128 v[224:227], v249 offset:480\n"
    "ds_read_b128 v[228:231], v249 offset:1376\n"
    "v_fmac_f32 v204, v239, v128\n"
    "v_fmac_f32 v205, v239, v129\n"
    "s_waitcnt lgkmcnt(8)\n"
    "v_mfma_f32_32x32x16_bf16 v[144:159], v[184:187], v[24:27], v[144:159]\n"
    "ds_read_b128 v[184:187], v242 offset:45056\n"
    "buffer_load_dwordx4 v246, s[40:43], s12 offen offset:3072 sc1 lds\n"
    "v_fmac_f32 v206, v239, v130\n"
    "v_fmac_f32 v207, v239, v131\n"
    "v_mfma_f32_32x32x16_bf16 v[144:159], v[188:191], v[28:31], v[144:159]\n"
    "ds_read_b128 v[188:191], v242 offset:46080\n"
    "v_exp_f32 v204, v204\n"
    "v_exp_f32 v205, v205\n"
    "s_waitcnt lgkmcnt(8)\n"
    "v_mfma_f32_32x32x16_bf16 v[144:159], v[192:195], v[32:35], v[144:159]\n"
    "ds_read_b128 v[192:195], v242 offset:47104\n"
    "s_add_u32 s12, s9, 200704\n"
    "s_add_u32 m0, s10, 4096\n"
    "s_nop 0\n"
    "buffer_load_dwordx4 v246, s[40:43], s12 offen offset:0 sc1 lds\n"
    "v_exp_f32 v206, v206\n"
    "v_mfma_f32_32x32x16_bf16 v[144:159], v[196:199], v[36:39], v[144:159]\n"
    "ds_read_b128 v[196:199], v242 offset:48128\n"
    "v_exp_f32 v207, v207\n"
    "v_fmac_f32 v212, v240, v208\n"
    "v_fmac_f32 v213, v240, v209\n"
    "v_fmac_f32 v214, v240, v210\n"
    "s_waitcnt lgkmcnt(7)\n"
    "v_mfma_f32_32x32x16_bf16 v[144:159], v[176:179], v[40:43], v[144:159]\n"
    "ds_read_b128 v[176:179], v242 offset:49152\n"
    "buffer_load_dwordx4 v246, s[40:43], s12 offen offset:1024 sc1 lds\n"
    "v_fmac_f32 v215, v240, v211\n"
    "v_add_f32 v204, 1.0, v204\n"
    "v_add_f32 v205, 1.0, v205\n"
    "v_mfma_f32_32x32x16_bf16 v[144:159], v[180:183], v[44:47], v[144:159]\n"
    "ds_read_b128 v[180:183], v242 offset:50176\n"
    "v_add_f32 v206, 1.0, v206\n"
    "v_add_f32 v207, 1.0, v207\n"
    "v_rcp_f32 v204, v204\n"
    "s_waitcnt lgkmcnt(4)\n"
    "v_mfma_f32_32x32x16_bf16 v[144:159], v[184:187], v[48:51], v[144:159]\n"
    "ds_read_b128 v[184:187], v242 offset:51200\n"
    "buffer_load_dwordx4 v246, s[40:43], s12 offen offset:2048 sc1 lds\n"
    "v_rcp_f32 v205, v205\n"
    "v_mfma_f32_32x32x16_bf16 v[144:159], v[188:191], v[52:55], v[144:159]\n"
    "ds_read_b128 v[188:191], v242 offset:52224\n"
    "v_rcp_f32 v206, v206\n"
    "v_rcp_f32 v207, v207\n"
    "v_fmac_f32 v212, v238, v136\n"
    "s_waitcnt lgkmcnt(4)\n"
    "v_mfma_f32_32x32x16_bf16 v[144:159], v[192:195], v[56:59], v[144:159]\n"
    "ds_read_b128 v[192:195], v242 offset:53248\n"
    "buffer_load_dwordx4 v246, s[40:43], s12 offen offset:3072 sc1 lds\n"
    "v_fmac_f32 v213, v238, v137\n"
    "v_fmac_f32 v214, v238, v138\n"
    "v_fmac_f32 v215, v238, v139\n"
    "v_mfma_f32_32x32x16_bf16 v[144:159], v[196:199], v[60:63], v[144:159]\n"
    "ds_read_b128 v[196:199], v242 offset:54272\n"
    "v_mul_f32 v212, v212, v204\n"
    "v_mul_f32 v213, v213, v205\n"
    "v_mul_f32 v214, v214, v206\n"
    "v_mul_f32 v215, v215, v207\n"
    "v_cvt_pk_bf16_f32 v232, v212, v213\n"
    "s_waitcnt lgkmcnt(4)\n"
    "v_mfma_f32_32x32x16_bf16 v[144:159], v[176:179], v[64:67], v[144:159]\n"
    "ds_read_b128 v[176:179], v242 offset:55296\n"
    "buffer_load_dwordx4 a[88:91], v245, s[40:43], s78 offen offset:2048 sc1\n"
    "v_cvt_pk_bf16_f32 v233, v214, v215\n"
    "v_fmac_f32 v220, v240, v216\n"
    "v_mfma_f32_32x32x16_bf16 v[144:159], v[180:183], v[68:71], v[144:159]\n"
    "ds_read_b128 v[180:183], v242 offset:56320\n"
    "v_fmac_f32 v221, v240, v217\n"
    "v_fmac_f32 v222, v240, v218\n"
    "v_fmac_f32 v223, v240, v219\n"
    "v_fmac_f32 v220, v239, v132\n"
    "v_fmac_f32 v221, v239, v133\n"
    "s_waitcnt lgkmcnt(4)\n"
    "v_mfma_f32_32x32x16_bf16 v[144:159], v[184:187], v[72:75], v[144:159]\n"
    "ds_read_b128 v[184:187], v242 offset:57344\n"
    "buffer_load_dwordx4 a[92:95], v245, s[40:43], s79 offen offset:2048 sc1\n"
    "v_fmac_f32 v222, v239, v134\n"
    "v_fmac_f32 v223, v239, v135\n"
    "ds_read_b128 v[200:203], v249 offset:512\n"
    "v_mfma_f32_32x32x16_bf16 v[144:159], v[188:191], v[76:79], v[144:159]\n"
    "ds_read_b128 v[188:191], v242 offset:58368\n"
    "ds_read_b128 v[204:207], v249 offset:1408\n"
    "ds_read_b128 v[208:211], v249 offset:576\n"
    "ds_read_b128 v[212:215], v249 offset:1472\n"
    "v_exp_f32 v220, v220\n"
    "s_waitcnt lgkmcnt(8)\n"
    "v_mfma_f32_32x32x16_bf16 v[144:159], v[192:195], v[80:83], v[144:159]\n"
    "ds_read_b128 v[192:195], v242 offset:59392\n"
    "buffer_load_dwordx4 a[96:99], v246, s[40:43], s78 offen offset:3072 sc1\n"
    "v_exp_f32 v221, v221\n"
    "v_mfma_f32_32x32x16_bf16 v[144:159], v[196:199], v[84:87], v[144:159]\n"
    "ds_read_b128 v[196:199], v242 offset:60416\n"
    "v_exp_f32 v222, v222\n"
    "v_exp_f32 v223, v223\n"
    "v_fmac_f32 v228, v240, v224\n"
    "s_waitcnt lgkmcnt(8)\n"
    "v_mfma_f32_32x32x16_bf16 v[144:159], v[176:179], v[88:91], v[144:159]\n"
    "ds_read_b128 v[176:179], v242 offset:61440\n"
    "buffer_load_dwordx4 a[100:103], v246, s[40:43], s79 offen offset:3072 sc1\n"
    "v_fmac_f32 v229, v240, v225\n"
    "v_fmac_f32 v230, v240, v226\n"
    "v_fmac_f32 v231, v240, v227\n"
    "v_mfma_f32_32x32x16_bf16 v[144:159], v[180:183], v[92:95], v[144:159]\n"
    "ds_read_b128 v[180:183], v242 offset:62464\n"
    "v_add_f32 v220, 1.0, v220\n"
    "v_add_f32 v221, 1.0, v221\n"
    "v_add_f32 v222, 1.0, v222\n"
    "v_add_f32 v223, 1.0, v223\n"
    "s_waitcnt lgkmcnt(7)\n"
    "v_mfma_f32_32x32x16_bf16 v[144:159], v[184:187], v[96:99], v[144:159]\n"
    "ds_read_b128 v[184:187], v242 offset:63488\n"
    "buffer_load_dwordx4 a[104:107], v243, s[40:43], s78 offen offset:3072 sc1\n"
    "v_rcp_f32 v220, v220\n"
    "s_waitcnt vmcnt(24)\n"
    "s_barrier\n"
    "v_mfma_f32_32x32x16_bf16 v[144:159], v[188:191], v[100:103], v[144:159]\n"
    "ds_read_b128 v[188:191], v242 offset:64512\n"
    "v_rcp_f32 v221, v221\n"
    "v_rcp_f32 v222, v222\n"
    "s_waitcnt lgkmcnt(4)\n"
    "v_mfma_f32_32x32x16_bf16 v[144:159], v[192:195], v[104:107], v[144:159]\n"
    "ds_read_b128 v[192:195], v242 offset:0\n"
    "buffer_load_dwordx4 a[108:111], v243, s[40:43], s79 offen offset:3072 sc1\n"
    "v_rcp_f32 v223, v223\n"
    "v_fmac_f32 v228, v238, v140\n"
    "v_mfma_f32_32x32x16_bf16 v[144:159], v[196:199], v[108:111], v[144:159]\n"
    "ds_read_b128 v[196:199], v242 offset:1024\n"
    "v_fmac_f32 v229, v238, v141\n"
    "v_fmac_f32 v230, v238, v142\n"
    "v_fmac_f32 v231, v238, v143\n"
    "v_mul_f32 v228, v228, v220\n"
    "v_mul_f32 v229, v229, v221\n"
    "s_waitcnt lgkmcnt(4)\n"
    "v_mfma_f32_32x32x16_bf16 v[144:159], v[176:179], v[112:115], v[144:159]\n"
    "ds_read_b128 v[176:179], v242 offset:2048\n"
    "buffer_load_dwordx4 a[112:115], v244, s[40:43], s78 offen offset:3072 sc1\n"
    "v_mul_f32 v230, v230, v222\n"
    "v_mul_f32 v231, v231, v223\n"
    "v_cvt_pk_bf16_f32 v234, v228, v229\n"
    "v_mfma_f32_32x32x16_bf16 v[144:159], v[180:183], v[116:119], v[144:159]\n"
    "ds_read_b128 v[180:183], v242 offset:3072\n"
    "v_cvt_pk_bf16_f32 v235, v230, v231\n"
    "buffer_store_dwordx4 v[232:235], v246, s[40:43], s82 offen offset:3072 sc1\n"
    "s_waitcnt lgkmcnt(4)\n"
    "v_mfma_f32_32x32x16_bf16 v[144:159], v[184:187], v[120:123], v[144:159]\n"
    "ds_read_b128 v[184:187], v242 offset:4096\n"
    "buffer_load_dwordx4 a[116:119], v244, s[40:43], s79 offen offset:3072 sc1\n"
    "v_mfma_f32_32x32x16_bf16 v[144:159], v[188:191], v[124:127], v[144:159]\n"
    "ds_read_b128 v[188:191], v242 offset:5120\n"
    "s_waitcnt lgkmcnt(4)\n"
    "v_mfma_f32_32x32x16_bf16 v[160:175], v[192:195], v[0:3], 0\n"
    "ds_read_b128 v[192:195], v242 offset:6144\n"
    "buffer_load_dwordx4 a[120:123], v245, s[40:43], s78 offen offset:3072 sc1\n"
    "v_mfma_f32_32x32x16_bf16 v[160:175], v[196:199], v[4:7], v[160:175]\n"
    "ds_read_b128 v[196:199], v242 offset:7168\n"
    "s_waitcnt lgkmcnt(4)\n"
    "v_mfma_f32_32x32x16_bf16 v[160:175], v[176:179], v[8:11], v[160:175]\n"
    "ds_read_b128 v[176:179], v242 offset:8192\n"
    "buffer_load_dwordx4 a[124:127], v245, s[40:43], s79 offen offset:3072 sc1\n"
    "v_mfma_f32_32x32x16_bf16 v[160:175], v[180:183], v[12:15], v[160:175]\n"
    "ds_read_b128 v[180:183], v242 offset:9216\n"
    "v_fmac_f32 v204, v240, v200\n"
    "v_fmac_f32 v205, v240, v201\n"
    "v_fmac_f32 v206, v240, v202\n"
    "v_fmac_f32 v207, v240, v203\n"
    "s_waitcnt lgkmcnt(4)\n"
    "v_mfma_f32_32x32x16_bf16 v[160:175], v[184:187], v[16:19], v[160:175]\n"
    "ds_read_b128 v[184:187], v242 offset:10240\n"
    "buffer_load_dwordx4 a[128:131], v246, s[40:43], s80 offen offset:0 sc1\n"
    "ds_read_b128 v[216:219], v249 offset:544\n"
    "ds_read_b128 v[220:223], v249 offset:1440\n"
    "ds_read_b128 v[224:227], v249 offset:608\n"
    "v_mfma_f32_32x32x16_bf16 v[160:175], v[188:191], v[20:23], v[160:175]\n"
    "ds_read_b128 v[188:191], v242 offset:11264\n"
    "ds_read_b128 v[228:231], v249 offset:1504\n"
    "v_fmac_f32 v204, v239, v144\n"
    "v_fmac_f32 v205, v239, v145\n"
    "v_fmac_f32 v206, v239, v146\n"
    "v_fmac_f32 v207, v239, v147\n"
    "s_waitcnt lgkmcnt(8)\n"
    "v_mfma_f32_32x32x16_bf16 v[160:175], v[192:195], v[24:27], v[160:175]\n"
    "ds_read_b128 v[192:195], v242 offset:12288\n"
    "buffer_load_dwordx4 a[132:135], v246, s[40:43], s81 offen offset:0 sc1\n"
    "v_exp_f32 v204, v204\n"
    "v_mfma_f32_32x32x16_bf16 v[160:175], v[196:199], v[28:31], v[160:175]\n"
    "ds_read_b128 v[196:199], v242 offset:13312\n"
    "v_exp_f32 v205, v205\n"
    "v_exp_f32 v206, v206\n"
    "s_waitcnt lgkmcnt(8)\n"
    "v_mfma_f32_32x32x16_bf16 v[160:175], v[176:179], v[32:35], v[160:175]\n"
    "ds_read_b128 v[176:179], v242 offset:14336\n"
    "buffer_load_dwordx4 a[136:139], v243, s[40:43], s80 offen offset:0 sc1\n"
    "v_exp_f32 v207, v207\n"
    "v_fmac_f32 v212, v240, v208\n"
    "v_mfma_f32_32x32x16_bf16 v[160:175], v[180:183], v[36:39], v[160:175]\n"
    "ds_read_b128 v[180:183], v242 offset:15360\n"
    "v_fmac_f32 v213, v240, v209\n"
    "v_fmac_f32 v214, v240, v210\n"
    "v_fmac_f32 v215, v240, v211\n"
    "v_add_f32 v204, 1.0, v204\n"
    "v_add_f32 v205, 1.0, v205\n"
    "s_waitcnt lgkmcnt(5)\n"
    "v_mfma_f32_32x32x16_bf16 v[160:175], v[184:187], v[40:43], v[160:175]\n"
    "ds_read_b128 v[184:187], v242 offset:16384\n"
    "buffer_load_dwordx4 a[140:143], v243, s[40:43], s81 offen offset:0 sc1\n"
    "v_add_f32 v206, 1.0, v206\n"
    "v_add_f32 v207, 1.0, v207\n"
    "v_mfma_f32_32x32x16_bf16 v[160:175], v[188:191], v[44:47], v[160:175]\n"
    "ds_read_b128 v[188:191], v242 offset:17408\n"
    "v_rcp_f32 v204, v204\n"
    "v_rcp_f32 v205, v205\n"
    "s_waitcnt lgkmcnt(4)\n"
    "v_mfma_f32_32x32x16_bf16 v[160:175], v[192:195], v[48:51], v[160:175]\n"
    "ds_read_b128 v[192:195], v242 offset:18432\n"
    "buffer_load_dwordx4 a[144:147], v244, s[40:43], s80 offen offset:0 sc1\n"
    "v_rcp_f32 v206, v206\n"
    "s_waitcnt vmcnt(22)\n"
    "v_mov_b32 v248, 3\n"
    "s_mov_b64 exec, 1\n"
    "buffer_store_dword v248, off, s[40:43], s84 sc1\n"
    "s_mov_b64 exec, -1\n"
    "v_mfma_f32_32x32x16_bf16 v[160:175], v[196:199], v[52:55], v[160:175]\n"
    "ds_read_b128 v[196:199], v242 offset:19456\n"
    "v_rcp_f32 v207, v207\n"
    "v_fmac_f32 v212, v238, v152\n"
    "v_fmac_f32 v213, v238, v153\n"
    "v_fmac_f32 v214, v238, v154\n"
    "s_waitcnt lgkmcnt(4)\n"
    "v_mfma_f32_32x32x16_bf16 v[160:175], v[176:179], v[56:59], v[160:175]\n"
    "ds_read_b128 v[176:179], v242 offset:20480\n"
    "buffer_load_dwordx4 a[148:151], v244, s[40:43], s81 offen offset:0 sc1\n"
    "v_fmac_f32 v215, v238, v155\n"
    "v_mul_f32 v212, v212, v204\n"
    "v_mul_f32 v213, v213, v205\n"
    "v_mfma_f32_32x32x16_bf16 v[160:175], v[180:183], v[60:63], v[160:175]\n"
    "ds_read_b128 v[180:183], v242 offset:21504\n"
    "v_mul_f32 v214, v214, v206\n"
    "v_mul_f32 v215, v215, v207\n"
    "v_cvt_pk_bf16_f32 v232, v212, v213\n"
    "v_cvt_pk_bf16_f32 v233, v214, v215\n"
    "s_waitcnt lgkmcnt(4)\n"
    "v_mfma_f32_32x32x16_bf16 v[160:175], v[184:187], v[64:67], v[160:175]\n"
    "ds_read_b128 v[184:187], v242 offset:22528\n"
    "buffer_load_dwordx4 a[152:155], v245, s[40:43], s80 offen offset:0 sc1\n"
    "v_fmac_f32 v220, v240, v216\n"
    "v_fmac_f32 v221, v240, v217\n"
    "v_mfma_f32_32x32x16_bf16 v[160:175], v[188:191], v[68:71], v[160:175]\n"
    "ds_read_b128 v[188:191], v242 offset:23552\n"
    "v_fmac_f32 v222, v240, v218\n"
    "v_fmac_f32 v223, v240, v219\n"
    "v_fmac_f32 v220, v239, v148\n"
    "v_fmac_f32 v221, v239, v149\n"
    "v_fmac_f32 v222, v239, v150\n"
    "s_waitcnt lgkmcnt(4)\n"
    "v_mfma_f32_32x32x16_bf16 v[160:175], v[192:195], v[72:75], v[160:175]\n"
    "ds_read_b128 v[192:195], v242 offset:24576\n"
    "buffer_load_dwordx4 a[156:159], v245, s[40:43], s81 offen offset:0 sc1\n"
    "v_fmac_f32 v223, v239, v151\n"
    "ds_read_b128 v[200:203], v249 offset:640\n"
    "ds_read_b128 v[204:207], v249 offset:1536\n"
    "v_mfma_f32_32x32x16_bf16 v[160:175], v[196:199], v[76:79], v[160:175]\n"
    "ds_read_b128 v[196:199], v242 offset:25600\n"
    "ds_read_b128 v[208:211], v249 offset:704\n"
    "ds_read_b128 v[212:215], v249 offset:1600\n"
    "v_exp_f32 v220, v220\n"
    "s_waitcnt lgkmcnt(8)\n"
    "v_mfma_f32_32x32x16_bf16 v[160:175], v[176:179], v[80:83], v[160:175]\n"
    "ds_read_b128 v[176:179], v242 offset:26624\n"
    "buffer_load_dwordx4 a[160:163], v246, s[40:43], s80 offen offset:1024 sc1\n"
    "v_exp_f32 v221, v221\n"
    "v_mfma_f32_32x32x16_bf16 v[160:175], v[180:183], v[84:87], v[160:175]\n"
    "ds_read_b128 v[180:183], v242 offset:27648\n"
    "v_exp_f32 v222, v222\n"
    "v_exp_f32 v223, v223\n"
    "v_fmac_f32 v228, v240, v224\n"
    "s_waitcnt lgkmcnt(8)\n"
    "v_mfma_f32_32x32x16_bf16 v[160:175], v[184:187], v[88:91], v[160:175]\n"
    "ds_read_b128 v[184:187], v242 offset:28672\n"
    "buffer_load_dwordx4 a[164:167], v246, s[40:43], s81 offen offset:1024 sc1\n"
    "v_fmac_f32 v229, v240, v225\n"
    "v_fmac_f32 v230, v240, v226\n"
    "v_fmac_f32 v231, v240, v227\n"
    "v_mfma_f32_32x32x16_bf16 v[160:175], v[188:191], v[92:95], v[160:175]\n"
    "ds_read_b128 v[188:191], v242 offset:29696\n"
    "v_add_f32 v220, 1.0, v220\n"
    "v_add_f32 v221, 1.0, v221\n"
    "v_add_f32 v222, 1.0, v222\n"
    "v_add_f32 v223, 1.0, v223\n"
    "s_waitcnt lgkmcnt(6)\n"
    "v_mfma_f32_32x32x16_bf16 v[160:175], v[192:195], v[96:99], v[160:175]\n"
    "ds_read_b128 v[192:195], v242 offset:30720\n"
    "buffer_load_dwordx4 a[168:171], v243, s[40:43], s80 offen offset:1024 sc1\n"
    "v_rcp_f32 v220, v220\n"
    "s_waitcnt vmcnt(23)\n"
    "s_barrier\n"
    "v_mfma_f32_32x32x16_bf16 v[160:175], v[196:199], v[100:103], v[160:175]\n"
    "ds_read_b128 v[196:199], v242 offset:31744\n"
    "v_rcp_f32 v221, v221\n"
    "v_rcp_f32 v222, v222\n"
    "s_waitcnt lgkmcnt(4)\n"
    "v_mfma_f32_32x32x16_bf16 v[160:175], v[176:179], v[104:107], v[160:175]\n"
    "ds_read_b128 v[176:179], v246 offset:0\n"
    "buffer_load_dwordx4 a[172:175], v243, s[40:43], s81 offen offset:1024 sc1\n"
    "v_rcp_f32 v223, v223\n"
    "v_fmac_f32 v228, v238, v156\n"
    "v_mfma_f32_32x32x16_bf16 v[160:175], v[180:183], v[108:111], v[160:175]\n"
    "ds_read_b128 v[180:183], v246 offset:1024\n"
    "v_fmac_f32 v229, v238, v157\n"
    "v_fmac_f32 v230, v238, v158\n"
    "v_fmac_f32 v231, v238, v159\n"
    "v_mul_f32 v228, v228, v220\n"
    "v_mul_f32 v229, v229, v221\n"
    "s_waitcnt lgkmcnt(4)\n"
    "v_mfma_f32_32x32x16_bf16 v[160:175], v[184:187], v[112:115], v[160:175]\n"
    "ds_read_b128 v[184:187], v246 offset:2048\n"
    "buffer_load_dwordx4 a[176:179], v244, s[40:43], s80 offen offset:1024 sc1\n"
    "v_mul_f32 v230, v230, v222\n"
    "v_mul_f32 v231, v231, v223\n"
    "v_cvt_pk_bf16_f32 v234, v228, v229\n"
    "v_mfma_f32_32x32x16_bf16 v[160:175], v[188:191], v[116:119], v[160:175]\n"
    "ds_read_b128 v[188:191], v246 offset:3072\n"
    "v_cvt_pk_bf16_f32 v235, v230, v231\n"
    "buffer_store_dwordx4 v[232:235], v246, s[40:43], s83 offen offset:0 sc1\n"
    "s_waitcnt lgkmcnt(4)\n"
    "v_mfma_f32_32x32x16_bf16 v[160:175], v[192:195], v[120:123], v[160:175]\n"
    "ds_read_b128 v[192:195], v246 offset:4096\n"
    "buffer_load_dwordx4 a[180:183], v244, s[40:43], s81 offen offset:1024 sc1\n"
    "v_mfma_f32_32x32x16_bf16 v[160:175], v[196:199], v[124:127], v[160:175]\n"
    "ds_read_b128 v[196:199], v246 offset:5120\n"
    "s_waitcnt lgkmcnt(4)\n"
    "v_mfma_f32_32x32x16_bf16 v[128:143], v[176:179], v[0:3], 0\n"
    "ds_read_b128 v[176:179], v246 offset:6144\n"
    "buffer_load_dwordx4 a[184:187], v245, s[40:43], s80 offen offset:1024 sc1\n"
    "v_mfma_f32_32x32x16_bf16 v[128:143], v[180:183], v[4:7], v[128:143]\n"
    "ds_read_b128 v[180:183], v246 offset:7168\n"
    "s_waitcnt lgkmcnt(4)\n"
    "v_mfma_f32_32x32x16_bf16 v[128:143], v[184:187], v[8:11], v[128:143]\n"
    "ds_read_b128 v[184:187], v246 offset:8192\n"
    "buffer_load_dwordx4 a[188:191], v245, s[40:43], s81 offen offset:1024 sc1\n"
    "v_mfma_f32_32x32x16_bf16 v[128:143], v[188:191], v[12:15], v[128:143]\n"
    "ds_read_b128 v[188:191], v246 offset:9216\n"
    "v_fmac_f32 v204, v240, v200\n"
    "v_fmac_f32 v205, v240, v201\n"
    "v_fmac_f32 v206, v240, v202\n"
    "v_fmac_f32 v207, v240, v203\n"
    "s_waitcnt lgkmcnt(4)\n"
    "v_mfma_f32_32x32x16_bf16 v[128:143], v[192:195], v[16:19], v[128:143]\n"
    "ds_read_b128 v[192:195], v246 offset:10240\n"
    "buffer_load_dwordx4 a[192:195], v246, s[40:43], s80 offen offset:2048 sc1\n"
    "ds_read_b128 v[216:219], v249 offset:672\n"
    "ds_read_b128 v[220:223], v249 offset:1568\n"
    "ds_read_b128 v[224:227], v249 offset:736\n"
    "v_mfma_f32_32x32x16_bf16 v[128:143], v[196:199], v[20:23], v[128:143]\n"
    "ds_read_b128 v[196:199], v246 offset:11264\n"
    "ds_read_b128 v[228:231], v249 offset:1632\n"
    "v_fmac_f32 v204, v239, v160\n"
    "v_fmac_f32 v205, v239, v161\n"
    "v_fmac_f32 v206, v239, v162\n"
    "v_fmac_f32 v207, v239, v163\n"
    "s_waitcnt lgkmcnt(8)\n"
    "v_mfma_f32_32x32x16_bf16 v[128:143], v[176:179], v[24:27], v[128:143]\n"
    "ds_read_b128 v[176:179], v246 offset:12288\n"
    "buffer_load_dwordx4 a[196:199], v246, s[40:43], s81 offen offset:2048 sc1\n"
    "v_exp_f32 v204, v204\n"
    "v_mfma_f32_32x32x16_bf16 v[128:143], v[180:183], v[28:31], v[128:143]\n"
    "ds_read_b128 v[180:183], v246 offset:13312\n"
    "v_exp_f32 v205, v205\n"
    "v_exp_f32 v206, v206\n"
    "s_waitcnt lgkmcnt(8)\n"
    "v_mfma_f32_32x32x16_bf16 v[128:143], v[184:187], v[32:35], v[128:143]\n"
    "ds_read_b128 v[184:187], v246 offset:14336\n"
    "buffer_load_dwordx4 a[200:203], v243, s[40:43], s80 offen offset:2048 sc1\n"
    "v_exp_f32 v207, v207\n"
    "v_fmac_f32 v212, v240, v208\n"
    "buffer_load_dword v241, v250, s[40:43], 0 offen sc1\n"
    "v_mfma_f32_32x32x16_bf16 v[128:143], v[188:191], v[36:39], v[128:143]\n"
    "ds_read_b128 v[188:191], v246 offset:15360\n"
    "v_fmac_f32 v213, v240, v209\n"
    "v_fmac_f32 v214, v240, v210\n"
    "v_fmac_f32 v215, v240, v211\n"
    "v_add_f32 v204, 1.0, v204\n"
    "v_add_f32 v205, 1.0, v205\n"
    "s_waitcnt lgkmcnt(5)\n"
    "v_mfma_f32_32x32x16_bf16 v[128:143], v[192:195], v[40:43], v[128:143]\n"
    "ds_read_b128 v[192:195], v246 offset:16384\n"
    "buffer_load_dwordx4 a[204:207], v243, s[40:43], s81 offen offset:2048 sc1\n"
    "v_add_f32 v206, 1.0, v206\n"
    "v_add_f32 v207, 1.0, v207\n"
    "v_mfma_f32_32x32x16_bf16 v[128:143], v[196:199], v[44:47], v[128:143]\n"
    "ds_read_b128 v[196:199], v246 offset:17408\n"
    "v_rcp_f32 v204, v204\n"
    "v_rcp_f32 v205, v205\n"
    "s_waitcnt lgkmcnt(4)\n"
    "v_mfma_f32_32x32x16_bf16 v[128:143], v[176:179], v[48:51], v[128:143]\n"
    "ds_read_b128 v[176:179], v246 offset:18432\n"
    "buffer_load_dwordx4 a[208:211], v244, s[40:43], s80 offen offset:2048 sc1\n"
    "v_rcp_f32 v206, v206\n"
    "v_mfma_f32_32x32x16_bf16 v[128:143], v[180:183], v[52:55], v[128:143]\n"
    "ds_read_b128 v[180:183], v246 offset:19456\n"
    "v_rcp_f32 v207, v207\n"
    "v_fmac_f32 v212, v238, v168\n"
    "v_fmac_f32 v213, v238, v169\n"
    "v_fmac_f32 v214, v238, v170\n"
    "s_waitcnt lgkmcnt(4)\n"
    "v_mfma_f32_32x32x16_bf16 v[128:143], v[184:187], v[56:59], v[128:143]\n"
    "ds_read_b128 v[184:187], v246 offset:20480\n"
    "buffer_load_dwordx4 a[212:215], v244, s[40:43], s81 offen offset:2048 sc1\n"
    "v_fmac_f32 v215, v238, v171\n"
    "v_mul_f32 v212, v212, v204\n"
    "v_mul_f32 v213, v213, v205\n"
    "v_mfma_f32_32x32x16_bf16 v[128:143], v[188:191], v[60:63], v[128:143]\n"
    "ds_read_b128 v[188:191], v246 offset:21504\n"
    "v_mul_f32 v214, v214, v206\n"
    "v_mul_f32 v215, v215, v207\n"
    "v_cvt_pk_bf16_f32 v232, v212, v213\n"
    "v_cvt_pk_bf16_f32 v233, v214, v215\n"
    "s_waitcnt lgkmcnt(4)\n"
    "v_mfma_f32_32x32x16_bf16 v[128:143], v[192:195], v[64:67], v[128:143]\n"
    "ds_read_b128 v[192:195], v246 offset:22528\n"
    "buffer_load_dwordx4 a[216:219], v245, s[40:43], s80 offen offset:2048 sc1\n"
    "v_fmac_f32 v220, v240, v216\n"
    "v_fmac_f32 v221, v240, v217\n"
    "v_mfma_f32_32x32x16_bf16 v[128:143], v[196:199], v[68:71], v[128:143]\n"
    "ds_read_b128 v[196:199], v246 offset:23552\n"
    "v_fmac_f32 v222, v240, v218\n"
    "v_fmac_f32 v223, v240, v219\n"
    "v_fmac_f32 v220, v239, v164\n"
    "v_fmac_f32 v221, v239, v165\n"
    "v_fmac_f32 v222, v239, v166\n"
    "s_waitcnt lgkmcnt(4)\n"
    "v_mfma_f32_32x32x16_bf16 v[128:143], v[176:179], v[72:75], v[128:143]\n"
    "ds_read_b128 v[176:179], v246 offset:24576\n"
    "buffer_load_dwordx4 a[220:223], v245, s[40:43], s81 offen offset:2048 sc1\n"
    "v_fmac_f32 v223, v239, v167\n"
    "ds_read_b128 v[200:203], v249 offset:768\n"
    "ds_read_b128 v[204:207], v249 offset:1664\n"
    "v_mfma_f32_32x32x16_bf16 v[128:143], v[180:183], v[76:79], v[128:143]\n"
    "ds_read_b128 v[180:183], v246 offset:25600\n"
    "ds_read_b128 v[208:211], v249 offset:832\n"
    "ds_read_b128 v[212:215], v249 offset:1728\n"
    "v_exp_f32 v220, v220\n"
    "s_waitcnt lgkmcnt(8)\n"
    "v_mfma_f32_32x32x16_bf16 v[128:143], v[184:187], v[80:83], v[128:143]\n"
    "ds_read_b128 v[184:187], v246 offset:26624\n"
    "v_exp_f32 v221, v221\n"
    "v_exp_f32 v222, v222\n"
    "v_mfma_f32_32x32x16_bf16 v[128:143], v[188:191], v[84:87], v[128:143]\n"
    "ds_read_b128 v[188:191], v246 offset:27648\n"
    "v_exp_f32 v223, v223\n"
    "v_fmac_f32 v228, v240, v224\n"
    "v_fmac_f32 v229, v240, v225\n"
    "v_fmac_f32 v230, v240, v226\n"
    "s_waitcnt lgkmcnt(8)\n"
    "v_mfma_f32_32x32x16_bf16 v[128:143], v[192:195], v[88:91], v[128:143]\n"
    "ds_read_b128 v[192:195], v246 offset:28672\n"
    "v_fmac_f32 v231, v240, v227\n"
    "v_add_f32 v220, 1.0, v220\n"
    "v_add_f32 v221, 1.0, v221\n"
    "v_add_f32 v222, 1.0, v222\n"
    "v_mfma_f32_32x32x16_bf16 v[128:143], v[196:199], v[92:95], v[128:143]\n"
    "ds_read_b128 v[196:199], v246 offset:29696\n"
    "v_add_f32 v223, 1.0, v223\n"
    "v_rcp_f32 v220, v220\n"
    "v_rcp_f32 v221, v221\n"
    "s_waitcnt lgkmcnt(6)\n"
    "v_mfma_f32_32x32x16_bf16 v[128:143], v[176:179], v[96:99], v[128:143]\n"
    "ds_read_b128 v[176:179], v246 offset:30720\n"
    "v_rcp_f32 v222, v222\n"
    "v_rcp_f32 v223, v223\n"
    "s_waitcnt vmcnt(5)\n"
    "v_cmp_gt_u32 vcc, 3, v241\n"
    "s_cbranch_vccnz .Lk_reA\n"
    ".Lk_goA:\n"
    "buffer_load_dwordx4 v[0:3], v246, s[40:43], s13 offen offset:0 sc1\n"
    "v_mfma_f32_32x32x16_bf16 v[128:143], v[180:183], v[100:103], v[128:143]\n"
    "ds_read_b128 v[180:183], v246 offset:31744\n"
    "v_fmac_f32 v228, v238, v172\n"
    "v_fmac_f32 v229, v238, v173\n"
    "v_fmac_f32 v230, v238, v174\n"
    "v_fmac_f32 v231, v238, v175\n"
    "v_mul_f32 v228, v228, v220\n"
    "buffer_load_dwordx4 v[4:7], v246, s[40:43], s14 offen offset:0 sc1\n"
    "s_waitcnt lgkmcnt(4)\n"
    "v_mfma_f32_32x32x16_bf16 v[128:143], v[184:187], v[104:107], v[128:143]\n"
    "v_mul_f32 v229, v229, v221\n"
    "v_mul_f32 v230, v230, v222\n"
    "v_mul_f32 v231, v231, v223\n"
    "v_cvt_pk_bf16_f32 v234, v228, v229\n"
    "buffer_load_dwordx4 v[8:11], v243, s[40:43], s13 offen offset:0 sc1\n"
    "v_mfma_f32_32x32x16_bf16 v[128:143], v[188:191], v[108:111], v[128:143]\n"
    "v_cvt_pk_bf16_f32 v235, v230, v231\n"
    "buffer_store_dwordx4 v[232:235], v246, s[40:43], s83 offen offset:1024 sc1\n"
    "ds_read_b128 v[216:219], v249 offset:800\n"
    "ds_read_b128 v[220:223], v249 offset:1696\n"
    "ds_read_b128 v[224:227], v249 offset:864\n"
    "buffer_load_dwordx4 v[12:15], v243, s[40:43], s14 offen offset:0 sc1\n"
    "s_waitcnt lgkmcnt(5)\n"
    "v_mfma_f32_32x32x16_bf16 v[128:143], v[192:195], v[112:115], v[128:143]\n"
    "ds_read_b128 v[228:231], v249 offset:1760\n"
    "buffer_load_dwordx4 v[16:19], v244, s[40:43], s13 offen offset:0 sc1\n"
    "v_mfma_f32_32x32x16_bf16 v[128:143], v[196:199], v[116:119], v[128:143]\n"
    "buffer_load_dwordx4 v[20:23], v244, s[40:43], s14 offen offset:0 sc1\n"
    "s_waitcnt lgkmcnt(4)\n"
    "v_mfma_f32_32x32x16_bf16 v[128:143], v[176:179], v[120:123], v[128:143]\n"
    "buffer_load_dwordx4 v[24:27], v245, s[40:43], s13 offen offset:0 sc1\n"
    "v_mfma_f32_32x32x16_bf16 v[128:143], v[180:183], v[124:127], v[128:143]\n"
    "buffer_load_dwordx4 v[28:31], v245, s[40:43], s14 offen offset:0 sc1\n"
    "s_nop 7\n"
    "v_fmac_f32 v204, v240, v200\n"
    "v_fmac_f32 v205, v240, v201\n"
    "v_fmac_f32 v206, v240, v202\n"
    "v_fmac_f32 v207, v240, v203\n"
    "v_fmac_f32 v204, v239, v128\n"
    "v_fmac_f32 v205, v239, v129\n"
    "v_fmac_f32 v206, v239, v130\n"
    "v_fmac_f32 v207, v239, v131\n"
    "buffer_load_dwordx4 v[32:35], v246, s[40:43], s13 offen offset:1024 sc1\n"
    "v_exp_f32 v204, v204\n"
    "v_exp_f32 v205, v205\n"
    "v_exp_f32 v206, v206\n"
    "v_exp_f32 v207, v207\n"
    "v_fmac_f32 v212, v240, v208\n"
    "v_fmac_f32 v213, v240, v209\n"
    "v_fmac_f32 v214, v240, v210\n"
    "v_fmac_f32 v215, v240, v211\n"
    "buffer_load_dwordx4 v[36:39], v246, s[40:43], s14 offen offset:1024 sc1\n"
    "v_add_f32 v204, 1.0, v204\n"
    "v_add_f32 v205, 1.0, v205\n"
    "v_add_f32 v206, 1.0, v206\n"
    "v_add_f32 v207, 1.0, v207\n"
    "v_rcp_f32 v204, v204\n"
    "v_rcp_f32 v205, v205\n"
    "v_rcp_f32 v206, v206\n"
    "v_rcp_f32 v207, v207\n"
    "buffer_load_dwordx4 v[40:43], v243, s[40:43], s13 offen offset:1024 sc1\n"
    "v_fmac_f32 v212, v238, v136\n"
    "v_fmac_f32 v213, v238, v137\n"
    "v_fmac_f32 v214, v238, v138\n"
    "v_fmac_f32 v215, v238, v139\n"
    "v_mul_f32 v212, v212, v204\n"
    "v_mul_f32 v213, v213, v205\n"
    "v_mul_f32 v214, v214, v206\n"
    "v_mul_f32 v215, v215, v207\n"
    "buffer_load_dwordx4 v[44:47], v243, s[40:43], s14 offen offset:1024 sc1\n"
    "v_cvt_pk_bf16_f32 v232, v212, v213\n"
    "v_cvt_pk_bf16_f32 v233, v214, v215\n"
    "s_waitcnt lgkmcnt(0)\n"
    "v_fmac_f32 v220, v240, v216\n"
    "v_fmac_f32 v221, v240, v217\n"
    "v_fmac_f32 v222, v240, v218\n"
    "v_fmac_f32 v223, v240, v219\n"
    "v_fmac_f32 v220, v239, v132\n"
    "v_fmac_f32 v221, v239, v133\n"
    "buffer_load_dwordx4 v[48:51], v244, s[40:43], s13 offen offset:1024 sc1\n"
    "v_fmac_f32 v222, v239, v134\n"
    "v_fmac_f32 v223, v239, v135\n"
    "v_exp_f32 v220, v220\n"
    "v_exp_f32 v221, v221\n"
    "v_exp_f32 v222, v222\n"
    "v_exp_f32 v223, v223\n"
    "v_fmac_f32 v228, v240, v224\n"
    "v_fmac_f32 v229, v240, v225\n"
    "buffer_load_dwordx4 v[52:55], v244, s[40:43], s14 offen offset:1024 sc1\n"
    "v_fmac_f32 v230, v240, v226\n"
    "v_fmac_f32 v231, v240, v227\n"
    "v_add_f32 v220, 1.0, v220\n"
    "v_add_f32 v221, 1.0, v221\n"
    "v_add_f32 v222, 1.0, v222\n"
    "v_add_f32 v223, 1.0, v223\n"
    "v_rcp_f32 v220, v220\n"
    "v_rcp_f32 v221, v221\n"
    "buffer_load_dwordx4 v[56:59], v245, s[40:43], s13 offen offset:1024 sc1\n"
    "v_rcp_f32 v222, v222\n"
    "v_rcp_f32 v223, v223\n"
    "v_fmac_f32 v228, v238, v140\n"
    "v_fmac_f32 v229, v238, v141\n"
    "v_fmac_f32 v230, v238, v142\n"
    "v_fmac_f32 v231, v238, v143\n"
    "v_mul_f32 v228, v228, v220\n"
    "v_mul_f32 v229, v229, v221\n"
    "buffer_load_dwordx4 v[60:63], v245, s[40:43], s14 offen offset:1024 sc1\n"
    "v_mul_f32 v230, v230, v222\n"
    "v_mul_f32 v231, v231, v223\n"
    "v_cvt_pk_bf16_f32 v234, v228, v229\n"
    "v_cvt_pk_bf16_f32 v235, v230, v231\n"
    "buffer_store_dwordx4 v[232:235], v246, s[40:43], s83 offen offset:2048 sc1\n"
    "buffer_load_dwordx4 v[64:67], v246, s[40:43], s13 offen offset:2048 sc1\n"
    "buffer_load_dwordx4 v[68:71], v246, s[40:43], s14 offen offset:2048 sc1\n"
    "s_waitcnt vmcnt(18)\n"
    "v_mfma_f32_32x32x16_bf16 v[144:159], a[0:3], v[0:3], 0\n"
    "buffer_load_dwordx4 v[72:75], v243, s[40:43], s13 offen offset:2048 sc1\n"
    "v_lshrrev_b32 v222, 4, v246\n"
    "v_and_b32 v219, 31, v222\n"
    "v_lshrrev_b32 v220, 5, v222\n"
    "s_lshl_b32 s54, s88, 15\n"
    "v_mfma_f32_32x32x16_bf16 a[224:239], a[4:7], v[0:3], 0\n"
    "buffer_load_dwordx4 v[76:79], v243, s[40:43], s14 offen offset:2048 sc1\n"
    "s_lshl_b32 s55, s87, 11\n"
    "s_add_u32 s55, s55, s54\n"
    "s_add_u32 s55, s55, %[cX2]\n"
    "v_lshlrev_b32 v216, 2, v219\n"
    "v_mfma_f32_32x32x16_bf16 v[160:175], a[0:3], v[4:7], 0\n"
    "s_waitcnt vmcnt(17)\n"
    "v_lshl_add_u32 v216, v220, 8, v216\n"
    "v_add_u32 v216, s55, v216\n"
    "s_lshl_b32 s55, s87, 6\n"
    "s_add_u32 s54, s54, s55\n"
    "v_mfma_f32_32x32x16_bf16 a[240:255], a[4:7], v[4:7], 0\n"
    "v_lshlrev_b32 v217, 4, v220\n"
    "v_lshl_add_u32 v218, v219, 10, v217\n"
    "v_add_u32 v218, s54, v218\n"
    "s_lshl_b32 s55, s87, 7\n"
    "s_add_u32 s55, s55, %[cGB]\n"
    "v_mfma_f32_32x32x16_bf16 v[144:159], a[8:11], v[8:11], v[144:159]\n"
    "buffer_load_dwordx4 v[80:83], v244, s[40:43], s13 offen offset:2048 sc1\n"
    "v_add_u32 v217, s55, v217\n"
    "s_lshl_b32 s54, s49, 16\n"
    "s_mul_i32 s55, s50, 12288\n"
    "s_add_u32 s56, s54, s55\n"
    "v_mfma_f32_32x32x16_bf16 a[224:239], a[12:15], v[8:11], a[224:239]\n"
    "buffer_load_dwordx4 v[84:87], v244, s[40:43], s14 offen offset:2048 sc1\n"
    "s_add_u32 s56, s56, 32768\n"
    "v_add_u32 v222, s56, v246\n"
    "s_sub_u32 s56, s54, s55\n"
    "s_add_u32 s56, s56, 45056\n"
    "v_mfma_f32_32x32x16_bf16 v[160:175], a[8:11], v[12:15], v[160:175]\n"
    "s_waitcnt vmcnt(17)\n"
    "v_add_u32 v219, s56, v246\n"
    "s_sub_u32 s56, s55, s54\n"
    "s_add_u32 s56, s56, 102400\n"
    "v_add_u32 v220, s56, v246\n"
    "v_mfma_f32_32x32x16_bf16 a[240:255], a[12:15], v[12:15], a[240:255]\n"
    "s_add_u32 s56, s54, s55\n"
    "s_sub_u32 s56, 118784, s56\n"
    "v_add_u32 v221, s56, v246\n"
    "v_mfma_f32_32x32x16_bf16 v[144:159], a[16:19], v[16:19], v[144:159]\n"
    "buffer_load_dwordx4 v[88:91], v245, s[40:43], s13 offen offset:2048 sc1\n"
    "v_mfma_f32_32x32x16_bf16 a[224:239], a[20:23], v[16:19], a[224:239]\n"
    "buffer_load_dwordx4 v[92:95], v245, s[40:43], s14 offen offset:2048 sc1\n"
    "v_mfma_f32_32x32x16_bf16 v[160:175], a[16:19], v[20:23], v[160:175]\n"
    "s_waitcnt vmcnt(17)\n"
    "v_mfma_f32_32x32x16_bf16 a[240:255], a[20:23], v[20:23], a[240:255]\n"
    "v_mfma_f32_32x32x16_bf16 v[144:159], a[24:27], v[24:27], v[144:159]\n"
    "buffer_load_dwordx4 v[96:99], v246, s[40:43], s13 offen offset:3072 sc1\n"
    "v_mfma_f32_32x32x16_bf16 a[224:239], a[28:31], v[24:27], a[224:239]\n"
    "buffer_load_dwordx4 v[100:103], v246, s[40:43], s14 offen offset:3072 sc1\n"
    "v_mfma_f32_32x32x16_bf16 v[160:175], a[24:27], v[28:31], v[160:175]\n"
    "s_waitcnt vmcnt(17)\n"
    "v_mfma_f32_32x32x16_bf16 a[240:255], a[28:31], v[28:31], a[240:255]\n"
    "v_mfma_f32_32x32x16_bf16 v[144:159], a[32:35], v[32:35], v[144:159]\n"
    "buffer_load_dwordx4 v[104:107], v243, s[40:43], s13 offen offset:3072 sc1\n"
    "v_mfma_f32_32x32x16_bf16 a[224:239], a[36:39], v[32:35], a[224:239]\n"
    "buffer_load_dwordx4 v[108:111], v243, s[40:43], s14 offen offset:3072 sc1\n"
    "v_mfma_f32_32x32x16_bf16 v[160:175], a[32:35], v[36:39], v[160:175]\n"
    "s_waitcnt vmcnt(17)\n"
    "v_mfma_f32_32x32x16_bf16 a[240:255], a[36:39], v[36:39], a[240:255]\n"
    "v_mfma_f32_32x32x16_bf16 v[144:159], a[40:43], v[40:43], v[144:159]\n"
    "buffer_load_dwordx4 v[112:115], v244, s[40:43], s13 offen offset:3072 sc1\n"
    "v_mfma_f32_32x32x16_bf16 a[224:239], a[44:47], v[40:43], a[224:239]\n"
    "buffer_load_dwordx4 v[116:119], v244, s[40:43], s14 offen offset:3072 sc1\n"
    "v_mfma_f32_32x32x16_bf16 v[160:175], a[40:43], v[44:47], v[160:175]\n"
    "s_waitcnt vmcnt(17)\n"
    "v_mfma_f32_32x32x16_bf16 a[240:255], a[44:47], v[44:47], a[240:255]\n"
    "v_mfma_f32_32x32x16_bf16 v[144:159], a[48:51], v[48:51], v[144:159]\n"
    "buffer_load_dwordx4 v[120:123], v245, s[40:43], s13 offen offset:3072 sc1\n"
    "v_mfma_f32_32x32x16_bf16 a[224:239], a[52:55], v[48:51], a[224:239]\n"
    "buffer_load_dwordx4 v[124:127], v245, s[40:43], s14 offen offset:3072 sc1\n"
    "v_mfma_f32_32x32x16_bf16 v[160:175], a[48:51], v[52:55], v[160:175]\n"
    "s_waitcnt vmcnt(17)\n"
    "v_mfma_f32_32x32x16_bf16 a[240:255], a[52:55], v[52:55], a[240:255]\n"
    "v_mfma_f32_32x32x16_bf16 v[144:159], a[56:59], v[56:59], v[144:159]\n"
    "buffer_load_dwordx4 v[0:3], v246, s[40:43], s15 offen offset:0 sc1\n"
    "v_mfma_f32_32x32x16_bf16 a[224:239], a[60:63], v[56:59], a[224:239]\n"
    "buffer_load_dwordx4 v[4:7], v246, s[40:43], s16 offen offset:0 sc1\n"
    "v_mfma_f32_32x32x16_bf16 v[160:175], a[56:59], v[60:63], v[160:175]\n"
    "s_waitcnt vmcnt(16)\n"
    "v_mfma_f32_32x32x16_bf16 a[240:255], a[60:63], v[60:63], a[240:255]\n"
    "v_mfma_f32_32x32x16_bf16 v[144:159], a[64:67], v[64:67], v[144:159]\n"
    "buffer_load_dwordx4 v[8:11], v243, s[40:43], s15 offen offset:0 sc1\n"
    "v_mfma_f32_32x32x16_bf16 a[224:239], a[68:71], v[64:67], a[224:239]\n"
    "buffer_load_dwordx4 v[12:15], v243, s[40:43], s16 offen offset:0 sc1\n"
    "v_mfma_f32_32x32x16_bf16 v[160:175], a[64:67], v[68:71], v[160:175]\n"
    "s_waitcnt vmcnt(16)\n"
    "v_mfma_f32_32x32x16_bf16 a[240:255], a[68:71], v[68:71], a[240:255]\n"
    "v_mfma_f32_32x32x16_bf16 v[144:159], a[72:75], v[72:75], v[144:159]\n"
    "buffer_load_dwordx4 v[16:19], v244, s[40:43], s15 offen offset:0 sc1\n"
    "v_mfma_f32_32x32x16_bf16 a[224:239], a[76:79], v[72:75], a[224:239]\n"
    "buffer_load_dwordx4 v[20:23], v244, s[40:43], s16 offen offset:0 sc1\n"
    "v_mfma_f32_32x32x16_bf16 v[160:175], a[72:75], v[76:79], v[160:175]\n"
    "s_waitcnt vmcnt(16)\n"
    "v_mfma_f32_32x32x16_bf16 a[240:255], a[76:79], v[76:79], a[240:255]\n"
    "v_mfma_f32_32x32x16_bf16 v[144:159], a[80:83], v[80:83], v[144:159]\n"
    "buffer_load_dwordx4 v[24:27], v245, s[40:43], s15 offen offset:0 sc1\n"
    "v_mfma_f32_32x32x16_bf16 a[224:239], a[84:87], v[80:83], a[224:239]\n"
    "buffer_load_dwordx4 v[28:31], v245, s[40:43], s16 offen offset:0 sc1\n"
    "v_mfma_f32_32x32x16_bf16 v[160:175], a[80:83], v[84:87], v[160:175]\n"
    "s_waitcnt vmcnt(16)\n"
    "v_mfma_f32_32x32x16_bf16 a[240:255], a[84:87], v[84:87], a[240:255]\n"
    "v_mfma_f32_32x32x16_bf16 v[144:159], a[88:91], v[88:91], v[144:159]\n"
    "buffer_load_dwordx4 v[32:35], v246, s[40:43], s15 offen offset:1024 sc1\n"
    "v_mfma_f32_32x32x16_bf16 a[224:239], a[92:95], v[88:91], a[224:239]\n"
    "buffer_load_dwordx4 v[36:39], v246, s[40:43], s16 offen offset:1024 sc1\n"
    "v_mfma_f32_32x32x16_bf16 v[160:175], a[88:91], v[92:95], v[160:175]\n"
    "s_waitcnt vmcnt(16)\n"
    "v_max3_u32 v251, v96, v97, v98\n"
    "v_max3_u32 v247, v100, v101, v102\n"
    "v_max3_u32 v251, v251, v99, v103\n"
    "v_max_u32 v251, v251, v247\n"
    "v_mfma_f32_32x32x16_bf16 a[240:255], a[92:95], v[92:95], a[240:255]\n"
    "v_cmp_eq_u32 vcc, -1, v251\n"
    "s_cbranch_vccnz .Lk_reh12\n"
    ".Lk_goh12:\n"
    "v_mfma_f32_32x32x16_bf16 v[144:159], a[96:99], v[96:99], v[144:159]\n"
    "buffer_load_dwordx4 v[40:43], v243, s[40:43], s15 offen offset:1024 sc1\n"
    "v_mfma_f32_32x32x16_bf16 a[224:239], a[100:103], v[96:99], a[224:239]\n"
    "buffer_load_dwordx4 v[44:47], v243, s[40:43], s16 offen offset:1024 sc1\n"
    "v_mfma_f32_32x32x16_bf16 v[160:175], a[96:99], v[100:103], v[160:175]\n"
    "s_waitcnt vmcnt(16)\n"
    "v_max3_u32 v251, v104, v105, v106\n"
    "v_max3_u32 v247, v108, v109, v110\n"
    "v_max3_u32 v251, v251, v107, v111\n"
    "v_max_u32 v251, v251, v247\n"
    "v_mfma_f32_32x32x16_bf16 a[240:255], a[100:103], v[100:103], a[240:255]\n"
    "v_cmp_eq_u32 vcc, -1, v251\n"
    "s_cbranch_vccnz .Lk_reh13\n"
    ".Lk_goh13:\n"
    "v_mfma_f32_32x32x16_bf16 v[144:159], a[104:107], v[104:107], v[144:159]\n"
    "buffer_load_dwordx4 v[48:51], v244, s[40:43], s15 offen offset:1024 sc1\n"
    "v_mfma_f32_32x32x16_bf16 a[224:239], a[108:111], v[104:107], a[224:239]\n"
    "buffer_load_dwordx4 v[52:55], v244, s[40:43], s16 offen offset:1024 sc1\n"
    "v_mfma_f32_32x32x16_bf16 v[160:175], a[104:107], v[108:111], v[160:175]\n"
    "s_waitcnt vmcnt(16)\n"
    "v_max3_u32 v251, v112, v113, v114\n"
    "v_max3_u32 v247, v116, v117, v118\n"
    "v_max3_u32 v251, v251, v115, v119\n"
    "v_max_u32 v251, v251, v247\n"
    "v_mfma_f32_32x32x16_bf16 a[240:255], a[108:111], v[108:111], a[240:255]\n"
    "v_cmp_eq_u32 vcc, -1, v251\n"
    "s_cbranch_vccnz .Lk_reh14\n"
    ".Lk_goh14:\n"
    "v_mfma_f32_32x32x16_bf16 v[144:159], a[112:115], v[112:115], v[144:159]\n"
    "buffer_load_dwordx4 v[56:59], v245, s[40:43], s15 offen offset:1024 sc1\n"
    "v_mfma_f32_32x32x16_bf16 a[224:239], a[116:119], v[112:115], a[224:239]\n"
    "buffer_load_dwordx4 v[60:63], v245, s[40:43], s16 offen offset:1024 sc1\n"
    "v_mfma_f32_32x32x16_bf16 v[160:175], a[112:115], v[116:119], v[160:175]\n"
    "s_waitcnt vmcnt(16)\n"
    "v_max3_u32 v251, v120, v121, v122\n"
    "v_max3_u32 v247, v124, v125, v126\n"
    "v_max3_u32 v251, v251, v123, v127\n"
    "v_max_u32 v251, v251, v247\n"
    "v_mfma_f32_32x32x16_bf16 a[240:255], a[116:119], v[116:119], a[240:255]\n"
    "v_cmp_eq_u32 vcc, -1, v251\n"
    "s_cbranch_vccnz .Lk_reh15\n"
    ".Lk_goh15:\n"
    "v_mfma_f32_32x32x16_bf16 v[144:159], a[120:123], v[120:123], v[144:159]\n"
    "buffer_load_dwordx4 v[64:67], v246, s[40:43], s15 offen offset:2048 sc1\n"
    "v_mfma_f32_32x32x16_bf16 a[224:239], a[124:127], v[120:123], a[224:239]\n"
    "buffer_load_dwordx4 v[68:71], v246, s[40:43], s16 offen offset:2048 sc1\n"
    "v_mfma_f32_32x32x16_bf16 v[160:175], a[120:123], v[124:127], v[160:175]\n"
    "s_waitcnt vmcnt(16)\n"
    "v_max3_u32 v251, v0, v1, v2\n"
    "v_max3_u32 v247, v4, v5, v6\n"
    "v_max3_u32 v251, v251, v3, v7\n"
    "v_max_u32 v251, v251, v247\n"
    "v_mfma_f32_32x32x16_bf16 a[240:255], a[124:127], v[124:127], a[240:255]\n"
    "v_cmp_eq_u32 vcc, -1, v251\n"
    "s_cbranch_vccnz .Lk_reh16\n"
    ".Lk_goh16:\n"
    "v_mfma_f32_32x32x16_bf16 v[144:159], a[128:131], v[0:3], v[144:159]\n"
    "buffer_load_dwordx4 v[72:75], v243, s[40:43], s15 offen offset:2048 sc1\n"
    "v_mfma_f32_32x32x16_bf16 a[224:239], a[132:135], v[0:3], a[224:239]\n"
    "buffer_load_dwordx4 v[76:79], v243, s[40:43], s16 offen offset:2048 sc1\n"
    "v_mfma_f32_32x32x16_bf16 v[160:175], a[128:131], v[4:7], v[160:175]\n"
    "s_waitcnt vmcnt(16)\n"
    "v_max3_u32 v251, v8, v9, v10\n"
    "v_max3_u32 v247, v12, v13, v14\n"
    "v_max3_u32 v251, v251, v11, v15\n"
    "v_max_u32 v251, v251, v247\n"
    "v_mfma_f32_32x32x16_bf16 a[240:255], a[132:135], v[4:7], a[240:255]\n"
    "v_cmp_eq_u32 vcc, -1, v251\n"
    "s_cbranch_vccnz .Lk_reh17\n"
    ".Lk_goh17:\n"
    "v_mfma_f32_32x32x16_bf16 v[144:159], a[136:139], v[8:11], v[144:159]\n"
    "buffer_load_dwordx4 v[80:83], v244, s[40:43], s15 offen offset:2048 sc1\n"
    "v_mfma_f32_32x32x16_bf16 a[224:239], a[140:143], v[8:11], a[224:239]\n"
    "buffer_load_dwordx4 v[84:87], v244, s[40:43], s16 offen offset:2048 sc1\n"
    "v_mfma_f32_32x32x16_bf16 v[160:175], a[136:139], v[12:15], v[160:175]\n"
    "s_waitcnt vmcnt(16)\n"
    "v_max3_u32 v251, v16, v17, v18\n"
    "v_max3_u32 v247, v20, v21, v22\n"
    "v_max3_u32 v251, v251, v19, v23\n"
    "v_max_u32 v251, v251, v247\n"
    "v_mfma_f32_32x32x16_bf16 a[240:255], a[140:143], v[12:15], a[240:255]\n"
    "v_cmp_eq_u32 vcc, -1, v251\n"
    "s_cbranch_vccnz .Lk_reh18\n"
    ".Lk_goh18:\n"
    "v_mfma_f32_32x32x16_bf16 v[144:159], a[144:147], v[16:19], v[144:159]\n"
    "buffer_load_dwordx4 v[88:91], v245, s[40:43], s15 offen offset:2048 sc1\n"
    "v_mfma_f32_32x32x16_bf16 a[224:239], a[148:151], v[16:19], a[224:239]\n"
    "buffer_load_dwordx4 v[92:95], v245, s[40:43], s16 offen offset:2048 sc1\n"
    "v_mfma_f32_32x32x16_bf16 v[160:175], a[144:147], v[20:23], v[160:175]\n"
    "s_waitcnt vmcnt(16)\n"
    "v_max3_u32 v251, v24, v25, v26\n"
    "v_max3_u32 v247, v28, v29, v30\n"
    "v_max3_u32 v251, v251, v27, v31\n"
    "v_max_u32 v251, v251, v247\n"
    "v_mfma_f32_32x32x16_bf16 a[240:255], a[148:151], v[20:23], a[240:255]\n"
    "v_cmp_eq_u32 vcc, -1, v251\n"
    "s_cbranch_vccnz .Lk_reh19\n"
    ".Lk_goh19:\n"
    "v_mfma_f32_32x32x16_bf16 v[144:159], a[152:155], v[24:27], v[144:159]\n"
    "v_mfma_f32_32x32x16_bf16 a[224:239], a[156:159], v[24:27], a[224:239]\n"
    "v_mfma_f32_32x32x16_bf16 v[160:175], a[152:155], v[28:31], v[160:175]\n"
    "s_waitcnt vmcnt(14)\n"
    "v_max3_u32 v251, v32, v33, v34\n"
    "v_max3_u32 v247, v36, v37, v38\n"
    "v_max3_u32 v251, v251, v35, v39\n"
    "v_max_u32 v251, v251, v247\n"
    "v_mfma_f32_32x32x16_bf16 a[240:255], a[156:159], v[28:31], a[240:255]\n"
    "v_cmp_eq_u32 vcc, -1, v251\n"
    "s_cbranch_vccnz .Lk_reh20\n"
    ".Lk_goh20:\n"
    "buffer_load_dword v176, v216, s[40:43], 0 offen offset:0 sc1\n"
    "buffer_load_dword v177, v216, s[40:43], 0 offen offset:128 sc1\n"
    "buffer_load_dwordx4 v[184:187], v217, s[40:43], 0 offen offset:0 sc1\n"
    "buffer_load_dword v178, v216, s[40:43], 0 offen offset:512 sc1\n"
    "buffer_load_dword v179, v216, s[40:43], 0 offen offset:640 sc1\n"
    "buffer_load_dwordx4 v[188:191], v217, s[40:43], 0 offen offset:32 sc1\n"
    "buffer_load_dword v180, v216, s[40:43], 0 offen offset:1024 sc1\n"
    "buffer_load_dword v181, v216, s[40:43], 0 offen offset:1152 sc1\n"
    "buffer_load_dwordx4 v[192:195], v217, s[40:43], 0 offen offset:64 sc1\n"
    "buffer_load_dword v182, v216, s[40:43], 0 offen offset:1536 sc1\n"
    "buffer_load_dword v183, v216, s[40:43], 0 offen offset:1664 sc1\n"
    "buffer_load_dwordx4 v[196:199], v217, s[40:43], 0 offen offset:96 sc1\n"
    "v_mfma_f32_32x32x16_bf16 v[144:159], a[160:163], v[32:35], v[144:159]\n"
    "v_mfma_f32_32x32x16_bf16 a[224:239], a[164:167], v[32:35], a[224:239]\n"
    "v_mfma_f32_32x32x16_bf16 v[160:175], a[160:163], v[36:39], v[160:175]\n"
    "s_waitcnt vmcnt(24)\n"
    "v_max3_u32 v251, v40, v41, v42\n"
    "v_max3_u32 v247, v44, v45, v46\n"
    "v_max3_u32 v251, v251, v43, v47\n"
    "v_max_u32 v251, v251, v247\n"
    "v_mfma_f32_32x32x16_bf16 a[240:255], a[164:167], v[36:39], a[240:255]\n"
    "v_cmp_eq_u32 vcc, -1, v251\n"
    "s_cbranch_vccnz .Lk_reh21\n"
    ".Lk_goh21:\n"
    "v_mfma_f32_32x32x16_bf16 v[144:159], a[168:171], v[40:43], v[144:159]\n"
    "v_mfma_f32_32x32x16_bf16 a[224:239], a[172:175], v[40:43], a[224:239]\n"
    "v_mfma_f32_32x32x16_bf16 v[160:175], a[168:171], v[44:47], v[160:175]\n"
    "s_waitcnt vmcnt(22)\n"
    "v_max3_u32 v251, v48, v49, v50\n"
    "v_max3_u32 v247, v52, v53, v54\n"
    "v_max3_u32 v251, v251, v51, v55\n"
    "v_max_u32 v251, v251, v247\n"
    "v_mfma_f32_32x32x16_bf16 a[240:255], a[172:175], v[44:47], a[240:255]\n"
    "v_cmp_eq_u32 vcc, -1, v251\n"
    "s_cbranch_vccnz .Lk_reh22\n"
    ".Lk_goh22:\n"
    "v_mfma_f32_32x32x16_bf16 v[144:159], a[176:179], v[48:51], v[144:159]\n"
    "v_mfma_f32_32x32x16_bf16 a[224:239], a[180:183], v[48:51], a[224:239]\n"
    "v_mfma_f32_32x32x16_bf16 v[160:175], a[176:179], v[52:55], v[160:175]\n"
    "s_waitcnt vmcnt(20)\n"
    "v_max3_u32 v251, v56, v57, v58\n"
    "v_max3_u32 v247, v60, v61, v62\n"
    "v_max3_u32 v251, v251, v59, v63\n"
    "v_max_u32 v251, v251, v247\n"
    "v_mfma_f32_32x32x16_bf16 a[240:255], a[180:183], v[52:55], a[240:255]\n"
    "v_cmp_eq_u32 vcc, -1, v251\n"
    "s_cbranch_vccnz .Lk_reh23\n"
    ".Lk_goh23:\n"
    "v_mfma_f32_32x32x16_bf16 v[144:159], a[184:187], v[56:59], v[144:159]\n"
    "v_mfma_f32_32x32x16_bf16 a[224:239], a[188:191], v[56:59], a[224:239]\n"
    "v_mfma_f32_32x32x16_bf16 v[160:175], a[184:187], v[60:63], v[160:175]\n"
    "s_waitcnt vmcnt(18)\n"
    "v_max3_u32 v251, v64, v65, v66\n"
    "v_max3_u32 v247, v68, v69, v70\n"
    "v_max3_u32 v251, v251, v67, v71\n"
    "v_max_u32 v251, v251, v247\n"
    "v_mfma_f32_32x32x16_bf16 a[240:255], a[188:191], v[60:63], a[240:255]\n"
    "v_cmp_eq_u32 vcc, -1, v251\n"
    "s_cbranch_vccnz .Lk_reh24\n"
    ".Lk_goh24:\n"
    "s_waitcnt vmcnt(16)\n"
    "v_max3_u32 v251, v72, v73, v74\n"
    "v_max3_u32 v247, v76, v77, v78\n"
    "v_max3_u32 v251, v251, v75, v79\n"
    "v_max_u32 v251, v251, v247\n"
    "v_cmp_eq_u32 vcc, -1, v251\n"
    "s_cbranch_vccnz .Lk_reh25\n"
    ".Lk_goh25:\n"
    "s_waitcnt vmcnt(14)\n"
    "v_max3_u32 v251, v80, v81, v82\n"
    "v_max3_u32 v247, v84, v85, v86\n"
    "v_max3_u32 v251, v251, v83, v87\n"
    "v_max_u32 v251, v251, v247\n"
    "v_cmp_eq_u32 vcc, -1, v251\n"
    "s_cbranch_vccnz .Lk_reh26\n"
    ".Lk_goh26:\n"
    "s_waitcnt vmcnt(12)\n"
    "v_max3_u32 v251, v88, v89, v90\n"
    "v_max3_u32 v247, v92, v93, v94\n"
    "v_max3_u32 v251, v251, v91, v95\n"
    "v_max_u32 v251, v251, v247\n"
    "v_cmp_eq_u32 vcc, -1, v251\n"
    "s_cbranch_vccnz .Lk_reh27\n"
    ".Lk_goh27:\n"
    "v_mfma_f32_32x32x16_bf16 a[224:239], a[196:199], v[64:67], a[224:239]\n"
    "v_mfma_f32_32x32x16_bf16 a[224:239], a[204:207], v[72:75], a[224:239]\n"
    "v_mfma_f32_32x32x16_bf16 a[224:239], a[212:215], v[80:83], a[224:239]\n"
    "v_mfma_f32_32x32x16_bf16 a[224:239], a[220:223], v[88:91], a[224:239]\n"
    "v_mfma_f32_32x32x16_bf16 v[160:175], a[192:195], v[68:71], v[160:175]\n"
    "s_nop 10\n"
    "ds_write_b128 v220, a[224:227] offset:0\n"
    "v_mfma_f32_32x32x16_bf16 v[160:175], a[200:203], v[76:79], v[160:175]\n"
    "ds_write_b128 v220, a[228:231] offset:1024\n"
    "v_mfma_f32_32x32x16_bf16 v[160:175], a[208:211], v[84:87], v[160:175]\n"
    "ds_write_b128 v220, a[232:235] offset:2048\n"
    "v_mfma_f32_32x32x16_bf16 v[160:175], a[216:219], v[92:95], v[160:175]\n"
    "ds_write_b128 v220, a[236:239] offset:3072\n"
    "v_mfma_f32_32x32x16_bf16 a[240:255], a[196:199], v[68:71], a[240:255]\n"
    "s_nop 10\n"
    "ds_write_b128 v219, v[160:163] offset:0\n"
    "v_mfma_f32_32x32x16_bf16 a[240:255], a[204:207], v[76:79], a[240:255]\n"
    "ds_write_b128 v219, v[164:167] offset:1024\n"
    "v_mfma_f32_32x32x16_bf16 a[240:255], a[212:215], v[84:87], a[240:255]\n"
    "ds_write_b128 v219, v[168:171] offset:2048\n"
    "v_mfma_f32_32x32x16_bf16 a[240:255], a[220:223], v[92:95], a[240:255]\n"
    "ds_write_b128 v219, v[172:175] offset:3072\n"
    "v_mfma_f32_32x32x16_bf16 v[144:159], a[192:195], v[64:67], v[144:159]\n"
    "s_nop 10\n"
    "ds_write_b128 v221, a[240:243] offset:0\n"
    "v_mfma_f32_32x32x16_bf16 v[144:159], a[200:203], v[72:75], v[144:159]\n"
    "ds_write_b128 v221, a[244:247] offset:1024\n"
    "v_mfma_f32_32x32x16_bf16 v[144:159], a[208:211], v[80:83], v[144:159]\n"
    "ds_write_b128 v221, a[248:251] offset:2048\n"
    "v_mfma_f32_32x32x16_bf16 v[144:159], a[216:219], v[88:91], v[144:159]\n"
    "ds_write_b128 v221, a[252:255] offset:3072\n"
    "s_waitcnt vmcnt(0)\n"
    "v_lshlrev_b32 v64, 16, v176\n"
    "v_and_b32 v65, 0xffff0000, v176\n"
    "v_lshlrev_b32 v66, 16, v177\n"
    "v_and_b32 v67, 0xffff0000, v177\n"
    "v_lshlrev_b32 v68, 16, v178\n"
    "v_and_b32 v69, 0xffff0000, v178\n"
    "v_lshlrev_b32 v70, 16, v179\n"
    "v_and_b32 v71, 0xffff0000, v179\n"
    "v_lshlrev_b32 v72, 16, v180\n"
    "v_and_b32 v73, 0xffff0000, v180\n"
    "v_lshlrev_b32 v74, 16, v181\n"
    "v_and_b32 v75, 0xffff0000, v181\n"
    "v_lshlrev_b32 v76, 16, v182\n"
    "v_and_b32 v77, 0xffff0000, v182\n"
    "v_lshlrev_b32 v78, 16, v183\n"
    "v_and_b32 v79, 0xffff0000, v183\n"
    "s_waitcnt lgkmcnt(0)\n"
    "s_barrier\n"
    "ds_read_b128 v[0:3], v222 offset:0\n"
    "ds_read_b128 v[16:19], v222 offset:4096\n"
    "ds_read_b128 v[32:35], v222 offset:8192\n"
    "ds_read_b128 v[4:7], v222 offset:1024\n"
    "ds_read_b128 v[20:23], v222 offset:5120\n"
    "ds_read_b128 v[36:39], v222 offset:9216\n"
    "ds_read_b128 v[8:11], v222 offset:2048\n"
    "ds_read_b128 v[24:27], v222 offset:6144\n"
    "ds_read_b128 v[40:43], v222 offset:10240\n"
    "ds_read_b128 v[12:15], v222 offset:3072\n"
    "ds_read_b128 v[28:31], v222 offset:7168\n"
    "ds_read_b128 v[44:47], v222 offset:11264\n"
    "s_waitcnt lgkmcnt(9)\n"
    "v_pk_add_f32 v[144:145], v[144:145], v[0:1]\n"
    "v_pk_add_f32 v[146:147], v[146:147], v[2:3]\n"
    "v_pk_add_f32 v[144:145], v[144:145], v[16:17]\n"
    "v_pk_add_f32 v[146:147], v[146:147], v[18:19]\n"
    "v_pk_add_f32 v[144:145], v[144:145], v[32:33]\n"
    "v_pk_add_f32 v[146:147], v[146:147], v[34:35]\n"
    "v_pk_fma_f32 v[144:145], v[144:145], v[184:185], v[64:65]\n"
    "v_pk_fma_f32 v[146:147], v[146:147], v[186:187], v[66:67]\n"
    "v_cvt_pk_bf16_f32 v96, v144, v145\n"
    "v_cvt_pk_bf16_f32 v97, v146, v147\n"
    "s_waitcnt lgkmcnt(6)\n"
    "v_pk_add_f32 v[148:149], v[148:149], v[4:5]\n"
    "v_pk_add_f32 v[150:151], v[150:151], v[6:7]\n"
    "v_pk_add_f32 v[148:149], v[148:149], v[20:21]\n"
    "v_pk_add_f32 v[150:151], v[150:151], v[22:23]\n"
    "v_pk_add_f32 v[148:149], v[148:149], v[36:37]\n"
    "v_pk_add_f32 v[150:151], v[150:151], v[38:39]\n"
    "v_pk_fma_f32 v[148:149], v[148:149], v[188:189], v[68:69]\n"
    "v_pk_fma_f32 v[150:151], v[150:151], v[190:191], v[70:71]\n"
    "v_cvt_pk_bf16_f32 v98, v148, v149\n"
    "v_cvt_pk_bf16_f32 v99, v150, v151\n"
    "s_nop 1\n"
    "v_permlane32_swap_b32 v96, v98\n"
    "v_permlane32_swap_b32 v97, v99\n"
    "buffer_store_dwordx4 v[96:99], v218, s[44:47], 0 offen offset:0\n"
    "s_waitcnt lgkmcnt(3)\n"
    "v_pk_add_f32 v[152:153], v[152:153], v[8:9]\n"
    "v_pk_add_f32 v[154:155], v[154:155], v[10:11]\n"
    "v_pk_add_f32 v[152:153], v[152:153], v[24:25]\n"
    "v_pk_add_f32 v[154:155], v[154:155], v[26:27]\n"
    "v_pk_add_f32 v[152:153], v[152:153], v[40:41]\n"
    "v_pk_add_f32 v[154:155], v[154:155], v[42:43]\n"
    "v_pk_fma_f32 v[152:153], v[152:153], v[192:193], v[72:73]\n"
    "v_pk_fma_f32 v[154:155], v[154:155], v[194:195], v[74:75]\n"
    "v_cvt_pk_bf16_f32 v100, v152, v153\n"
    "v_cvt_pk_bf16_f32 v101, v154, v155\n"
    "s_waitcnt lgkmcnt(0)\n"
    "v_pk_add_f32 v[156:157], v[156:157], v[12:13]\n"
    "v_pk_add_f32 v[158:159], v[158:159], v[14:15]\n"
    "v_pk_add_f32 v[156:157], v[156:157], v[28:29]\n"
    "v_pk_add_f32 v[158:159], v[158:159], v[30:31]\n"
    "v_pk_add_f32 v[156:157], v[156:157], v[44:45]\n"
    "v_pk_add_f32 v[158:159], v[158:159], v[46:47]\n"
    "v_pk_fma_f32 v[156:157], v[156:157], v[196:197], v[76:77]\n"
    "v_pk_fma_f32 v[158:159], v[158:159], v[198:199], v[78:79]\n"
    "v_cvt_pk_bf16_f32 v102, v156, v157\n"
    "v_cvt_pk_bf16_f32 v103, v158, v159\n"
    "s_nop 1\n"
    "v_permlane32_swap_b32 v100, v102\n"
    "v_permlane32_swap_b32 v101, v103\n"
    "buffer_store_dwordx4 v[100:103], v218, s[44:47], 0 offen offset:32\n"
    "s_endpgm\n"
    ".Lk_rex0:\n"
    "s_mov_b32 s19, 0x40000\n"
    ".Lk_rlx0:\n"
    "s_sleep 2\n"
    "s_mov_b32 s56, m0\n"
    "s_add_u32 s54, s8, 0\n"
    "s_add_u32 m0, s10, 0\n"
    "s_nop 0\n"
    "buffer_load_dwordx4 v246, s[40:43], s54 offen offset:0 sc1 lds\n"
    "buffer_load_dwordx4 v246, s[40:43], s54 offen offset:1024 sc1 lds\n"
    "buffer_load_dwordx4 v246, s[40:43], s54 offen offset:2048 sc1 lds\n"
    "buffer_load_dwordx4 v246, s[40:43], s54 offen offset:3072 sc1 lds\n"
    "s_add_u32 s54, s8, 4096\n"
    "s_add_u32 m0, s10, 4096\n"
    "s_nop 0\n"
    "buffer_load_dwordx4 v246, s[40:43], s54 offen offset:0 sc1 lds\n"
    "buffer_load_dwordx4 v246, s[40:43], s54 offen offset:1024 sc1 lds\n"
    "s_mov_b32 m0, s56\n"
    "s_waitcnt vmcnt(0)\n"
    "ds_read2st64_b32 v[0:1], v252 offset0:0 offset1:1\n"
    "ds_read2st64_b32 v[2:3], v254 offset0:0 offset1:1\n"
    "ds_read2st64_b32 v[4:5], v252 offset0:4 offset1:5\n"
    "ds_read2st64_b32 v[6:7], v254 offset0:4 offset1:5\n"
    "s_waitcnt lgkmcnt(0)\n"
    "s_waitcnt vmcnt(0)\n"
    "v_max3_u32 v251, v0, v1, v2\n"
    "v_max3_u32 v247, v4, v5, v6\n"
    "v_max3_u32 v251, v251, v3, v7\n"
    "v_max_u32 v251, v251, v247\n"
    "v_cmp_eq_u32 vcc, -1, v251\n"
    "s_cbranch_vccz .Lk_gox0\n"
    "s_sub_u32 s19, s19, 1\n"
    "s_cmp_lg_u32 s19, 0\n"
    "s_cbranch_scc1 .Lk_rlx0\n"
    "s_branch .Lk_gox0\n"
    ".Lk_rex1:\n"
    "s_mov_b32 s19, 0x40000\n"
    ".Lk_rlx1:\n"
    "s_sleep 2\n"
    "s_mov_b32 s56, m0\n"
    "s_add_u32 s54, s8, 0\n"
    "s_add_u32 m0, s10, 0\n"
    "s_nop 0\n"
    "buffer_load_dwordx4 v246, s[40:43], s54 offen offset:2048 sc1 lds\n"
    "buffer_load_dwordx4 v246, s[40:43], s54 offen offset:3072 sc1 lds\n"
    "s_add_u32 s54, s8, 4096\n"
    "s_add_u32 m0, s10, 4096\n"
    "s_nop 0\n"
    "buffer_load_dwordx4 v246, s[40:43], s54 offen offset:0 sc1 lds\n"
    "buffer_load_dwordx4 v246, s[40:43], s54 offen offset:1024 sc1 lds\n"
    "buffer_load_dwordx4 v246, s[40:43], s54 offen offset:2048 sc1 lds\n"
    "buffer_load_dwordx4 v246, s[40:43], s54 offen offset:3072 sc1 lds\n"
    "s_mov_b32 m0, s56\n"
    "s_waitcnt vmcnt(0)\n"
    "ds_read2st64_b32 v[8:9], v252 offset0:8 offset1:9\n"
    "ds_read2st64_b32 v[10:11], v254 offset0:8 offset1:9\n"
    "ds_read2st64_b32 v[12:13], v252 offset0:12 offset1:13\n"
    "ds_read2st64_b32 v[14:15], v254 offset0:12 offset1:13\n"
    "s_waitcnt lgkmcnt(0)\n"
    "s_waitcnt vmcnt(0)\n"
    "v_max3_u32 v251, v8, v9, v10\n"
    "v_max3_u32 v247, v12, v13, v14\n"
    "v_max3_u32 v251, v251, v11, v15\n"
    "v_max_u32 v251, v251, v247\n"
    "v_cmp_eq_u32 vcc, -1, v251\n"
    "s_cbranch_vccz .Lk_gox1\n"
    "s_sub_u32 s19, s19, 1\n"
    "s_cmp_lg_u32 s19, 0\n"
    "s_cbranch_scc1 .Lk_rlx1\n"
    "s_branch .Lk_gox1\n"
    ".Lk_rex2:\n"
    "s_mov_b32 s19, 0x40000\n"
    ".Lk_rlx2:\n"
    "s_sleep 2\n"
    "s_mov_b32 s56, m0\n"
    "s_add_u32 s54, s8, 4096\n"
    "s_add_u32 m0, s10, 4096\n"
    "s_nop 0\n"
    "buffer_load_dwordx4 v246, s[40:43], s54 offen offset:0 sc1 lds\n"
    "buffer_load_dwordx4 v246, s[40:43], s54 offen offset:1024 sc1 lds\n"
    "buffer_load_dwordx4 v246, s[40:43], s54 offen offset:2048 sc1 lds\n"
    "buffer_load_dwordx4 v246, s[40:43], s54 offen offset:3072 sc1 lds\n"
    "s_add_u32 s54, s8, 8192\n"
    "s_add_u32 m0, s10, 32768\n"
    "s_nop 0\n"
    "buffer_load_dwordx4 v246, s[40:43], s54 offen offset:0 sc1 lds\n"
    "buffer_load_dwordx4 v246, s[40:43], s54 offen offset:1024 sc1 lds\n"
    "s_mov_b32 m0, s56\n"
    "s_waitcnt vmcnt(0)\n"
    "ds_read2st64_b32 v[16:17], v252 offset0:16 offset1:17\n"
    "ds_read2st64_b32 v[18:19], v254 offset0:16 offset1:17\n"
    "ds_read2st64_b32 v[20:21], v252 offset0:20 offset1:21\n"
    "ds_read2st64_b32 v[22:23], v254 offset0:20 offset1:21\n"
    "s_waitcnt lgkmcnt(0)\n"
    "s_waitcnt vmcnt(0)\n"
    "v_max3_u32 v251, v16, v17, v18\n"
    "v_max3_u32 v247, v20, v21, v22\n"
    "v_max3_u32 v251, v251, v19, v23\n"
    "v_max_u32 v251, v251, v247\n"
    "v_cmp_eq_u32 vcc, -1, v251\n"
    "s_cbranch_vccz .Lk_gox2\n"
    "s_sub_u32 s19, s19, 1\n"
    "s_cmp_lg_u32 s19, 0\n"
    "s_cbranch_scc1 .Lk_rlx2\n"
    "s_branch .Lk_gox2\n"
    ".Lk_rex3:\n"
    "s_mov_b32 s19, 0x40000\n"
    ".Lk_rlx3:\n"
    "s_sleep 2\n"
    "s_mov_b32 s56, m0\n"
    "s_add_u32 s54, s8, 4096\n"
    "s_add_u32 m0, s10, 4096\n"
    "s_nop 0\n"
    "buffer_load_dwordx4 v246, s[40:43], s54 offen offset:2048 sc1 lds\n"
    "buffer_load_dwordx4 v246, s[40:43], s54 offen offset:3072 sc1 lds\n"
    "s_add_u32 s54, s8, 8192\n"
    "s_add_u32 m0, s10, 32768\n"
    "s_nop 0\n"
    "buffer_load_dwordx4 v246, s[40:43], s54 offen offset:0 sc1 lds\n"
    "buffer_load_dwordx4 v246, s[40:43], s54 offen offset:1024 sc1 lds\n"
    "buffer_load_dwordx4 v246, s[40:43], s54 offen offset:2048 sc1 lds\n"
    "buffer_load_dwordx4 v246, s[40:43], s54 offen offset:3072 sc1 lds\n"
    "s_mov_b32 m0, s56\n"
    "s_waitcnt vmcnt(0)\n"
    "ds_read2st64_b32 v[24:25], v252 offset0:24 offset1:25\n"
    "ds_read2st64_b32 v[26:27], v254 offset0:24 offset1:25\n"
    "ds_read2st64_b32 v[28:29], v252 offset0:28 offset1:29\n"
    "ds_read2st64_b32 v[30:31], v254 offset0:28 offset1:29\n"
    "s_waitcnt lgkmcnt(0)\n"
    "s_waitcnt vmcnt(0)\n"
    "v_max3_u32 v251, v24, v25, v26\n"
    "v_max3_u32 v247, v28, v29, v30\n"
    "v_max3_u32 v251, v251, v27, v31\n"
    "v_max_u32 v251, v251, v247\n"
    "v_cmp_eq_u32 vcc, -1, v251\n"
    "s_cbranch_vccz .Lk_gox3\n"
    "s_sub_u32 s19, s19, 1\n"
    "s_cmp_lg_u32 s19, 0\n"
    "s_cbranch_scc1 .Lk_rlx3\n"
    "s_branch .Lk_gox3\n"
    ".Lk_rex4:\n"
    "s_mov_b32 s19, 0x40000\n"
    ".Lk_rlx4:\n"
    "s_sleep 2\n"
    "s_mov_b32 s56, m0\n"
    "s_add_u32 s54, s8, 8192\n"
    "s_add_u32 m0, s10, 32768\n"
    "s_nop 0\n"
    "buffer_load_dwordx4 v246, s[40:43], s54 offen offset:0 sc1 lds\n"
    "buffer_load_dwordx4 v246, s[40:43], s54 offen offset:1024 sc1 lds\n"
    "buffer_load_dwordx4 v246, s[40:43], s54 offen offset:2048 sc1 lds\n"
    "buffer_load_dwordx4 v246, s[40:43], s54 offen offset:3072 sc1 lds\n"
    "s_add_u32 s54, s8, 12288\n"
    "s_add_u32 m0, s10, 36864\n"
    "s_nop 0\n"
    "buffer_load_dwordx4 v246, s[40:43], s54 offen offset:0 sc1 lds\n"
    "buffer_load_dwordx4 v246, s[40:43], s54 offen offset:1024 sc1 lds\n"
    "s_mov_b32 m0, s56\n"
    "s_waitcnt vmcnt(0)\n"
    "ds_read2st64_b32 v[32:33], v252 offset0:128 offset1:129\n"
    "ds_read2st64_b32 v[34:35], v254 offset0:128 offset1:129\n"
    "ds_read2st64_b32 v[36:37], v252 offset0:132 offset1:133\n"
    "ds_read2st64_b32 v[38:39], v254 offset0:132 offset1:133\n"
    "s_waitcnt lgkmcnt(0)\n"
    "s_waitcnt vmcnt(0)\n"
    "v_max3_u32 v251, v32, v33, v34\n"
    "v_max3_u32 v247, v36, v37, v38\n"
    "v_max3_u32 v251, v251, v35, v39\n"
    "v_max_u32 v251, v251, v247\n"
    "v_cmp_eq_u32 vcc, -1, v251\n"
    "s_cbranch_vccz .Lk_gox4\n"
    "s_sub_u32 s19, s19, 1\n"
    "s_cmp_lg_u32 s19, 0\n"
    "s_cbranch_scc1 .Lk_rlx4\n"
    "s_branch .Lk_gox4\n"
    ".Lk_rex5:\n"
    "s_mov_b32 s19, 0x40000\n"
    ".Lk_rlx5:\n"
    "s_sleep 2\n"
    "s_mov_b32 s56, m0\n"
    "s_add_u32 s54, s8, 8192\n"
    "s_add_u32 m0, s10, 32768\n"
    "s_nop 0\n"
    "buffer_load_dwordx4 v246, s[40:43], s54 offen offset:2048 sc1 lds\n"
    "buffer_load_dwordx4 v246, s[40:43], s54 offen offset:3072 sc1 lds\n"
    "s_add_u32 s54, s8, 12288\n"
    "s_add_u32 m0, s10, 36864\n"
    "s_nop 0\n"
    "buffer_load_dwordx4 v246, s[40:43], s54 offen offset:0 sc1 lds\n"
    "buffer_load_dwordx4 v246, s[40:43], s54 offen offset:1024 sc1 lds\n"
    "buffer_load_dwordx4 v246, s[40:43], s54 offen offset:2048 sc1 lds\n"
    "buffer_load_dwordx4 v246, s[40:43], s54 offen offset:3072 sc1 lds\n"
    "s_mov_b32 m0, s56\n"
    "s_waitcnt vmcnt(0)\n"
    "ds_read2st64_b32 v[40:41], v252 offset0:136 offset1:137\n"
    "ds_read2st64_b32 v[42:43], v254 offset0:136 offset1:137\n"
    "ds_read2st64_b32 v[44:45], v252 offset0:140 offset1:141\n"
    "ds_read2st64_b32 v[46:47], v254 offset0:140 offset1:141\n"
    "s_waitcnt lgkmcnt(0)\n"
    "s_waitcnt vmcnt(0)\n"
    "v_max3_u32 v251, v40, v41, v42\n"
    "v_max3_u32 v247, v44, v45, v46\n"
    "v_max3_u32 v251, v251, v43, v47\n"
    "v_max_u32 v251, v251, v247\n"
    "v_cmp_eq_u32 vcc, -1, v251\n"
    "s_cbranch_vccz .Lk_gox5\n"
    "s_sub_u32 s19, s19, 1\n"
    "s_cmp_lg_u32 s19, 0\n"
    "s_cbranch_scc1 .Lk_rlx5\n"
    "s_branch .Lk_gox5\n"
    ".Lk_rex6:\n"
    "s_mov_b32 s19, 0x40000\n"
    ".Lk_rlx6:\n"
    "s_sleep 2\n"
    "s_mov_b32 s56, m0\n"
    "s_add_u32 s54, s8, 12288\n"
    "s_add_u32 m0, s10, 36864\n"
    "s_nop 0\n"
    "buffer_load_dwordx4 v246, s[40:43], s54 offen offset:0 sc1 lds\n"
    "buffer_load_dwordx4 v246, s[40:43], s54 offen offset:1024 sc1 lds\n"
    "buffer_load_dwordx4 v246, s[40:43], s54 offen offset:2048 sc1 lds\n"
    "buffer_load_dwordx4 v246, s[40:43], s54 offen offset:3072 sc1 lds\n"
    "s_add_u32 s54, s8, 16384\n"
    "s_add_u32 m0, s10, 0\n"
    "s_nop 0\n"
    "buffer_load_dwordx4 v246, s[40:43], s54 offen offset:0 sc1 lds\n"
    "buffer_load_dwordx4 v246, s[40:43], s54 offen offset:1024 sc1 lds\n"
    "s_mov_b32 m0, s56\n"
    "s_waitcnt vmcnt(0)\n"
    "ds_read2st64_b32 v[48:49], v252 offset0:144 offset1:145\n"
    "ds_read2st64_b32 v[50:51], v254 offset0:144 offset1:145\n"
    "ds_read2st64_b32 v[52:53], v252 offset0:148 offset1:149\n"
    "ds_read2st64_b32 v[54:55], v254 offset0:148 offset1:149\n"
    "s_waitcnt lgkmcnt(0)\n"
    "s_waitcnt vmcnt(0)\n"
    "v_max3_u32 v251, v48, v49, v50\n"
    "v_max3_u32 v247, v52, v53, v54\n"
    "v_max3_u32 v251, v251, v51, v55\n"
    "v_max_u32 v251, v251, v247\n"
    "v_cmp_eq_u32 vcc, -1, v251\n"
    "s_cbranch_vccz .Lk_gox6\n"
    "s_sub_u32 s19, s19, 1\n"
    "s_cmp_lg_u32 s19, 0\n"
    "s_cbranch_scc1 .Lk_rlx6\n"
    "s_branch .Lk_gox6\n"
    ".Lk_rex7:\n"
    "s_mov_b32 s19, 0x40000\n"
    ".Lk_rlx7:\n"
    "s_sleep 2\n"
    "s_mov_b32 s56, m0\n"
    "s_add_u32 s54, s8, 12288\n"
    "s_add_u32 m0, s10, 36864\n"
    "s_nop 0\n"
    "buffer_load_dwordx4 v246, s[40:43], s54 offen offset:2048 sc1 lds\n"
    "buffer_load_dwordx4 v246, s[40:43], s54 offen offset:3072 sc1 lds\n"
    "s_add_u32 s54, s8, 16384\n"
    "s_add_u32 m0, s10, 0\n"
    "s_nop 0\n"
    "buffer_load_dwordx4 v246, s[40:43], s54 offen offset:0 sc1 lds\n"
    "buffer_load_dwordx4 v246, s[40:43], s54 offen offset:1024 sc1 lds\n"
    "buffer_load_dwordx4 v246, s[40:43], s54 offen offset:2048 sc1 lds\n"
    "buffer_load_dwordx4 v246, s[40:43], s54 offen offset:3072 sc1 lds\n"
    "s_mov_b32 m0, s56\n"
    "s_waitcnt vmcnt(0)\n"
    "ds_read2st64_b32 v[56:57], v252 offset0:152 offset1:153\n"
    "ds_read2st64_b32 v[58:59], v254 offset0:152 offset1:153\n"
    "ds_read2st64_b32 v[60:61], v252 offset0:156 offset1:157\n"
    "ds_read2st64_b32 v[62:63], v254 offset0:156 offset1:157\n"
    "s_waitcnt lgkmcnt(0)\n"
    "s_waitcnt vmcnt(0)\n"
    "v_max3_u32 v251, v56, v57, v58\n"
    "v_max3_u32 v247, v60, v61, v62\n"
    "v_max3_u32 v251, v251, v59, v63\n"
    "v_max_u32 v251, v251, v247\n"
    "v_cmp_eq_u32 vcc, -1, v251\n"
    "s_cbranch_vccz .Lk_gox7\n"
    "s_sub_u32 s19, s19, 1\n"
    "s_cmp_lg_u32 s19, 0\n"
    "s_cbranch_scc1 .Lk_rlx7\n"
    "s_branch .Lk_gox7\n"
    ".Lk_rex8:\n"
    "s_mov_b32 s19, 0x40000\n"
    ".Lk_rlx8:\n"
    "s_sleep 2\n"
    "s_mov_b32 s56, m0\n"
    "s_add_u32 s54, s8, 16384\n"
    "s_add_u32 m0, s10, 0\n"
    "s_nop 0\n"
    "buffer_load_dwordx4 v246, s[40:43], s54 offen offset:0 sc1 lds\n"
    "buffer_load_dwordx4 v246, s[40:43], s54 offen offset:1024 sc1 lds\n"
    "buffer_load_dwordx4 v246, s[40:43], s54 offen offset:2048 sc1 lds\n"
    "buffer_load_dwordx4 v246, s[40:43], s54 offen offset:3072 sc1 lds\n"
    "s_add_u32 s54, s8, 20480\n"
    "s_add_u32 m0, s10, 4096\n"
    "s_nop 0\n"
    "buffer_load_dwordx4 v246, s[40:43], s54 offen offset:0 sc1 lds\n"
    "buffer_load_dwordx4 v246, s[40:43], s54 offen offset:1024 sc1 lds\n"
    "s_mov_b32 m0, s56\n"
    "s_waitcnt vmcnt(0)\n"
    "ds_read2st64_b32 v[64:65], v252 offset0:0 offset1:1\n"
    "ds_read2st64_b32 v[66:67], v254 offset0:0 offset1:1\n"
    "ds_read2st64_b32 v[68:69], v252 offset0:4 offset1:5\n"
    "ds_read2st64_b32 v[70:71], v254 offset0:4 offset1:5\n"
    "s_waitcnt lgkmcnt(0)\n"
    "s_waitcnt vmcnt(0)\n"
    "v_max3_u32 v251, v64, v65, v66\n"
    "v_max3_u32 v247, v68, v69, v70\n"
    "v_max3_u32 v251, v251, v67, v71\n"
    "v_max_u32 v251, v251, v247\n"
    "v_cmp_eq_u32 vcc, -1, v251\n"
    "s_cbranch_vccz .Lk_gox8\n"
    "s_sub_u32 s19, s19, 1\n"
    "s_cmp_lg_u32 s19, 0\n"
    "s_cbranch_scc1 .Lk_rlx8\n"
    "s_branch .Lk_gox8\n"
    ".Lk_rex9:\n"
    "s_mov_b32 s19, 0x40000\n"
    ".Lk_rlx9:\n"
    "s_sleep 2\n"
    "s_mov_b32 s56, m0\n"
    "s_add_u32 s54, s8, 16384\n"
    "s_add_u32 m0, s10, 0\n"
    "s_nop 0\n"
    "buffer_load_dwordx4 v246, s[40:43], s54 offen offset:2048 sc1 lds\n"
    "buffer_load_dwordx4 v246, s[40:43], s54 offen offset:3072 sc1 lds\n"
    "s_add_u32 s54, s8, 20480\n"
    "s_add_u32 m0, s10, 4096\n"
    "s_nop 0\n"
    "buffer_load_dwordx4 v246, s[40:43], s54 offen offset:0 sc1 lds\n"
    "buffer_load_dwordx4 v246, s[40:43], s54 offen offset:1024 sc1 lds\n"
    "buffer_load_dwordx4 v246, s[40:43], s54 offen offset:2048 sc1 lds\n"
    "buffer_load_dwordx4 v246, s[40:43], s54 offen offset:3072 sc1 lds\n"
    "s_mov_b32 m0, s56\n"
    "s_waitcnt vmcnt(0)\n"
    "ds_read2st64_b32 v[72:73], v252 offset0:8 offset1:9\n"
    "ds_read2st64_b32 v[74:75], v254 offset0:8 offset1:9\n"
    "ds_read2st64_b32 v[76:77], v252 offset0:12 offset1:13\n"
    "ds_read2st64_b32 v[78:79], v254 offset0:12 offset1:13\n"
    "s_waitcnt lgkmcnt(0)\n"
    "s_waitcnt vmcnt(0)\n"
    "v_max3_u32 v251, v72, v73, v74\n"
    "v_max3_u32 v247, v76, v77, v78\n"
    "v_max3_u32 v251, v251, v75, v79\n"
    "v_max_u32 v251, v251, v247\n"
    "v_cmp_eq_u32 vcc, -1, v251\n"
    "s_cbranch_vccz .Lk_gox9\n"
    "s_sub_u32 s19, s19, 1\n"
    "s_cmp_lg_u32 s19, 0\n"
    "s_cbranch_scc1 .Lk_rlx9\n"
    "s_branch .Lk_gox9\n"
    ".Lk_rex10:\n"
    "s_mov_b32 s19, 0x40000\n"
    ".Lk_rlx10:\n"
    "s_sleep 2\n"
    "s_mov_b32 s56, m0\n"
    "s_add_u32 s54, s8, 20480\n"
    "s_add_u32 m0, s10, 4096\n"
    "s_nop 0\n"
    "buffer_load_dwordx4 v246, s[40:43], s54 offen offset:0 sc1 lds\n"
    "buffer_load_dwordx4 v246, s[40:43], s54 offen offset:1024 sc1 lds\n"
    "buffer_load_dwordx4 v246, s[40:43], s54 offen offset:2048 sc1 lds\n"
    "buffer_load_dwordx4 v246, s[40:43], s54 offen offset:3072 sc1 lds\n"
    "s_add_u32 s54, s8, 24576\n"
    "s_add_u32 m0, s10, 32768\n"
    "s_nop 0\n"
    "buffer_load_dwordx4 v246, s[40:43], s54 offen offset:0 sc1 lds\n"
    "buffer_load_dwordx4 v246, s[40:43], s54 offen offset:1024 sc1 lds\n"
    "s_mov_b32 m0, s56\n"
    "s_waitcnt vmcnt(0)\n"
    "ds_read2st64_b32 v[80:81], v252 offset0:16 offset1:17\n"
    "ds_read2st64_b32 v[82:83], v254 offset0:16 offset1:17\n"
    "ds_read2st64_b32 v[84:85], v252 offset0:20 offset1:21\n"
    "ds_read2st64_b32 v[86:87], v254 offset0:20 offset1:21\n"
    "s_waitcnt lgkmcnt(0)\n"
    "s_waitcnt vmcnt(0)\n"
    "v_max3_u32 v251, v80, v81, v82\n"
    "v_max3_u32 v247, v84, v85, v86\n"
    "v_max3_u32 v251, v251, v83, v87\n"
    "v_max_u32 v251, v251, v247\n"
    "v_cmp_eq_u32 vcc, -1, v251\n"
    "s_cbranch_vccz .Lk_gox10\n"
    "s_sub_u32 s19, s19, 1\n"
    "s_cmp_lg_u32 s19, 0\n"
    "s_cbranch_scc1 .Lk_rlx10\n"
    "s_branch .Lk_gox10\n"
    ".Lk_rex11:\n"
    "s_mov_b32 s19, 0x40000\n"
    ".Lk_rlx11:\n"
    "s_sleep 2\n"
    "s_mov_b32 s56, m0\n"
    "s_add_u32 s54, s8, 20480\n"
    "s_add_u32 m0, s10, 4096\n"
    "s_nop 0\n"
    "buffer_load_dwordx4 v246, s[40:43], s54 offen offset:2048 sc1 lds\n"
    "buffer_load_dwordx4 v246, s[40:43], s54 offen offset:3072 sc1 lds\n"
    "s_add_u32 s54, s8, 24576\n"
    "s_add_u32 m0, s10, 32768\n"
    "s_nop 0\n"
    "buffer_load_dwordx4 v246, s[40:43], s54 offen offset:0 sc1 lds\n"
    "buffer_load_dwordx4 v246, s[40:43], s54 offen offset:1024 sc1 lds\n"
    "buffer_load_dwordx4 v246, s[40:43], s54 offen offset:2048 sc1 lds\n"
    "buffer_load_dwordx4 v246, s[40:43], s54 offen offset:3072 sc1 lds\n"
    "s_mov_b32 m0, s56\n"
    "s_waitcnt vmcnt(0)\n"
    "ds_read2st64_b32 v[88:89], v252 offset0:24 offset1:25\n"
    "ds_read2st64_b32 v[90:91], v254 offset0:24 offset1:25\n"
    "ds_read2st64_b32 v[92:93], v252 offset0:28 offset1:29\n"
    "ds_read2st64_b32 v[94:95], v254 offset0:28 offset1:29\n"
    "s_waitcnt lgkmcnt(0)\n"
    "s_waitcnt vmcnt(0)\n"
    "v_max3_u32 v251, v88, v89, v90\n"
    "v_max3_u32 v247, v92, v93, v94\n"
    "v_max3_u32 v251, v251, v91, v95\n"
    "v_max_u32 v251, v251, v247\n"
    "v_cmp_eq_u32 vcc, -1, v251\n"
    "s_cbranch_vccz .Lk_gox11\n"
    "s_sub_u32 s19, s19, 1\n"
    "s_cmp_lg_u32 s19, 0\n"
    "s_cbranch_scc1 .Lk_rlx11\n"
    "s_branch .Lk_gox11\n"
    ".Lk_rex12:\n"
    "s_mov_b32 s19, 0x40000\n"
    ".Lk_rlx12:\n"
    "s_sleep 2\n"
    "s_mov_b32 s56, m0\n"
    "s_add_u32 s54, s8, 24576\n"
    "s_add_u32 m0, s10, 32768\n"
    "s_nop 0\n"
    "buffer_load_dwordx4 v246, s[40:43], s54 offen offset:0 sc1 lds\n"
    "buffer_load_dwordx4 v246, s[40:43], s54 offen offset:1024 sc1 lds\n"
    "buffer_load_dwordx4 v246, s[40:43], s54 offen offset:2048 sc1 lds\n"
    "buffer_load_dwordx4 v246, s[40:43], s54 offen offset:3072 sc1 lds\n"
    "s_add_u32 s54, s8, 28672\n"
    "s_add_u32 m0, s10, 36864\n"
    "s_nop 0\n"
    "buffer_load_dwordx4 v246, s[40:43], s54 offen offset:0 sc1 lds\n"
    "buffer_load_dwordx4 v246, s[40:43], s54 offen offset:1024 sc1 lds\n"
    "s_mov_b32 m0, s56\n"
    "s_waitcnt vmcnt(0)\n"
    "ds_read2st64_b32 v[96:97], v252 offset0:128 offset1:129\n"
    "ds_read2st64_b32 v[98:99], v254 offset0:128 offset1:129\n"
    "ds_read2st64_b32 v[100:101], v252 offset0:132 offset1:133\n"
    "ds_read2st64_b32 v[102:103], v254 offset0:132 offset1:133\n"
    "s_waitcnt lgkmcnt(0)\n"
    "s_waitcnt vmcnt(0)\n"
    "v_max3_u32 v251, v96, v97, v98\n"
    "v_max3_u32 v247, v100, v101, v102\n"
    "v_max3_u32 v251, v251, v99, v103\n"
    "v_max_u32 v251, v251, v247\n"
    "v_cmp_eq_u32 vcc, -1, v251\n"
    "s_cbranch_vccz .Lk_gox12\n"
    "s_sub_u32 s19, s19, 1\n"
    "s_cmp_lg_u32 s19, 0\n"
    "s_cbranch_scc1 .Lk_rlx12\n"
    "s_branch .Lk_gox12\n"
    ".Lk_rex13:\n"
    "s_mov_b32 s19, 0x40000\n"
    ".Lk_rlx13:\n"
    "s_sleep 2\n"
    "s_mov_b32 s56, m0\n"
    "s_add_u32 s54, s8, 24576\n"
    "s_add_u32 m0, s10, 32768\n"
    "s_nop 0\n"
    "buffer_load_dwordx4 v246, s[40:43], s54 offen offset:2048 sc1 lds\n"
    "buffer_load_dwordx4 v246, s[40:43], s54 offen offset:3072 sc1 lds\n"
    "s_add_u32 s54, s8, 28672\n"
    "s_add_u32 m0, s10, 36864\n"
    "s_nop 0\n"
    "buffer_load_dwordx4 v246, s[40:43], s54 offen offset:0 sc1 lds\n"
    "buffer_load_dwordx4 v246, s[40:43], s54 offen offset:1024 sc1 lds\n"
    "buffer_load_dwordx4 v246, s[40:43], s54 offen offset:2048 sc1 lds\n"
    "buffer_load_dwordx4 v246, s[40:43], s54 offen offset:3072 sc1 lds\n"
    "s_mov_b32 m0, s56\n"
    "s_waitcnt vmcnt(0)\n"
    "ds_read2st64_b32 v[104:105], v252 offset0:136 offset1:137\n"
    "ds_read2st64_b32 v[106:107], v254 offset0:136 offset1:137\n"
    "ds_read2st64_b32 v[108:109], v252 offset0:140 offset1:141\n"
    "ds_read2st64_b32 v[110:111], v254 offset0:140 offset1:141\n"
    "s_waitcnt lgkmcnt(0)\n"
    "s_waitcnt vmcnt(0)\n"
    "v_max3_u32 v251, v104, v105, v106\n"
    "v_max3_u32 v247, v108, v109, v110\n"
    "v_max3_u32 v251, v251, v107, v111\n"
    "v_max_u32 v251, v251, v247\n"
    "v_cmp_eq_u32 vcc, -1, v251\n"
    "s_cbranch_vccz .Lk_gox13\n"
    "s_sub_u32 s19, s19, 1\n"
    "s_cmp_lg_u32 s19, 0\n"
    "s_cbranch_scc1 .Lk_rlx13\n"
    "s_branch .Lk_gox13\n"
    ".Lk_rex14:\n"
    "s_mov_b32 s19, 0x40000\n"
    ".Lk_rlx14:\n"
    "s_sleep 2\n"
    "s_mov_b32 s56, m0\n"
    "s_add_u32 s54, s8, 28672\n"
    "s_add_u32 m0, s10, 36864\n"
    "s_nop 0\n"
    "buffer_load_dwordx4 v246, s[40:43], s54 offen offset:0 sc1 lds\n"
    "buffer_load_dwordx4 v246, s[40:43], s54 offen offset:1024 sc1 lds\n"
    "buffer_load_dwordx4 v246, s[40:43], s54 offen offset:2048 sc1 lds\n"
    "buffer_load_dwordx4 v246, s[40:43], s54 offen offset:3072 sc1 lds\n"
    "s_mov_b32 m0, s56\n"
    "s_waitcnt vmcnt(0)\n"
    "ds_read2st64_b32 v[112:113], v252 offset0:144 offset1:145\n"
    "ds_read2st64_b32 v[114:115], v254 offset0:144 offset1:145\n"
    "ds_read2st64_b32 v[116:117], v252 offset0:148 offset1:149\n"
    "ds_read2st64_b32 v[118:119], v254 offset0:148 offset1:149\n"
    "s_waitcnt lgkmcnt(0)\n"
    "s_waitcnt vmcnt(0)\n"
    "v_max3_u32 v251, v112, v113, v114\n"
    "v_max3_u32 v247, v116, v117, v118\n"
    "v_max3_u32 v251, v251, v115, v119\n"
    "v_max_u32 v251, v251, v247\n"
    "v_cmp_eq_u32 vcc, -1, v251\n"
    "s_cbranch_vccz .Lk_gox14\n"
    "s_sub_u32 s19, s19, 1\n"
    "s_cmp_lg_u32 s19, 0\n"
    "s_cbranch_scc1 .Lk_rlx14\n"
    "s_branch .Lk_gox14\n"
    ".Lk_rex15:\n"
    "s_mov_b32 s19, 0x40000\n"
    ".Lk_rlx15:\n"
    "s_sleep 2\n"
    "s_mov_b32 s56, m0\n"
    "s_add_u32 s54, s8, 28672\n"
    "s_add_u32 m0, s10, 36864\n"
    "s_nop 0\n"
    "buffer_load_dwordx4 v246, s[40:43], s54 offen offset:2048 sc1 lds\n"
    "buffer_load_dwordx4 v246, s[40:43], s54 offen offset:3072 sc1 lds\n"
    "s_mov_b32 m0, s56\n"
    "s_waitcnt vmcnt(0)\n"
    "ds_read2st64_b32 v[120:121], v252 offset0:152 offset1:153\n"
    "ds_read2st64_b32 v[122:123], v254 offset0:152 offset1:153\n"
    "ds_read2st64_b32 v[124:125], v252 offset0:156 offset1:157\n"
    "ds_read2st64_b32 v[126:127], v254 offset0:156 offset1:157\n"
    "s_waitcnt lgkmcnt(0)\n"
    "s_waitcnt vmcnt(0)\n"
    "v_max3_u32 v251, v120, v121, v122\n"
    "v_max3_u32 v247, v124, v125, v126\n"
    "v_max3_u32 v251, v251, v123, v127\n"
    "v_max_u32 v251, v251, v247\n"
    "v_cmp_eq_u32 vcc, -1, v251\n"
    "s_cbranch_vccz .Lk_gox15\n"
    "s_sub_u32 s19, s19, 1\n"
    "s_cmp_lg_u32 s19, 0\n"
    "s_cbranch_scc1 .Lk_rlx15\n"
    "s_branch .Lk_gox15\n"
    ".Lk_reA:\n"
    "s_mov_b32 s19, 0x40000\n"
    ".Lk_rlA:\n"
    "s_sleep 2\n"
    "buffer_load_dword v241, v250, s[40:43], 0 offen sc1\n"
    "s_waitcnt vmcnt(0)\n"
    "v_cmp_gt_u32 vcc, 3, v241\n"
    "s_cbranch_vccz .Lk_goA\n"
    "s_sub_u32 s19, s19, 1\n"
    "s_cmp_lg_u32 s19, 0\n"
    "s_cbranch_scc1 .Lk_rlA\n"
    "s_branch .Lk_goA\n"
    ".Lk_reh12:\n"
    "s_mov_b32 s19, 0x40000\n"
    ".Lk_rlh12:\n"
    "s_sleep 2\n"
    "buffer_load_dwordx4 v[96:99], v246, s[40:43], s13 offen offset:3072 sc1\n"
    "buffer_load_dwordx4 v[100:103], v246, s[40:43], s14 offen offset:3072 sc1\n"
    "s_waitcnt vmcnt(0)\n"
    "v_max3_u32 v251, v96, v97, v98\n"
    "v_max3_u32 v247, v100, v101, v102\n"
    "v_max3_u32 v251, v251, v99, v103\n"
    "v_max_u32 v251, v251, v247\n"
    "v_cmp_eq_u32 vcc, -1, v251\n"
    "s_cbranch_vccz .Lk_goh12\n"
    "s_sub_u32 s19, s19, 1\n"
    "s_cmp_lg_u32 s19, 0\n"
    "s_cbranch_scc1 .Lk_rlh12\n"
    "s_branch .Lk_goh12\n"
    ".Lk_reh13:\n"
    "s_mov_b32 s19, 0x40000\n"
    ".Lk_rlh13:\n"
    "s_sleep 2\n"
    "buffer_load_dwordx4 v[104:107], v243, s[40:43], s13 offen offset:3072 sc1\n"
    "buffer_load_dwordx4 v[108:111], v243, s[40:43], s14 offen offset:3072 sc1\n"
    "s_waitcnt vmcnt(0)\n"
    "v_max3_u32 v251, v104, v105, v106\n"
    "v_max3_u32 v247, v108, v109, v110\n"
    "v_max3_u32 v251, v251, v107, v111\n"
    "v_max_u32 v251, v251, v247\n"
    "v_cmp_eq_u32 vcc, -1, v251\n"
    "s_cbranch_vccz .Lk_goh13\n"
    "s_sub_u32 s19, s19, 1\n"
    "s_cmp_lg_u32 s19, 0\n"
    "s_cbranch_scc1 .Lk_rlh13\n"
    "s_branch .Lk_goh13\n"
    ".Lk_reh14:\n"
    "s_mov_b32 s19, 0x40000\n"
    ".Lk_rlh14:\n"
    "s_sleep 2\n"
    "buffer_load_dwordx4 v[112:115], v244, s[40:43], s13 offen offset:3072 sc1\n"
    "buffer_load_dwordx4 v[116:119], v244, s[40:43], s14 offen offset:3072 sc1\n"
    "s_waitcnt vmcnt(0)\n"
    "v_max3_u32 v251, v112, v113, v114\n"
    "v_max3_u32 v247, v116, v117, v118\n"
    "v_max3_u32 v251, v251, v115, v119\n"
    "v_max_u32 v251, v251, v247\n"
    "v_cmp_eq_u32 vcc, -1, v251\n"
    "s_cbranch_vccz .Lk_goh14\n"
    "s_sub_u32 s19, s19, 1\n"
    "s_cmp_lg_u32 s19, 0\n"
    "s_cbranch_scc1 .Lk_rlh14\n"
    "s_branch .Lk_goh14\n"
    ".Lk_reh15:\n"
    "s_mov_b32 s19, 0x40000\n"
    ".Lk_rlh15:\n"
    "s_sleep 2\n"
    "buffer_load_dwordx4 v[120:123], v245, s[40:43], s13 offen offset:3072 sc1\n"
    "buffer_load_dwordx4 v[124:127], v245, s[40:43], s14 offen offset:3072 sc1\n"
    "s_waitcnt vmcnt(0)\n"
    "v_max3_u32 v251, v120, v121, v122\n"
    "v_max3_u32 v247, v124, v125, v126\n"
    "v_max3_u32 v251, v251, v123, v127\n"
    "v_max_u32 v251, v251, v247\n"
    "v_cmp_eq_u32 vcc, -1, v251\n"
    "s_cbranch_vccz .Lk_goh15\n"
    "s_sub_u32 s19, s19, 1\n"
    "s_cmp_lg_u32 s19, 0\n"
    "s_cbranch_scc1 .Lk_rlh15\n"
    "s_branch .Lk_goh15\n"
    ".Lk_reh16:\n"
    "s_mov_b32 s19, 0x40000\n"
    ".Lk_rlh16:\n"
    "s_sleep 2\n"
    "buffer_load_dwordx4 v[0:3], v246, s[40:43], s15 offen offset:0 sc1\n"
    "buffer_load_dwordx4 v[4:7], v246, s[40:43], s16 offen offset:0 sc1\n"
    "s_waitcnt vmcnt(0)\n"
    "v_max3_u32 v251, v0, v1, v2\n"
    "v_max3_u32 v247, v4, v5, v6\n"
    "v_max3_u32 v251, v251, v3, v7\n"
    "v_max_u32 v251, v251, v247\n"
    "v_cmp_eq_u32 vcc, -1, v251\n"
    "s_cbranch_vccz .Lk_goh16\n"
    "s_sub_u32 s19, s19, 1\n"
    "s_cmp_lg_u32 s19, 0\n"
    "s_cbranch_scc1 .Lk_rlh16\n"
    "s_branch .Lk_goh16\n"
    ".Lk_reh17:\n"
    "s_mov_b32 s19, 0x40000\n"
    ".Lk_rlh17:\n"
    "s_sleep 2\n"
    "buffer_load_dwordx4 v[8:11], v243, s[40:43], s15 offen offset:0 sc1\n"
    "buffer_load_dwordx4 v[12:15], v243, s[40:43], s16 offen offset:0 sc1\n"
    "s_waitcnt vmcnt(0)\n"
    "v_max3_u32 v251, v8, v9, v10\n"
    "v_max3_u32 v247, v12, v13, v14\n"
    "v_max3_u32 v251, v251, v11, v15\n"
    "v_max_u32 v251, v251, v247\n"
    "v_cmp_eq_u32 vcc, -1, v251\n"
    "s_cbranch_vccz .Lk_goh17\n"
    "s_sub_u32 s19, s19, 1\n"
    "s_cmp_lg_u32 s19, 0\n"
    "s_cbranch_scc1 .Lk_rlh17\n"
    "s_branch .Lk_goh17\n"
    ".Lk_reh18:\n"
    "s_mov_b32 s19, 0x40000\n"
    ".Lk_rlh18:\n"
    "s_sleep 2\n"
    "buffer_load_dwordx4 v[16:19], v244, s[40:43], s15 offen offset:0 sc1\n"
    "buffer_load_dwordx4 v[20:23], v244, s[40:43], s16 offen offset:0 sc1\n"
    "s_waitcnt vmcnt(0)\n"
    "v_max3_u32 v251, v16, v17, v18\n"
    "v_max3_u32 v247, v20, v21, v22\n"
    "v_max3_u32 v251, v251, v19, v23\n"
    "v_max_u32 v251, v251, v247\n"
    "v_cmp_eq_u32 vcc, -1, v251\n"
    "s_cbranch_vccz .Lk_goh18\n"
    "s_sub_u32 s19, s19, 1\n"
    "s_cmp_lg_u32 s19, 0\n"
    "s_cbranch_scc1 .Lk_rlh18\n"
    "s_branch .Lk_goh18\n"
    ".Lk_reh19:\n"
    "s_mov_b32 s19, 0x40000\n"
    ".Lk_rlh19:\n"
    "s_sleep 2\n"
    "buffer_load_dwordx4 v[24:27], v245, s[40:43], s15 offen offset:0 sc1\n"
    "buffer_load_dwordx4 v[28:31], v245, s[40:43], s16 offen offset:0 sc1\n"
    "s_waitcnt vmcnt(0)\n"
    "v_max3_u32 v251, v24, v25, v26\n"
    "v_max3_u32 v247, v28, v29, v30\n"
    "v_max3_u32 v251, v251, v27, v31\n"
    "v_max_u32 v251, v251, v247\n"
    "v_cmp_eq_u32 vcc, -1, v251\n"
    "s_cbranch_vccz .Lk_goh19\n"
    "s_sub_u32 s19, s19, 1\n"
    "s_cmp_lg_u32 s19, 0\n"
    "s_cbranch_scc1 .Lk_rlh19\n"
    "s_branch .Lk_goh19\n"
    ".Lk_reh20:\n"
    "s_mov_b32 s19, 0x40000\n"
    ".Lk_rlh20:\n"
    "s_sleep 2\n"
    "buffer_load_dwordx4 v[32:35], v246, s[40:43], s15 offen offset:1024 sc1\n"
    "buffer_load_dwordx4 v[36:39], v246, s[40:43], s16 offen offset:1024 sc1\n"
    "s_waitcnt vmcnt(0)\n"
    "v_max3_u32 v251, v32, v33, v34\n"
    "v_max3_u32 v247, v36, v37, v38\n"
    "v_max3_u32 v251, v251, v35, v39\n"
    "v_max_u32 v251, v251, v247\n"
    "v_cmp_eq_u32 vcc, -1, v251\n"
    "s_cbranch_vccz .Lk_goh20\n"
    "s_sub_u32 s19, s19, 1\n"
    "s_cmp_lg_u32 s19, 0\n"
    "s_cbranch_scc1 .Lk_rlh20\n"
    "s_branch .Lk_goh20\n"
    ".Lk_reh21:\n"
    "s_mov_b32 s19, 0x40000\n"
    ".Lk_rlh21:\n"
    "s_sleep 2\n"
    "buffer_load_dwordx4 v[40:43], v243, s[40:43], s15 offen offset:1024 sc1\n"
    "buffer_load_dwordx4 v[44:47], v243, s[40:43], s16 offen offset:1024 sc1\n"
    "s_waitcnt vmcnt(0)\n"
    "v_max3_u32 v251, v40, v41, v42\n"
    "v_max3_u32 v247, v44, v45, v46\n"
    "v_max3_u32 v251, v251, v43, v47\n"
    "v_max_u32 v251, v251, v247\n"
    "v_cmp_eq_u32 vcc, -1, v251\n"
    "s_cbranch_vccz .Lk_goh21\n"
    "s_sub_u32 s19, s19, 1\n"
    "s_cmp_lg_u32 s19, 0\n"
    "s_cbranch_scc1 .Lk_rlh21\n"
    "s_branch .Lk_goh21\n"
    ".Lk_reh22:\n"
    "s_mov_b32 s19, 0x40000\n"
    ".Lk_rlh22:\n"
    "s_sleep 2\n"
    "buffer_load_dwordx4 v[48:51], v244, s[40:43], s15 offen offset:1024 sc1\n"
    "buffer_load_dwordx4 v[52:55], v244, s[40:43], s16 offen offset:1024 sc1\n"
    "s_waitcnt vmcnt(0)\n"
    "v_max3_u32 v251, v48, v49, v50\n"
    "v_max3_u32 v247, v52, v53, v54\n"
    "v_max3_u32 v251, v251, v51, v55\n"
    "v_max_u32 v251, v251, v247\n"
    "v_cmp_eq_u32 vcc, -1, v251\n"
    "s_cbranch_vccz .Lk_goh22\n"
    "s_sub_u32 s19, s19, 1\n"
    "s_cmp_lg_u32 s19, 0\n"
    "s_cbranch_scc1 .Lk_rlh22\n"
    "s_branch .Lk_goh22\n"
    ".Lk_reh23:\n"
    "s_mov_b32 s19, 0x40000\n"
    ".Lk_rlh23:\n"
    "s_sleep 2\n"
    "buffer_load_dwordx4 v[56:59], v245, s[40:43], s15 offen offset:1024 sc1\n"
    "buffer_load_dwordx4 v[60:63], v245, s[40:43], s16 offen offset:1024 sc1\n"
    "s_waitcnt vmcnt(0)\n"
    "v_max3_u32 v251, v56, v57, v58\n"
    "v_max3_u32 v247, v60, v61, v62\n"
    "v_max3_u32 v251, v251, v59, v63\n"
    "v_max_u32 v251, v251, v247\n"
    "v_cmp_eq_u32 vcc, -1, v251\n"
    "s_cbranch_vccz .Lk_goh23\n"
    "s_sub_u32 s19, s19, 1\n"
    "s_cmp_lg_u32 s19, 0\n"
    "s_cbranch_scc1 .Lk_rlh23\n"
    "s_branch .Lk_goh23\n"
    ".Lk_reh24:\n"
    "s_mov_b32 s19, 0x40000\n"
    ".Lk_rlh24:\n"
    "s_sleep 2\n"
    "buffer_load_dwordx4 v[64:67], v246, s[40:43], s15 offen offset:2048 sc1\n"
    "buffer_load_dwordx4 v[68:71], v246, s[40:43], s16 offen offset:2048 sc1\n"
    "s_waitcnt vmcnt(0)\n"
    "v_max3_u32 v251, v64, v65, v66\n"
    "v_max3_u32 v247, v68, v69, v70\n"
    "v_max3_u32 v251, v251, v67, v71\n"
    "v_max_u32 v251, v251, v247\n"
    "v_cmp_eq_u32 vcc, -1, v251\n"
    "s_cbranch_vccz .Lk_goh24\n"
    "s_sub_u32 s19, s19, 1\n"
    "s_cmp_lg_u32 s19, 0\n"
    "s_cbranch_scc1 .Lk_rlh24\n"
    "s_branch .Lk_goh24\n"
    ".Lk_reh25:\n"
    "s_mov_b32 s19, 0x40000\n"
    ".Lk_rlh25:\n"
    "s_sleep 2\n"
    "buffer_load_dwordx4 v[72:75], v243, s[40:43], s15 offen offset:2048 sc1\n"
    "buffer_load_dwordx4 v[76:79], v243, s[40:43], s16 offen offset:2048 sc1\n"
    "s_waitcnt vmcnt(0)\n"
    "v_max3_u32 v251, v72, v73, v74\n"
    "v_max3_u32 v247, v76, v77, v78\n"
    "v_max3_u32 v251, v251, v75, v79\n"
    "v_max_u32 v251, v251, v247\n"
    "v_cmp_eq_u32 vcc, -1, v251\n"
    "s_cbranch_vccz .Lk_goh25\n"
    "s_sub_u32 s19, s19, 1\n"
    "s_cmp_lg_u32 s19, 0\n"
    "s_cbranch_scc1 .Lk_rlh25\n"
    "s_branch .Lk_goh25\n"
    ".Lk_reh26:\n"
    "s_mov_b32 s19, 0x40000\n"
    ".Lk_rlh26:\n"
    "s_sleep 2\n"
    "buffer_load_dwordx4 v[80:83], v244, s[40:43], s15 offen offset:2048 sc1\n"
    "buffer_load_dwordx4 v[84:87], v244, s[40:43], s16 offen offset:2048 sc1\n"
    "s_waitcnt vmcnt(0)\n"
    "v_max3_u32 v251, v80, v81, v82\n"
    "v_max3_u32 v247, v84, v85, v86\n"
    "v_max3_u32 v251, v251, v83, v87\n"
    "v_max_u32 v251, v251, v247\n"
    "v_cmp_eq_u32 vcc, -1, v251\n"
    "s_cbranch_vccz .Lk_goh26\n"
    "s_sub_u32 s19, s19, 1\n"
    "s_cmp_lg_u32 s19, 0\n"
    "s_cbranch_scc1 .Lk_rlh26\n"
    "s_branch .Lk_goh26\n"
    ".Lk_reh27:\n"
    "s_mov_b32 s19, 0x40000\n"
    ".Lk_rlh27:\n"
    "s_sleep 2\n"
    "buffer_load_dwordx4 v[88:91], v245, s[40:43], s15 offen offset:2048 sc1\n"
    "buffer_load_dwordx4 v[92:95], v245, s[40:43], s16 offen offset:2048 sc1\n"
    "s_waitcnt vmcnt(0)\n"
    "v_max3_u32 v251, v88, v89, v90\n"
    "v_max3_u32 v247, v92, v93, v94\n"
    "v_max3_u32 v251, v251, v91, v95\n"
    "v_max_u32 v251, v251, v247\n"
    "v_cmp_eq_u32 vcc, -1, v251\n"
    "s_cbranch_vccz .Lk_goh27\n"
    "s_sub_u32 s19, s19, 1\n"
    "s_cmp_lg_u32 s19, 0\n"
    "s_cbranch_scc1 .Lk_rlh27\n"
    "s_branch .Lk_goh27\n"
    "k3_code_end:\n"
        : [tid] "+v"(tid)
        : [karg] "s"(__builtin_amdgcn_kernarg_segment_ptr()), [bid] "s"(blockIdx.x), [cZX] "i"(WS_ZX), [cX2] "i"(WS_X2),
          [cMOD] "i"(WS_MOD), [cEP] "i"(WS_EP), [cST] "i"(WS_ST_), [cWGU] "i"(WS_WGU), [cUV] "i"(WS_UV), [cWD] "i"(WS_WD), [cHH] "i"(WS_HH),
          [cFL] "i"(WS_FL), [cFW] "i"(WS_FW), [cGB] "i"(WS_GB), [cEND] "i"(WS_END)
        : "memory", "vcc", "scc", K2_CLOBBERS);
}

extern "C" void kernel_launch(void* const* d_in, const int* in_sizes, int n_in, void* d_out, int out_size,
                              void* d_ws, size_t ws_size, hipStream_t stream) {
    (void)hipFuncSetAttribute((const void*)k_fused, hipFuncAttributeMaxDynamicSharedMemorySize, K2_LDS);
    const u16* const* in = (const u16* const*)d_in;
    k_fused<<<256, 256, K2_LDS, stream>>>(in[0], in[13], in[14], in[12], in[10], in[11], (char*)d_ws, (u16*)d_out, in[1], in[2],
                                          in[3], in[4], in[5], in[6], in[7], in[8]);
}
```
